```python
import torch

def _softmax(x, axis=-1):
    return torch.softmax(x, dim=axis)
def _gelu(x):
    return torch.nn.functional.gelu(x, approximate="tanh")
def _silu(x):
    return torch.nn.functional.silu(x)
def _sigmoid(x):
    return torch.sigmoid(x)
def _layernorm(x, axis=-1, eps=1e-5):
    m = x.mean(dim=axis, keepdim=True)
    v = x.var(dim=axis, keepdim=True, unbiased=False)
    return (x - m) / torch.sqrt(v + eps)
def _scatter_add(tgt, idx, src, axis=0):
    return tgt.clone().index_add_(axis, idx, src)
def _segment_sum(src, seg, n):
    out = torch.zeros((n,) + src.shape[1:], dtype=src.dtype)
    return out.index_add_(0, seg, src)
def _pad(x, pw):
    flat = tuple(v for pair in reversed(pw) for v in pair)
    return torch.nn.functional.pad(x, flat)

kernel_name = 'standalone__gqa_softmax__sampling'

def setup_inputs():

    _w = (lambda a: (lambda t: t.to(torch.bfloat16) if t.is_floating_point() else t)(torch.as_tensor(a)))
    B, Hq, Hkv, Sq, Sk, D = 16, 32, 8, 16, 2048, 128
    return {
        "q": _w((lambda *s: torch.randn(*s, dtype=torch.bfloat16) * 0.8)(B, Hq, Sq, D)),
        "k": _w((lambda *s: torch.randn(*s, dtype=torch.bfloat16) * 0.8)(B, Hkv, Sk, D)),
        "v": _w((lambda *s: torch.randn(*s, dtype=torch.bfloat16) * 0.05)(B, Hkv, Sk, D)),
    }

def reference(q, k, v):
    B, Hq, Sq, D = q.shape
    Hkv = k.shape[1]
    rep = Hq // Hkv
    k = (lambda x, n, axis: torch.repeat_interleave(x, n, dim=axis))(k, rep, axis=1)
    v = (lambda x, n, axis: torch.repeat_interleave(x, n, dim=axis))(v, rep, axis=1)
    scale = D ** -0.5
    scores = torch.einsum("bhqd,bhkd->bhqk", q, k) * scale
    p = _softmax(scores, axis=-1)
    return torch.einsum("bhqk,bhkd->bhqd", p, v)
```

```cpp
#include <hip/hip_runtime.h>
#include <hip/hip_bf16.h>
#include <stdint.h>

#define TK 64
#define DEPTH 5
#define NLOAD 4
#define NT_F 17
#define KV_AUX 2

namespace {

constexpr int B_ = 16, HQ = 32, HKV = 8, SQ = 16, SK = 2048, D_ = 128;
constexpr int NPAIR = B_ * HKV;
constexpr int TILE_BYTES = TK * 256;
constexpr int NSUB = TK / 16, NKK = TK / 32;
constexpr int LDS_BYTES = 2 * DEPTH * TILE_BYTES;
constexpr int NTHREADS = 64 * (4 + NLOAD);
constexpr int LPS = NLOAD / 2;
constexpr int PPL = TK / 4 / LPS;
constexpr int KEYS_F = NT_F * 64, KEYS_H = SK - KEYS_F;
constexpr float C2 = 0.08838834764831845f * 1.4426950408889634f;
constexpr size_t CTRL_STRIDE = 128;
constexpr size_t CTRL_BYTES = (size_t)NPAIR * 4 * CTRL_STRIDE;
constexpr size_t PART_BYTES = 9 * 1024;
static_assert(LDS_BYTES <= 163840, "LDS");
static_assert((DEPTH - 1) * PPL <= 63, "vmcnt");

typedef __attribute__((ext_vector_type(8))) __bf16 bf16x8;
typedef __attribute__((ext_vector_type(4))) __bf16 bf16x4;
typedef __attribute__((ext_vector_type(2))) __bf16 bf16x2;
typedef __attribute__((ext_vector_type(4))) float f32x4;
typedef __attribute__((ext_vector_type(2))) float f32x2;
typedef __attribute__((ext_vector_type(4))) unsigned u32x4;
typedef __attribute__((ext_vector_type(2))) unsigned u32x2;
#define LAS __attribute__((address_space(3)))
#define GAS __attribute__((address_space(1)))

#define BARRIER() asm volatile("s_waitcnt lgkmcnt(0)\n\ts_barrier" ::: "memory")

template <int N> __device__ __forceinline__ void wait_vm() { asm volatile("s_waitcnt vmcnt(%0)" ::"n"(N) : "memory"); }
template <int A> __device__ __forceinline__ void wait_tiles(int allowed) {
  if constexpr (A == 0) { wait_vm<0>(); }
  else { if (allowed >= A) wait_vm<A * PPL>(); else wait_tiles<A - 1>(allowed); }
}

__device__ __forceinline__ float xmax4(float x) {
  auto s = __builtin_amdgcn_permlane16_swap(__float_as_uint(x), __float_as_uint(x), false, false);
  x = fmaxf(__uint_as_float(s[0]), __uint_as_float(s[1]));
  auto t = __builtin_amdgcn_permlane32_swap(__float_as_uint(x), __float_as_uint(x), false, false);
  return fmaxf(__uint_as_float(t[0]), __uint_as_float(t[1]));
}
__device__ __forceinline__ float xsum4(float x) {
  auto s = __builtin_amdgcn_permlane16_swap(__float_as_uint(x), __float_as_uint(x), false, false);
  x = __uint_as_float(s[0]) + __uint_as_float(s[1]);
  auto t = __builtin_amdgcn_permlane32_swap(__float_as_uint(x), __float_as_uint(x), false, false);
  return __uint_as_float(t[0]) + __uint_as_float(t[1]);
}
__device__ __forceinline__ bf16x2 pk(float a, float b) { f32x2 v = {a, b}; return __builtin_convertvector(v, bf16x2); }

struct State {
  f32x4 o[8];
  bf16x8 p[NKK];
  float m, l;
};

__device__ __forceinline__ void qk(f32x4 (&s)[NSUB], const bf16x8 (&q)[4], const unsigned (&ka)[4], unsigned base, LAS char* lds) {
#pragma unroll
  for (int sub = 0; sub < NSUB; ++sub) s[sub] = f32x4{0.f, 0.f, 0.f, 0.f};
#pragma unroll
  for (int st = 0; st < 4; ++st) {
#pragma unroll
    for (int sub = 0; sub < NSUB; ++sub) {
      const bf16x8 kf = *(const LAS bf16x8*)(lds + (base + ka[st]) + sub * 4096);
      s[sub] = __builtin_amdgcn_mfma_f32_16x16x32_bf16(kf, q[st], s[sub], 0, 0, 0);
    }
  }
}

__device__ __forceinline__ void pv(State& st, unsigned va, LAS char* lds) {
#pragma unroll
  for (int db = 0; db < 8; ++db) {
    LAS char* a = lds + (va ^ (db << 5));
#pragma unroll
    for (int kk = 0; kk < NKK; ++kk) {
      const bf16x4 lo = __builtin_amdgcn_ds_read_tr16_b64_v4bf16((LAS bf16x4*)(a + kk * 8192));
      const bf16x4 hi = __builtin_amdgcn_ds_read_tr16_b64_v4bf16((LAS bf16x4*)(a + kk * 8192 + 4096));
      const bf16x8 vf = __builtin_shufflevector(lo, hi, 0, 1, 2, 3, 4, 5, 6, 7);
      st.o[db] = __builtin_amdgcn_mfma_f32_16x16x32_bf16(vf, st.p[kk], st.o[db], 0, 0, 0);
    }
  }
}

__device__ __forceinline__ void softmax(State& st, const f32x4 (&s)[NSUB]) {
  float t = s[0][0];
#pragma unroll
  for (int sub = 0; sub < NSUB; ++sub)
#pragma unroll
    for (int r = 0; r < 4; ++r) t = fmaxf(t, s[sub][r]);
  t = xmax4(t);
  const float mn = fmaxf(st.m, t * C2);
  const float alpha = __builtin_amdgcn_exp2f(st.m - mn);
  st.m = mn;
  float sum = 0.f;
  float e[NSUB][4];
#pragma unroll
  for (int sub = 0; sub < NSUB; ++sub)
#pragma unroll
    for (int r = 0; r < 4; ++r) { e[sub][r] = __builtin_amdgcn_exp2f(fmaf(s[sub][r], C2, -mn)); sum += e[sub][r]; }
  st.l = fmaf(st.l, alpha, sum);
#pragma unroll
  for (int kk = 0; kk < NKK; ++kk) {
    const bf16x2 a = pk(e[2 * kk][0], e[2 * kk][1]), b = pk(e[2 * kk][2], e[2 * kk][3]);
    const bf16x2 c = pk(e[2 * kk + 1][0], e[2 * kk + 1][1]), d = pk(e[2 * kk + 1][2], e[2 * kk + 1][3]);
    st.p[kk] = bf16x8{a[0], a[1], b[0], b[1], c[0], c[1], d[0], d[1]};
  }
#pragma unroll
  for (int db = 0; db < 8; ++db) st.o[db] *= alpha;
}

__device__ __forceinline__ void finish(const State& st, float l, const u32x4 (&oth)[9], __hip_bfloat16* orow) {
  const float m2 = __uint_as_float(oth[8][0]), l2 = __uint_as_float(oth[8][1]);
  const float m = fmaxf(st.m, m2);
  const float a1 = __builtin_amdgcn_exp2f(st.m - m), a2 = __builtin_amdgcn_exp2f(m2 - m);
  const float inv = 1.0f / fmaf(a1, l, a2 * l2);
  const float w1 = a1 * inv, w2 = a2 * inv;
#pragma unroll
  for (int db = 0; db < 8; ++db) {
    float r[4];
#pragma unroll
    for (int i = 0; i < 4; ++i) r[i] = fmaf(st.o[db][i], w1, __uint_as_float(oth[db][i]) * w2);
    const bf16x2 x = pk(r[0], r[1]), y = pk(r[2], r[3]);
    *(bf16x4*)(orow + db * 16) = bf16x4{x[0], x[1], y[0], y[1]};
  }
}

__global__ void __launch_bounds__(NTHREADS, (NTHREADS + 255) / 256)
gqa_attn(const __hip_bfloat16* __restrict__ Q, const __hip_bfloat16* __restrict__ K, const __hip_bfloat16* __restrict__ V,
         __hip_bfloat16* __restrict__ O, unsigned char* __restrict__ ws) {
  __shared__ __attribute__((aligned(1024))) char smem[LDS_BYTES];
  LAS char* lds = (LAS char*)smem;
  const int lane = threadIdx.x & 63;
  const int wave = __builtin_amdgcn_readfirstlane(threadIdx.x >> 6);
  const int pair = blockIdx.x & (NPAIR - 1), role = blockIdx.x >> 7;
  const int key0 = role ? KEYS_F : 0;
  const int n = (role ? KEYS_H : KEYS_F) / TK;
  const int g = lane >> 4, c = lane & 15;

  if (wave >= 4) {
    const int lw = wave - 4, isv = lw / LPS, part = lw % LPS;
    const __hip_bfloat16* src = (isv ? V : K) + ((size_t)pair * SK + key0) * D_;
    const __amdgpu_buffer_rsrc_t rs = __builtin_amdgcn_make_buffer_rsrc((void*)src, 0, n * TILE_BYTES, 0x00020000);
    int vo[4];
#pragma unroll
    for (int k = 0; k < 4; ++k) {
      const int row = 4 * k + g;
      const int sw = isv ? ((row & 7) << 1) : row;
      vo[k] = g * 256 + ((c ^ sw) << 4);
    }
    LAS char* ring = lds + isv * (DEPTH * TILE_BYTES) + part * (PPL * 1024);
    auto issue = [&](int tile, int slot) {
      const int so = tile * TILE_BYTES + part * (PPL * 1024);
      LAS char* dst = ring + slot * TILE_BYTES;
#pragma unroll
      for (int p4 = 0; p4 < PPL / 4; ++p4) {
        __builtin_amdgcn_raw_ptr_buffer_load_lds(rs, dst + p4 * 4096, 16, vo[0], so + p4 * 4096, 0, KV_AUX);
        __builtin_amdgcn_raw_ptr_buffer_load_lds(rs, dst + p4 * 4096, 16, vo[1], so + p4 * 4096, 1024, KV_AUX);
        __builtin_amdgcn_raw_ptr_buffer_load_lds(rs, dst + p4 * 4096, 16, vo[2], so + p4 * 4096, 2048, KV_AUX);
        __builtin_amdgcn_raw_ptr_buffer_load_lds(rs, dst + p4 * 4096, 16, vo[3], so + p4 * 4096, 3072, KV_AUX);
      }
    };
    for (int t = 0; t < DEPTH; ++t) issue(t, t);
    if (isv) BARRIER();
    int slot = 0;
    for (int i = 0; i < n; ++i) {
      wait_tiles<DEPTH - 2>(n - 1 - i);
      BARRIER();
      if (i >= 1) {
        if (i + DEPTH - 1 < n) issue(i + DEPTH - 1, slot);
        slot = slot + 1 == DEPTH ? 0 : slot + 1;
      }
    }
    if (!isv) BARRIER();
    return;
  }

  const size_t qrow = ((size_t)pair * 4 + wave) * SQ + c;
  bf16x8 q[4];
#pragma unroll
  for (int st = 0; st < 4; ++st) q[st] = *(const bf16x8*)(Q + qrow * D_ + st * 32 + g * 8);
  GAS unsigned long long* ctrl = (GAS unsigned long long*)(ws + ((size_t)pair * 4 + wave) * CTRL_STRIDE);
  const unsigned long long tag = __hip_atomic_load(ctrl, __ATOMIC_RELAXED, __HIP_MEMORY_SCOPE_AGENT) + 0x9E3779B97F4A7C15ull;
  const __amdgpu_buffer_rsrc_t prs = __builtin_amdgcn_make_buffer_rsrc(
      (void*)(ws + CTRL_BYTES + ((size_t)pair * 4 + wave) * 2 * PART_BYTES), 0, 2 * PART_BYTES, 0x00020000);

  unsigned ka[4];
#pragma unroll
  for (int st = 0; st < 4; ++st) ka[st] = c * 256 + (((4 * st + g) ^ c) << 4);
  const int vr = 4 * g + (c >> 2);
  const unsigned va0 = DEPTH * TILE_BYTES + vr * 256 + ((vr & 7) << 5) + (c & 3) * 8;

  State st;
#pragma unroll
  for (int db = 0; db < 8; ++db) st.o[db] = f32x4{0.f, 0.f, 0.f, 0.f};
  st.m = -3.0e38f; st.l = 0.f;
  f32x4 s[NSUB];

  BARRIER();
  qk(s, q, ka, 0, lds);
  softmax(st, s);
  unsigned kb = TILE_BYTES, vb = 0;
  for (int j = 1; j < n - 1; ++j) {
    BARRIER();
    qk(s, q, ka, kb, lds);
    pv(st, va0 + vb, lds);
    softmax(st, s);
    vb = kb; kb = kb + TILE_BYTES == DEPTH * TILE_BYTES ? 0 : kb + TILE_BYTES;
  }
  BARRIER();
  const unsigned long long peek = __hip_atomic_load(ctrl + 1, __ATOMIC_RELAXED, __HIP_MEMORY_SCOPE_AGENT);
  qk(s, q, ka, kb, lds);
  pv(st, va0 + vb, lds);
  softmax(st, s);
  vb = kb;
  const float l = xsum4(st.l);
  __hip_bfloat16* orow = O + qrow * D_ + g * 4;
  const int oth_off = (role ^ 1) * (int)PART_BYTES + lane * 16, own_off = role * (int)PART_BYTES + lane * 16;
  u32x4 oth[9];
  if (__builtin_amdgcn_readfirstlane(peek == tag)) {
    __builtin_amdgcn_fence(__ATOMIC_ACQUIRE, "agent");
#pragma unroll
    for (int i = 0; i < 9; ++i) oth[i] = __builtin_amdgcn_raw_buffer_load_b128(prs, oth_off + i * 1024, 0, 16);
    BARRIER();
    pv(st, va0 + vb, lds);
  } else {
    BARRIER();
    pv(st, va0 + vb, lds);
#pragma unroll
    for (int i = 0; i < 8; ++i) __builtin_amdgcn_raw_buffer_store_b128(__builtin_bit_cast(u32x4, st.o[i]), prs, own_off + i * 1024, 0, 16);
    __builtin_amdgcn_raw_buffer_store_b128(u32x4{__float_as_uint(st.m), __float_as_uint(l), 0u, 0u}, prs, own_off + 8 * 1024, 0, 16);
    asm volatile("s_waitcnt vmcnt(0)" ::: "memory");
    unsigned long long old = 0;
    if (lane == 0) old = __hip_atomic_exchange(ctrl + 1, tag, __ATOMIC_RELAXED, __HIP_MEMORY_SCOPE_AGENT);
    if (!__builtin_amdgcn_readfirstlane(old == tag)) return;
    __builtin_amdgcn_fence(__ATOMIC_ACQUIRE, "agent");
#pragma unroll
    for (int i = 0; i < 9; ++i) oth[i] = __builtin_amdgcn_raw_buffer_load_b128(prs, oth_off + i * 1024, 0, 16);
  }
  finish(st, l, oth, orow);
  if (lane == 0) {
    __hip_atomic_store(ctrl, tag, __ATOMIC_RELAXED, __HIP_MEMORY_SCOPE_AGENT);
    __hip_atomic_store(ctrl + 1, ~tag, __ATOMIC_RELAXED, __HIP_MEMORY_SCOPE_AGENT);
  }
}

}

extern "C" void kernel_launch(void* const* d_in, const int* in_sizes, int n_in, void* d_out, int out_size, void* d_ws, size_t ws_size,
                              hipStream_t stream) {
  gqa_attn<<<2 * NPAIR, NTHREADS, 0, stream>>>((const __hip_bfloat16*)d_in[0], (const __hip_bfloat16*)d_in[1], (const __hip_bfloat16*)d_in[2],
                                                (__hip_bfloat16*)d_out, (unsigned char*)d_ws);
}
```

```cpp
#include <hip/hip_runtime.h>
#include <hip/hip_bf16.h>
#include <stdint.h>

#define WIN 4
#define N1 12
#define KV_AUX 2
#define PEEK 5
#define PEEKW 3
#define KV_BYTES (SK * 256)
#define NOCOMP 0
#define NLOAD 4

namespace {

constexpr int B_ = 16, HKV = 8, SQ = 16, SK = 2048, D_ = 128;
constexpr int TK = 64, NTILE = SK / TK;
constexpr int NPAIR = B_ * HKV;
constexpr int TILE_BYTES = TK * 256;
constexpr int NS = WIN + 4;
constexpr int LDS_BYTES = NS * TILE_BYTES;
constexpr int NCONS = 4;
constexpr int NTHREADS = 64 * (NCONS + NLOAD);
constexpr float C2 = 0.08838834764831845f * 1.4426950408889634f;
constexpr float SLACK = 7.0f;
constexpr unsigned GE2 = 0x40004000u;
constexpr size_t CTRL_STRIDE = 128;
constexpr size_t CTRL_BYTES = (size_t)NPAIR * NCONS * CTRL_STRIDE;
constexpr int PART_BYTES = 9 * 1024;
static_assert(LDS_BYTES <= 163840, "LDS");

typedef __attribute__((ext_vector_type(8))) __bf16 bf16x8;
typedef __attribute__((ext_vector_type(4))) __bf16 bf16x4;
typedef __attribute__((ext_vector_type(2))) __bf16 bf16x2;
typedef __attribute__((ext_vector_type(4))) float f32x4;
typedef __attribute__((ext_vector_type(2))) float f32x2;
typedef __attribute__((ext_vector_type(4))) unsigned u32x4;
typedef __attribute__((ext_vector_type(2))) unsigned u32x2;
#define LAS __attribute__((address_space(3)))
#define GAS __attribute__((address_space(1)))

#define BARRIER() asm volatile("s_waitcnt lgkmcnt(0)\n\ts_barrier" ::: "memory")
#define DIAG_OUT(code)
#define SEG(i)
#define SB() __builtin_amdgcn_sched_barrier(0)

template <int N> __device__ __forceinline__ void wait_vm() { asm volatile("s_waitcnt vmcnt(%0)" ::"n"(N) : "memory"); }

__device__ __forceinline__ float xmax4(float x) {
  auto s = __builtin_amdgcn_permlane16_swap(__float_as_uint(x), __float_as_uint(x), false, false);
  x = fmaxf(__uint_as_float(s[0]), __uint_as_float(s[1]));
  auto t = __builtin_amdgcn_permlane32_swap(__float_as_uint(x), __float_as_uint(x), false, false);
  return fmaxf(__uint_as_float(t[0]), __uint_as_float(t[1]));
}
__device__ __forceinline__ unsigned pk(float a, float b) { f32x2 v = {a, b}; return __builtin_bit_cast(unsigned, __builtin_convertvector(v, bf16x2)); }
__device__ __forceinline__ f32x4 mfma(u32x4 a, u32x4 b, f32x4 c) {
  return __builtin_amdgcn_mfma_f32_16x16x32_bf16(__builtin_bit_cast(bf16x8, a), __builtin_bit_cast(bf16x8, b), c, 0, 0, 0);
}
__device__ __forceinline__ u32x2 trd(LAS char* p) { return __builtin_bit_cast(u32x2, __builtin_amdgcn_ds_read_tr16_b64_v4bf16((LAS bf16x4*)p)); }
__device__ __forceinline__ unsigned ring_add(unsigned x, unsigned d) { x += d; return x >= (unsigned)LDS_BYTES ? x - LDS_BYTES : x; }

#define LOAD_PARTNER() \
  asm volatile("buffer_load_dwordx4 %0, %9, %10, 0 offen sc1\n\t" \
               "buffer_load_dwordx4 %1, %9, %10, 0 offen offset:1024 sc1\n\t" \
               "buffer_load_dwordx4 %2, %9, %10, 0 offen offset:2048 sc1\n\t" \
               "buffer_load_dwordx4 %3, %9, %10, 0 offen offset:3072 sc1\n\t" \
               "buffer_load_dwordx4 %4, %9, %10, %11 offen sc1\n\t" \
               "buffer_load_dwordx4 %5, %9, %10, %11 offen offset:1024 sc1\n\t" \
               "buffer_load_dwordx4 %6, %9, %10, %11 offen offset:2048 sc1\n\t" \
               "buffer_load_dwordx4 %7, %9, %10, %11 offen offset:3072 sc1\n\t" \
               "buffer_load_dwordx4 %8, %9, %10, %12 offen sc1" \
               : "+v"(oth[0]), "+v"(oth[1]), "+v"(oth[2]), "+v"(oth[3]), "+v"(oth[4]), "+v"(oth[5]), "+v"(oth[6]), "+v"(oth[7]), "+v"(oth[8]) \
               : "v"(oth_off), "s"(prs), "s"(4096), "s"(8192) : "memory")

__global__ void __launch_bounds__(NTHREADS, 2)
gqa_attn(const __hip_bfloat16* __restrict__ Q, const __hip_bfloat16* __restrict__ K, const __hip_bfloat16* __restrict__ V,
         __hip_bfloat16* __restrict__ O, unsigned char* __restrict__ ws) {
  __shared__ __attribute__((aligned(1024))) char smem[LDS_BYTES];
  LAS char* lds = (LAS char*)smem;
  const int lane = threadIdx.x & 63;
  const int wave = __builtin_amdgcn_readfirstlane(threadIdx.x >> 6);
  const int pair = blockIdx.x & (NPAIR - 1), role = blockIdx.x >> 7;
  const int n = role ? N1 : NTILE - N1;
  const int g = lane >> 4, c = lane & 15;

  if (__builtin_expect(wave < NLOAD, 1)) {
    const int vo0 = g * 256 + ((c ^ (g << 1)) << 4), vo1 = vo0 ^ 128;
    const char* kp = (const char*)K + (size_t)pair * (SK * 256) + wave * 4096;
    const char* vp = (const char*)V + (size_t)pair * (SK * 256) + wave * 4096;
    const char* qp = (const char*)Q + (size_t)pair * TILE_BYTES + wave * 4096;
    unsigned slot = wave * 4096;
    const int nu = 2 * n + 1;
#pragma unroll 1
    for (int e = 0; e < nu; ++e) {
      const int v = e < 3 ? e : e + 1;
      const bool isk = !(v & 1) && v < 2 * n;
      int t = isk ? v >> 1 : (v >= 2 * n ? v - n - 2 : (v - 5) >> 1);
      t = t < N1 ? 2 * t + role : N1 + t;
      const char* base = isk ? kp : vp;
      if (e == 1) { base = qp; t = 0; }
      const __amdgpu_buffer_rsrc_t rs = __builtin_amdgcn_make_buffer_rsrc((void*)base, 0, KV_BYTES, 0x00020000);
      const int so = t * TILE_BYTES;
      if (e >= WIN) wait_vm<4 * (WIN - 1)>();
      __builtin_amdgcn_raw_ptr_buffer_load_lds(rs, lds + slot, 16, vo0, so, 0, KV_AUX);
      __builtin_amdgcn_raw_ptr_buffer_load_lds(rs, lds + slot, 16, vo1, so, 1024, KV_AUX);
      __builtin_amdgcn_raw_ptr_buffer_load_lds(rs, lds + slot, 16, vo0, so, 2048, KV_AUX);
      __builtin_amdgcn_raw_ptr_buffer_load_lds(rs, lds + slot, 16, vo1, so, 3072, KV_AUX);
      slot = ring_add(slot, e == 2 ? 2 * TILE_BYTES : TILE_BYTES);
      const int u = e - WIN;
      if (u == 1 || (u > 0 && !(u & 1))) BARRIER();
    }
    if constexpr (WIN >= 5) { wait_vm<16>(); BARRIER(); }
    wait_vm<8>(); BARRIER();
    wait_vm<4>(); BARRIER();
    wait_vm<0>(); BARRIER();
    DIAG_OUT(0);
    return;
  }

  const int head = wave - NLOAD;
  GAS unsigned long long* ctrl = (GAS unsigned long long*)(ws + ((size_t)pair * NCONS + head) * CTRL_STRIDE);
  u32x2 epoch;
  asm volatile("global_load_dwordx2 %0, %1, %2 sc1" : "=v"(epoch) : "v"(0), "s"(ctrl) : "memory");
  const int oth_off = (role ^ 1) * PART_BYTES + lane * 16, own_off = role * PART_BYTES + lane * 16;
  const __amdgpu_buffer_rsrc_t prs = __builtin_amdgcn_make_buffer_rsrc(
      (void*)(ws + CTRL_BYTES + ((size_t)pair * NCONS + head) * 2 * PART_BYTES), 0, 2 * PART_BYTES, 0x00020000);
  const unsigned ka0 = c * 256 + ((g ^ ((c & 7) << 1)) << 4);
  const int vr = 4 * g + (c >> 2);
  const unsigned va0 = vr * 256 + ((vr & 7) << 5) + (c & 3) * 8;
  u32x4 ones = {0x3F803F80u, 0x3F803F80u, 0x3F803F80u, 0x3F803F80u};
  asm volatile("" : "+v"(ones));

  BARRIER();
  u32x4 q[4], kf[4][4];
#pragma unroll
  for (int st = 0; st < 4; ++st) {
    const u32x4 r = *(const LAS u32x4*)(lds + (TILE_BYTES + head * 4096) + (ka0 ^ (st << 6)));
#pragma unroll
    for (int i = 0; i < 4; ++i) q[st][i] = pk(__uint_as_float(r[i] << 16) * C2, __uint_as_float(r[i] & 0xFFFF0000u) * C2);
  }
#pragma unroll
  for (int sub = 0; sub < 4; ++sub)
#pragma unroll
    for (int st = 0; st < 4; ++st) kf[sub][st] = *(const LAS u32x4*)(lds + (ka0 ^ (st << 6)) + sub * 4096);
  f32x4 o[8], lsum = {0.f, 0.f, 0.f, 0.f};
#pragma unroll
  for (int db = 0; db < 8; ++db) o[db] = f32x4{0.f, 0.f, 0.f, 0.f};
  float m = -3.0e38f;
  f32x4 c0 = {0.f, 0.f, 0.f, 0.f};
  unsigned force = GE2;
  unsigned kb = 2 * TILE_BYTES, vb = 5 * TILE_BYTES;
  u32x4 oth[9];
  u32x2 peek;
  int got = 0;
  BARRIER();
#pragma unroll 1
  for (int j = 0; j < n; ++j) {
    SEG(0);
    f32x4 s[4];
    u32x4 p[2];
    const unsigned ka = ka0 + kb;
    kb = ring_add(kb, 2 * TILE_BYTES);
#define QK1(sub, st) do { s[sub] = mfma(kf[sub][st], q[st], (st) ? s[sub] : c0); kf[sub][st] = *(const LAS u32x4*)(lds + (ka ^ ((st) << 6)) + (sub) * 4096); } while (0)
#define EX2(sub, h) p[(sub) >> 1][2 * ((sub) & 1) + (h)] = pk(__builtin_amdgcn_exp2f(s[sub][2 * (h)]), __builtin_amdgcn_exp2f(s[sub][2 * (h) + 1]))
    QK1(0, 0); SB(); QK1(0, 1); SB(); QK1(0, 2); SB(); QK1(0, 3); SB(); QK1(1, 0); SB(); QK1(1, 1); SB();
    EX2(0, 0); QK1(1, 2); SB(); QK1(1, 3); SB(); EX2(0, 1); QK1(2, 0); SB(); QK1(2, 1); SB();
    EX2(1, 0); QK1(2, 2); SB(); QK1(2, 3); SB(); EX2(1, 1); QK1(3, 0); SB(); QK1(3, 1); SB();
    EX2(2, 0); QK1(3, 2); SB(); QK1(3, 3); SB(); EX2(2, 1); SB();
    SEG(1);
    unsigned any = (p[0][0] | p[0][1] | p[0][2]) | (p[0][3] | p[1][0] | p[1][1]) | force;
    asm volatile("" : "+v"(any));
    EX2(3, 0); EX2(3, 1);
    any |= p[1][2] | p[1][3];
    if (__builtin_expect(__builtin_amdgcn_ballot_w64((any & GE2) != 0) != 0, 0)) {
      float t = s[0][0];
#pragma unroll
      for (int sub = 0; sub < 4; ++sub)
#pragma unroll
        for (int r = 0; r < 4; ++r) t = fmaxf(t, s[sub][r]);
      const float mn = fmaxf(m, xmax4(t) - c0[0] + SLACK);
      const float alpha = __builtin_amdgcn_exp2f(m - mn), sub_ = c0[0] + mn;
      m = mn;
      const bool fold = fabsf(mn) <= 512.f;
      const float nm = fold ? -mn : 0.f;
      c0 = f32x4{nm, nm, nm, nm};
      force = fold ? 0u : GE2;
#pragma unroll
      for (int db = 0; db < 8; ++db) o[db] *= alpha;
      lsum *= alpha;
#pragma unroll
      for (int sub = 0; sub < 4; ++sub) {
        p[sub >> 1][2 * (sub & 1)] = pk(__builtin_amdgcn_exp2f(s[sub][0] - sub_), __builtin_amdgcn_exp2f(s[sub][1] - sub_));
        p[sub >> 1][2 * (sub & 1) + 1] = pk(__builtin_amdgcn_exp2f(s[sub][2] - sub_), __builtin_amdgcn_exp2f(s[sub][3] - sub_));
      }
    }
    if (__builtin_expect(j >= n - PEEK, 0)) {
      if (j == n - PEEK) asm volatile("global_load_dwordx2 %0, %1, %2 offset:8 sc1" : "=v"(peek) : "v"(0), "s"(ctrl) : "memory");
      if (j == n - PEEKW) {
        asm volatile("s_waitcnt vmcnt(0)" : "+v"(peek), "+v"(epoch) :: "memory");
        if (__builtin_amdgcn_readfirstlane(peek[0] == epoch[0] + 0x7F4A7C15u && peek[1] == epoch[1] + 0x9E3779B9u)) {
          LOAD_PARTNER();
          got = 1;
        }
      }
    }
    lsum = mfma(ones, p[0], lsum);
    lsum = mfma(ones, p[1], lsum);
    const unsigned va = va0 + vb;
    vb = ring_add(vb, j >= n - 3 ? TILE_BYTES : 2 * TILE_BYTES);
    unsigned vx[8];
#pragma unroll
    for (int db = 0; db < 8; ++db) vx[db] = va ^ (db << 5);
    SEG(2);
    BARRIER();
    SEG(3);
    u32x2 vf[16][2];
#define VRD(i) do { vf[i][0] = trd(lds + vx[(i) >> 1] + ((i) & 1) * 8192); vf[i][1] = trd(lds + vx[(i) >> 1] + ((i) & 1) * 8192 + 4096); } while (0)
#define PV1(i) o[(i) >> 1] = mfma(u32x4{vf[i][0][0], vf[i][0][1], vf[i][1][0], vf[i][1][1]}, p[(i) & 1], o[(i) >> 1])
    VRD(0); VRD(1); VRD(2); VRD(3); VRD(4); VRD(5); SB();
    PV1(0); VRD(6); SB(); PV1(1); VRD(7); SB(); PV1(2); VRD(8); SB(); PV1(3); VRD(9); SB();
    PV1(4); VRD(10); SB(); PV1(5); VRD(11); SB(); PV1(6); VRD(12); SB(); PV1(7); VRD(13); SB();
    PV1(8); VRD(14); SB(); PV1(9); VRD(15); SB(); PV1(10); PV1(11); PV1(12); PV1(13); PV1(14); PV1(15); SB();
    SEG(4);
  }
  float l = lsum[0], w1;
  asm volatile("s_waitcnt vmcnt(0)" : "+v"(epoch) :: "memory");
  const unsigned long long tag = ((unsigned long long)(epoch[1] + 0x9E3779B9u) << 32) | (epoch[0] + 0x7F4A7C15u);
  if (!got) {
#pragma unroll
    for (int i = 0; i < 8; ++i) __builtin_amdgcn_raw_buffer_store_b128(__builtin_bit_cast(u32x4, o[i]), prs, own_off + i * 1024, 0, 16);
    __builtin_amdgcn_raw_buffer_store_b128(u32x4{__float_as_uint(m), __float_as_uint(l), 0u, 0u}, prs, own_off + 8192, 0, 16);
    asm volatile("s_waitcnt vmcnt(0)" ::: "memory");
    unsigned long long old = 0;
    if (lane == 0) old = __hip_atomic_exchange(ctrl + 1, tag, __ATOMIC_RELAXED, __HIP_MEMORY_SCOPE_AGENT);
    if (!__builtin_amdgcn_readfirstlane(old == tag)) { DIAG_OUT(1); return; }
    LOAD_PARTNER();
  }
  asm volatile("s_waitcnt vmcnt(0)" : "+v"(oth[0]), "+v"(oth[1]), "+v"(oth[2]), "+v"(oth[3]), "+v"(oth[4]), "+v"(oth[5]), "+v"(oth[6]), "+v"(oth[7]), "+v"(oth[8]) :: "memory");
  {
    const float m2 = __uint_as_float(oth[8][0]), l2 = __uint_as_float(oth[8][1]);
    const float mm = fmaxf(m, m2);
    const float b1 = __builtin_amdgcn_exp2f(m - mm), b2 = __builtin_amdgcn_exp2f(m2 - mm);
    const float inv = __builtin_amdgcn_rcpf(fmaf(b1, l, b2 * l2));
    w1 = b1 * inv; const float w2 = b2 * inv;
#pragma unroll
    for (int i = 0; i < 8; ++i) o[i] = o[i] * w1 + __builtin_bit_cast(f32x4, oth[i]) * w2;
  }
  char* orow = (char*)O + (((size_t)pair * 4 + head) * SQ + c) * 256 + g * 8;
#pragma unroll
  for (int i = 0; i < 8; ++i) *(u32x2*)(orow + i * 32) = u32x2{pk(o[i][0], o[i][1]), pk(o[i][2], o[i][3])};
  if (lane == 0) {
    __hip_atomic_store(ctrl, tag, __ATOMIC_RELAXED, __HIP_MEMORY_SCOPE_AGENT);
    __hip_atomic_store(ctrl + 1, ~tag, __ATOMIC_RELAXED, __HIP_MEMORY_SCOPE_AGENT);
  }
  DIAG_OUT(got ? 2 : 3);
}

}

extern "C" void kernel_launch(void* const* d_in, const int* in_sizes, int n_in, void* d_out, int out_size, void* d_ws, size_t ws_size,
                              hipStream_t stream) {
  gqa_attn<<<(N1 ? 2 : 1) * NPAIR, NTHREADS, 0, stream>>>((const __hip_bfloat16*)d_in[0], (const __hip_bfloat16*)d_in[1], (const __hip_bfloat16*)d_in[2],
                                                (__hip_bfloat16*)d_out, (unsigned char*)d_ws);
}
```

```cpp
#define TOUCH 1
#define OUT_AUX 18
#define N1 15
#include <hip/hip_runtime.h>
#include <hip/hip_bf16.h>
#include <stdint.h>

#define KV_POL "nt"
#define PK1 3
#define PK2 1
#define DRY 0
#define PFMT 1
#define KV_BYTES (SK * 256)
#define MAP 0
#define WS_OFF 0
#define PACE 0
#define PACE0 400
#define STOUCH 0
#define NLOAD 4
#define EARLY_BUMP 1
#define BUMP_AUX 16
#define NOOUT 0

namespace {

constexpr int B_ = 16, HKV = 8, SQ = 16, SK = 2048;
constexpr int TK = 64, NTILE = SK / TK;
constexpr int NPAIR = B_ * HKV;
constexpr int TILE_BYTES = TK * 256;
constexpr int NS = 8;
constexpr int RING_BYTES = NS * TILE_BYTES;
constexpr int LDS_BYTES = RING_BYTES + 4 * 16 * 272;
constexpr int NCONS = 4;
constexpr int NTHREADS = 64 * (NCONS + NLOAD);
constexpr float C2 = 0.08838834764831845f * 1.4426950408889634f;
constexpr float SLACK = 7.0f;
constexpr unsigned GE2 = 0x40004000u;
constexpr int NPC = PFMT ? 4 : 8;
constexpr int FLAG_OFF = NPC * 1024, EPOCH_OFF = FLAG_OFF + 256;
constexpr int HEAD_BYTES = EPOCH_OFF + 256;
constexpr unsigned TAG_STEP = 0x9E3779B1u;

typedef __attribute__((ext_vector_type(8))) __bf16 bf16x8;
typedef __attribute__((ext_vector_type(4))) __bf16 bf16x4;
typedef __attribute__((ext_vector_type(2))) __bf16 bf16x2;
typedef __attribute__((ext_vector_type(4))) float f32x4;
typedef __attribute__((ext_vector_type(2))) float f32x2;
typedef __attribute__((ext_vector_type(4))) unsigned u32x4;
typedef __attribute__((ext_vector_type(2))) unsigned u32x2;
#define LAS __attribute__((address_space(3)))

#define BARRIER() asm volatile("s_waitcnt lgkmcnt(0)\n\ts_barrier" ::: "memory")
#define BARRIER_IF(x) BARRIER()
#define LBARRIER() asm volatile("s_barrier" ::: "memory")
#define DIAG_OUT(code)
#define RSTAMP(i)
#define SB() __builtin_amdgcn_sched_barrier(0)

template <int N> __device__ __forceinline__ void wait_vm() { asm volatile("s_waitcnt vmcnt(%0)" ::"n"(N) : "memory"); }

__device__ __forceinline__ float xmax4(float x) {
  auto s = __builtin_amdgcn_permlane16_swap(__float_as_uint(x), __float_as_uint(x), false, false);
  x = fmaxf(__uint_as_float(s[0]), __uint_as_float(s[1]));
  auto t = __builtin_amdgcn_permlane32_swap(__float_as_uint(x), __float_as_uint(x), false, false);
  return fmaxf(__uint_as_float(t[0]), __uint_as_float(t[1]));
}
__device__ __forceinline__ unsigned pk(float a, float b) { f32x2 v = {a, b}; return __builtin_bit_cast(unsigned, __builtin_convertvector(v, bf16x2)); }
__device__ __forceinline__ f32x4 mfma(u32x4 a, u32x4 b, f32x4 c) {
  return __builtin_amdgcn_mfma_f32_16x16x32_bf16(__builtin_bit_cast(bf16x8, a), __builtin_bit_cast(bf16x8, b), c, 0, 0, 0);
}
__device__ __forceinline__ u32x2 trd(LAS char* p) { return __builtin_bit_cast(u32x2, __builtin_amdgcn_ds_read_tr16_b64_v4bf16((LAS bf16x4*)p)); }

#define A_FLAG "v220", "v221", "v222", "v223"
#define A_PC0 "v224", "v225", "v226", "v227", "v228", "v229", "v230", "v231", "v232", "v233", "v234", "v235", "v236", "v237", "v238", "v239"
#define A_PC1 "v240", "v241", "v242", "v243", "v244", "v245", "v246", "v247", "v248", "v249", "v250", "v251", "v252", "v253", "v254", "v255"
#define PC_LOAD0() \
  asm volatile("buffer_load_dwordx4 v[224:227], %0, %1, 0 offen sc1\n\t" \
               "buffer_load_dwordx4 v[228:231], %0, %1, 0 offen offset:1024 sc1\n\t" \
               "buffer_load_dwordx4 v[232:235], %0, %1, 0 offen offset:2048 sc1\n\t" \
               "buffer_load_dwordx4 v[236:239], %0, %1, 0 offen offset:3072 sc1" :: "v"(lane * 16), "s"(rp) : "memory", A_PC0)
#define PC_LOAD1() \
  asm volatile("buffer_load_dwordx4 v[240:243], %0, %1, %2 offen sc1\n\t" \
               "buffer_load_dwordx4 v[244:247], %0, %1, %2 offen offset:1024 sc1\n\t" \
               "buffer_load_dwordx4 v[248:251], %0, %1, %2 offen offset:2048 sc1\n\t" \
               "buffer_load_dwordx4 v[252:255], %0, %1, %2 offen offset:3072 sc1" :: "v"(lane * 16), "s"(rp), "s"(4096) : "memory", A_PC1)
#define PC_LOAD_ALL() PC_LOAD0()
#define FLAG_LOAD() asm volatile("buffer_load_dwordx4 v[220:223], %0, %1, %2 offen sc1" :: "v"(c * 16), "s"(rp), "s"(FLAG_OFF) : "memory", A_FLAG)
template <int I> __device__ __forceinline__ unsigned areg() { unsigned x; asm volatile("v_mov_b32 %0, v%c1" : "=v"(x) : "n"(220 + I)); return x; }
template <int I> __device__ __forceinline__ float areg_lo() { float x; asm volatile("v_lshlrev_b32 %0, 16, v%c1" : "=v"(x) : "n"(220 + I)); return x; }
template <int I> __device__ __forceinline__ float areg_hi() { float x; asm volatile("v_and_b32 %0, 0xffff0000, v%c1" : "=v"(x) : "n"(220 + I)); return x; }
#define BUMP() __builtin_amdgcn_raw_buffer_store_b32(epoch + TAG_STEP, rp, lane == 0 ? EPOCH_OFF : HEAD_BYTES, 0, BUMP_AUX)
#define FLAG_SET() (__builtin_amdgcn_ballot_w64(((areg<1>() ^ (epoch + TAG_STEP)) | (areg<3>() ^ (epoch + TAG_STEP))) != 0) == 0)

__global__ void __launch_bounds__(NTHREADS, 2) __attribute__((amdgpu_num_vgpr(220)))
gqa_attn(const __hip_bfloat16* __restrict__ Q, const __hip_bfloat16* __restrict__ K, const __hip_bfloat16* __restrict__ V,
         __hip_bfloat16* __restrict__ O, unsigned char* __restrict__ ws) {
  __shared__ __attribute__((aligned(1024))) char smem[LDS_BYTES];
  LAS char* lds = (LAS char*)smem;
  const int lane = threadIdx.x & 63;
  const int wave = __builtin_amdgcn_readfirstlane(threadIdx.x >> 6);
  const int pair = blockIdx.x & (NPAIR - 1), role = blockIdx.x >> 7;
  const int n = role ? N1 : NTILE - N1;
  const int g = lane >> 4, c = lane & 15;

  const int vo0 = g * 256 + ((c ^ (g << 1)) << 4), vo1 = vo0 ^ 128;
  const __amdgpu_buffer_rsrc_t rk = __builtin_amdgcn_make_buffer_rsrc((char*)K + (size_t)pair * (SK * 256), 0, KV_BYTES, 0x00020000);
  const __amdgpu_buffer_rsrc_t rv = __builtin_amdgcn_make_buffer_rsrc((char*)V + (size_t)pair * (SK * 256), 0, KV_BYTES, 0x00020000);
  const __amdgpu_buffer_rsrc_t rq = __builtin_amdgcn_make_buffer_rsrc((char*)Q + (size_t)pair * TILE_BYTES, 0, TILE_BYTES, 0x00020000);
  const int wo = (wave & 3) * 4096;
  auto unit = [&](const __amdgpu_buffer_rsrc_t r, int so, unsigned slot) {
    const unsigned dst = (unsigned)(uintptr_t)lds + (slot & (NS - 1)) * TILE_BYTES + wo;
    asm volatile("s_mov_b32 m0, %0\n\ts_nop 0\n\t"
                 "buffer_load_dwordx4 %1, %3, %4 offen " KV_POL " lds\n\t"
                 "buffer_load_dwordx4 %2, %3, %4 offen offset:1024 " KV_POL " lds\n\t"
                 "buffer_load_dwordx4 %1, %3, %4 offen offset:2048 " KV_POL " lds\n\t"
                 "buffer_load_dwordx4 %2, %3, %4 offen offset:3072 " KV_POL " lds"
                 :: "s"(dst), "v"(vo0), "v"(vo1), "s"(r), "s"(so) : "memory");
  };
  auto off = [&](int t) { return (t < N1 ? 2 * t + role : N1 + t) * TILE_BYTES + wo; };
  if (__builtin_expect(wave >= NLOAD, 0)) {
  const int head = wave - NLOAD;
  const unsigned char* wp = ws + WS_OFF + ((size_t)pair * 4 + head) * HEAD_BYTES;
  const unsigned ka0 = c * 256 + ((g ^ ((c & 7) << 1)) << 4);
  const int vr = 4 * g + (c >> 2);
  const unsigned va0 = vr * 256 + ((vr & 7) << 5) + (c & 3) * 8;
  u32x4 ones = {0x3F803F80u, 0x3F803F80u, 0x3F803F80u, 0x3F803F80u};
  asm volatile("" : "+v"(ones));

  int live = DRY ? 0 : 1;
#pragma unroll 1
  for (;; live = 1) {
    const __amdgpu_buffer_rsrc_t rp = __builtin_amdgcn_make_buffer_rsrc((void*)wp, 0, live ? HEAD_BYTES : 0, 0x00020000);
    const __amdgpu_buffer_rsrc_t ro = __builtin_amdgcn_make_buffer_rsrc((char*)O + ((size_t)pair * 4 + head) * (SQ * 256), 0, (live && !NOOUT) ? SQ * 256 : 0, 0x00020000);
    f32x4 o[8], lsum = {0.f, 0.f, 0.f, 0.f};
#pragma unroll
    for (int db = 0; db < 8; ++db) { o[db] = f32x4{0.f, 0.f, 0.f, 0.f}; asm volatile("" : "+v"(o[db])); }
    if (live) { RSTAMP(50); BARRIER(); } else { RSTAMP(49); }
    u32x4 q[4], kf[4][4];
#pragma unroll
    for (int st = 0; st < 4; ++st) q[st] = *(const LAS u32x4*)(lds + (TILE_BYTES + head * 4096) + (ka0 ^ (st << 6)));
#pragma unroll
    for (int sub = 0; sub < 4; ++sub)
#pragma unroll
      for (int st = 0; st < 4; ++st) kf[sub][st] = *(const LAS u32x4*)(lds + (ka0 ^ (st << 6)) + sub * 4096);
    SB();
#pragma unroll
    for (int st = 0; st < 4; ++st)
#pragma unroll
      for (int i = 0; i < 4; ++i) q[st][i] = pk(__uint_as_float(q[st][i] << 16) * C2, __uint_as_float(q[st][i] & 0xFFFF0000u) * C2);
    float m = -3.0e38f;
    f32x4 c0 = {0.f, 0.f, 0.f, 0.f};
    unsigned force = GE2;
    unsigned kb = 2 * TILE_BYTES;
    unsigned kx[4];
#pragma unroll
    for (int st = 0; st < 4; ++st) kx[st] = (ka0 + kb) ^ (st << 6);
#pragma unroll
    for (int sub = 0; sub < 4; ++sub)
#pragma unroll
      for (int st = 0; st < 4; ++st) asm volatile("" : "+v"(kf[sub][st]));
    unsigned epoch;
    int got = 0;
    if (live) BARRIER();
#pragma unroll 1
    for (int left = live ? n : 1; left != 0; --left) {
      f32x4 s[4];
      float e[4][4];
      u32x4 p[2];
      const unsigned va = va0 + (left == 1 ? kb : (kb + 3 * TILE_BYTES) & (RING_BYTES - 1));
      unsigned vx[8];
#define QK1(sub, st) do { s[sub] = mfma(kf[sub][st], q[st], (st) ? s[sub] : c0); kf[sub][st] = *(const LAS u32x4*)(lds + kx[st] + (sub) * 4096); } while (0)
#define EX(sub, r) e[sub][r] = __builtin_amdgcn_exp2f(s[sub][r])
#define CV(sub, h) p[(sub) >> 1][2 * ((sub) & 1) + (h)] = pk(e[sub][2 * (h)], e[sub][2 * (h) + 1])
#define VX(i) do { vx[i] = va ^ ((i) << 5); if (i) asm volatile("" : "+v"(vx[i])); } while (0)
      QK1(0, 0); VX(0); SB(); QK1(1, 0); VX(1); SB(); QK1(0, 1); VX(2); SB(); QK1(1, 1); VX(3); SB();
      QK1(0, 2); VX(4); SB(); QK1(1, 2); VX(5); SB(); QK1(0, 3); VX(6); SB(); QK1(1, 3); VX(7); SB();
      QK1(2, 0); SB(); QK1(3, 0); SB();
      QK1(2, 1); EX(0, 0); EX(0, 1); SB(); QK1(3, 1); EX(0, 2); EX(0, 3); SB();
      QK1(2, 2); EX(1, 0); EX(1, 1); SB(); QK1(3, 2); EX(1, 2); EX(1, 3); SB();
      QK1(2, 3); CV(0, 0); CV(0, 1); SB(); QK1(3, 3); CV(1, 0); CV(1, 1); SB();
      unsigned any = (p[0][0] | p[0][1] | force) | p[0][2] | p[0][3];
      asm volatile("" : "+v"(any) :: "memory");
      kb = (kb + 2 * TILE_BYTES) & (RING_BYTES - 1);
      SB();
      EX(2, 0); EX(2, 1); EX(2, 2); EX(2, 3); EX(3, 0); EX(3, 1); EX(3, 2); EX(3, 3); SB();
      CV(2, 0); CV(2, 1); CV(3, 0); CV(3, 1);
      any = (any | p[1][0] | p[1][1]) | p[1][2] | p[1][3];
      if (__builtin_expect(__builtin_amdgcn_ballot_w64((any & GE2) != 0) != 0, 0)) {
        float t = s[0][0];
#pragma unroll
        for (int sub = 0; sub < 4; ++sub)
#pragma unroll
          for (int r = 0; r < 4; ++r) t = fmaxf(t, s[sub][r]);
        const float mn = fmaxf(m, xmax4(t) - c0[0] + SLACK);
        const float alpha = __builtin_amdgcn_exp2f(m - mn), sub_ = c0[0] + mn;
        m = mn;
        const bool fold = fabsf(mn) <= 512.f;
        const float nm = fold ? -mn : 0.f;
        c0 = f32x4{nm, nm, nm, nm};
        force = fold ? 0u : GE2;
#pragma unroll
        for (int db = 0; db < 8; ++db) o[db] *= alpha;
        lsum *= alpha;
#pragma unroll
        for (int sub = 0; sub < 4; ++sub) {
          p[sub >> 1][2 * (sub & 1)] = pk(__builtin_amdgcn_exp2f(s[sub][0] - sub_), __builtin_amdgcn_exp2f(s[sub][1] - sub_));
          p[sub >> 1][2 * (sub & 1) + 1] = pk(__builtin_amdgcn_exp2f(s[sub][2] - sub_), __builtin_amdgcn_exp2f(s[sub][3] - sub_));
        }
      }
      if (__builtin_expect(left <= PK1 + 1, 0)) {
        if (left == PK1 + 1) {
          asm volatile("s_load_dword %0, %1, %2" : "=&s"(epoch) : "s"(wp), "n"(EPOCH_OFF) : "memory");
          if (!role) FLAG_LOAD();
        }
        if (left == PK2 + 1 && !role) {
          wait_vm<0>();
          asm volatile("" : "+s"(epoch));
          if (FLAG_SET()) { PC_LOAD_ALL(); got = 1; if (EARLY_BUMP) BUMP(); } else FLAG_LOAD();
        }
      }
      BARRIER_IF(live);
      u32x2 vf[16][2];
#define VRD(i) do { vf[i][0] = trd(lds + vx[(i) & 7] + ((i) >> 3) * 8192); vf[i][1] = trd(lds + vx[(i) & 7] + ((i) >> 3) * 8192 + 4096); } while (0)
#define PV1(i) o[(i) & 7] = mfma(u32x4{vf[i][0][0], vf[i][0][1], vf[i][1][0], vf[i][1][1]}, p[(i) >> 3], o[(i) & 7])
      VRD(0); VRD(1); VRD(2); VRD(3); VRD(4); VRD(5); SB();
      lsum = mfma(ones, p[0], lsum);
      lsum = mfma(ones, p[1], lsum);
#pragma unroll
      for (int st = 0; st < 4; ++st) { kx[st] = (ka0 + kb) ^ (st << 6); if (st) asm volatile("" : "+v"(kx[st])); }
      SB();
      VRD(6); SB(); PV1(0); PV1(1); VRD(7); VRD(8); SB(); PV1(2); PV1(3); VRD(9); VRD(10); SB();
      PV1(4); PV1(5); VRD(11); VRD(12); SB(); PV1(6); PV1(7); VRD(13); VRD(14); SB();
      PV1(8); PV1(9); VRD(15); SB(); PV1(10); PV1(11); PV1(12); PV1(13); PV1(14); PV1(15); SB();
    }
    asm volatile("" : "+s"(epoch));
    RSTAMP(52);
    if (role) {
      const unsigned tag = epoch + TAG_STEP;
      const float inv = __builtin_amdgcn_rcpf(lsum[0]);
#pragma unroll
      for (int i = 0; i < 4; ++i) {
        const f32x4 x = o[2 * i] * inv, y = o[2 * i + 1] * inv;
        __builtin_amdgcn_raw_buffer_store_b128(u32x4{pk(x[0], x[1]), pk(x[2], x[3]), pk(y[0], y[1]), pk(y[2], y[3])}, rp, lane * 16 + i * 1024, 0, 16);
      }
      if (live) asm volatile("s_waitcnt vmcnt(0)" ::: "memory");
      __builtin_amdgcn_raw_buffer_store_b128(u32x4{__float_as_uint(m), tag, __float_as_uint(lsum[0]), tag}, rp, g == 0 ? FLAG_OFF + lane * 16 : HEAD_BYTES, 0, 16);
      if (live) { DIAG_OUT(1); return; }
      continue;
    }
    int spins = 0;
    if (live) {
      if (!got) {
        wait_vm<0>();
#pragma unroll 1
        while (!FLAG_SET() && ++spins <= (1 << 20)) { FLAG_LOAD(); wait_vm<0>(); }
        if (spins > (1 << 20)) lsum[0] = __uint_as_float(0x7FC00000u);
        PC_LOAD_ALL();
        if (EARLY_BUMP) BUMP();
      }
      wait_vm<EARLY_BUMP>();
      RSTAMP(53);
    }
    {
      const float m2 = __uint_as_float(areg<0>()), l2 = __uint_as_float(areg<2>());
      const float mm = fmaxf(m, m2);
      const float b1 = __builtin_amdgcn_exp2f(m - mm), b2 = __builtin_amdgcn_exp2f(m2 - mm);
      const float inv = __builtin_amdgcn_rcpf(fmaf(b1, lsum[0], b2 * l2));
      LAS char* xo = lds + RING_BYTES + head * (16 * 272);
      const float w1 = b1 * inv, w2 = b2 * l2 * inv;
#define OUT1(i) do { const f32x4 y = {areg_lo<4 + 2 * (i)>(), areg_hi<4 + 2 * (i)>(), areg_lo<5 + 2 * (i)>(), areg_hi<5 + 2 * (i)>()}; \
        const f32x4 r = o[i] * w1 + y * w2; \
        *(LAS u32x2*)(xo + c * 272 + g * 8 + (i) * 32) = u32x2{pk(r[0], r[1]), pk(r[2], r[3])}; } while (0)
      OUT1(0); OUT1(1); OUT1(2); OUT1(3); OUT1(4); OUT1(5); OUT1(6); OUT1(7);
      asm volatile("" ::: "memory");
      u32x4 rows[4];
#pragma unroll
      for (int i = 0; i < 4; ++i) rows[i] = *(LAS u32x4*)(xo + (4 * i + g) * 272 + c * 16);
#pragma unroll
      for (int i = 0; i < 4; ++i) __builtin_amdgcn_raw_buffer_store_b128(rows[i], ro, lane * 16 + i * 1024, 0, OUT_AUX);
      if (!EARLY_BUMP) BUMP();
    }
    if (live) { DIAG_OUT(got ? 2 : spins ? 5 : 3); return; }
  }
  }
  {
    unit(rk, off(0), 0); unit(rq, wo, 1); unit(rk, off(1), 2); unit(rv, off(0), 5);
#define PACE_WAIT()
    unsigned t0, t1;
    asm volatile("s_load_dword %0, %2, 0\n\ts_load_dword %1, %3, 0" : "=&s"(t0), "=&s"(t1)
                 : "s"(ws + WS_OFF + ((size_t)pair * 4 + wave) * HEAD_BYTES + EPOCH_OFF), "s"((TOUCH & 2) ? (char*)O + ((size_t)pair * 4 + wave) * (SQ * 256) : (char*)Q) : "memory");
    wait_vm<12>(); unit(rk, off(2), 4);
    wait_vm<12>(); unit(rv, off(1), 7); LBARRIER();
    wait_vm<12>(); unit(rk, off(3), 6); LBARRIER();
#pragma unroll 1
    for (int s = 2; s < n - 1; ++s) {
      const bool tail = s == n - 2;
      wait_vm<12>(); if (s < N1 - 2) PACE_WAIT(); unit(rv, off(s), 2 * s + 5);
      wait_vm<12>(); if (s < N1 - 2) PACE_WAIT(); unit(tail ? rv : rk, off(tail ? s + 1 : s + 2), 2 * s + 4); LBARRIER();
    }
    wait_vm<8>(); LBARRIER();
    wait_vm<4>(); LBARRIER();
    wait_vm<0>(); LBARRIER();
    asm volatile("s_waitcnt lgkmcnt(0)" :: "s"(t0), "s"(t1) : "memory");
    DIAG_OUT(0);
    return;
  }

}

}

extern "C" void kernel_launch(void* const* d_in, const int* in_sizes, int n_in, void* d_out, int out_size, void* d_ws, size_t ws_size,
                              hipStream_t stream) {
  gqa_attn<<<2 * NPAIR, NTHREADS, 0, stream>>>((const __hip_bfloat16*)d_in[0], (const __hip_bfloat16*)d_in[1], (const __hip_bfloat16*)d_in[2],
                                                (__hip_bfloat16*)d_out, (unsigned char*)d_ws);
}
```

```cpp
#define TOUCH 1
#define OUT_AUX 18
#define PROBE 50
#include <hip/hip_runtime.h>
#include <hip/hip_bf16.h>
#include <stdint.h>

#define N1 14
#define KV_POL "nt"
#define PK1 3
#define PK2 1
#define DRY 0
#define PFMT 1
#define KV_BYTES (SK * 256)
#define MAP 0
#define WS_OFF 0
#define PACE 0
#define PACE0 400
#define STOUCH 0
#define NLOAD 4
#define PROBE_BLK -1
#define EARLY_BUMP 1
#define BUMP_AUX 16
#define NOOUT 0

namespace {

constexpr int B_ = 16, HKV = 8, SQ = 16, SK = 2048;
constexpr int TK = 64, NTILE = SK / TK;
constexpr int NPAIR = B_ * HKV;
constexpr int TILE_BYTES = TK * 256;
constexpr int NS = 8;
constexpr int RING_BYTES = NS * TILE_BYTES;
constexpr int LDS_BYTES = RING_BYTES + 4 * 16 * 272;
constexpr int NCONS = 4;
constexpr int NTHREADS = 64 * (NCONS + NLOAD);
constexpr float C2 = 0.08838834764831845f * 1.4426950408889634f;
constexpr float SLACK = 7.0f;
constexpr unsigned GE2 = 0x40004000u;
constexpr int NPC = PFMT ? 4 : 8;
constexpr int FLAG_OFF = NPC * 1024, EPOCH_OFF = FLAG_OFF + 256;
constexpr int HEAD_BYTES = EPOCH_OFF + 256;
constexpr unsigned TAG_STEP = 0x9E3779B1u;

typedef __attribute__((ext_vector_type(8))) __bf16 bf16x8;
typedef __attribute__((ext_vector_type(4))) __bf16 bf16x4;
typedef __attribute__((ext_vector_type(2))) __bf16 bf16x2;
typedef __attribute__((ext_vector_type(4))) float f32x4;
typedef __attribute__((ext_vector_type(2))) float f32x2;
typedef __attribute__((ext_vector_type(4))) unsigned u32x4;
typedef __attribute__((ext_vector_type(2))) unsigned u32x2;
#define LAS __attribute__((address_space(3)))

#define BARRIER() asm volatile("s_waitcnt lgkmcnt(0)\n\ts_barrier" ::: "memory")
#define BARRIER_IF(x) BARRIER()
#define LBARRIER() asm volatile("s_barrier" ::: "memory")
#define DIAG_OUT(code)
#define RSTAMP(i)
#define SB() __builtin_amdgcn_sched_barrier(0)

template <int N> __device__ __forceinline__ void wait_vm() { asm volatile("s_waitcnt vmcnt(%0)" ::"n"(N) : "memory"); }

__device__ __forceinline__ float xmax4(float x) {
  auto s = __builtin_amdgcn_permlane16_swap(__float_as_uint(x), __float_as_uint(x), false, false);
  x = fmaxf(__uint_as_float(s[0]), __uint_as_float(s[1]));
  auto t = __builtin_amdgcn_permlane32_swap(__float_as_uint(x), __float_as_uint(x), false, false);
  return fmaxf(__uint_as_float(t[0]), __uint_as_float(t[1]));
}
__device__ __forceinline__ unsigned pk(float a, float b) { f32x2 v = {a, b}; return __builtin_bit_cast(unsigned, __builtin_convertvector(v, bf16x2)); }
__device__ __forceinline__ f32x4 mfma(u32x4 a, u32x4 b, f32x4 c) {
  return __builtin_amdgcn_mfma_f32_16x16x32_bf16(__builtin_bit_cast(bf16x8, a), __builtin_bit_cast(bf16x8, b), c, 0, 0, 0);
}
__device__ __forceinline__ u32x2 trd(LAS char* p) { return __builtin_bit_cast(u32x2, __builtin_amdgcn_ds_read_tr16_b64_v4bf16((LAS bf16x4*)p)); }

#define A_FLAG "v220", "v221", "v222", "v223"
#define A_PC0 "v224", "v225", "v226", "v227", "v228", "v229", "v230", "v231", "v232", "v233", "v234", "v235", "v236", "v237", "v238", "v239"
#define A_PC1 "v240", "v241", "v242", "v243", "v244", "v245", "v246", "v247", "v248", "v249", "v250", "v251", "v252", "v253", "v254", "v255"
#define PC_LOAD0() \
  asm volatile("buffer_load_dwordx4 v[224:227], %0, %1, 0 offen sc1\n\t" \
               "buffer_load_dwordx4 v[228:231], %0, %1, 0 offen offset:1024 sc1\n\t" \
               "buffer_load_dwordx4 v[232:235], %0, %1, 0 offen offset:2048 sc1\n\t" \
               "buffer_load_dwordx4 v[236:239], %0, %1, 0 offen offset:3072 sc1" :: "v"(lane * 16), "s"(rp) : "memory", A_PC0)
#define PC_LOAD1() \
  asm volatile("buffer_load_dwordx4 v[240:243], %0, %1, %2 offen sc1\n\t" \
               "buffer_load_dwordx4 v[244:247], %0, %1, %2 offen offset:1024 sc1\n\t" \
               "buffer_load_dwordx4 v[248:251], %0, %1, %2 offen offset:2048 sc1\n\t" \
               "buffer_load_dwordx4 v[252:255], %0, %1, %2 offen offset:3072 sc1" :: "v"(lane * 16), "s"(rp), "s"(4096) : "memory", A_PC1)
#define PC_LOAD_ALL() PC_LOAD0()
#define FLAG_LOAD() asm volatile("buffer_load_dwordx4 v[220:223], %0, %1, %2 offen sc1" :: "v"(c * 16), "s"(rp), "s"(FLAG_OFF) : "memory", A_FLAG)
template <int I> __device__ __forceinline__ unsigned areg() { unsigned x; asm volatile("v_mov_b32 %0, v%c1" : "=v"(x) : "n"(220 + I)); return x; }
template <int I> __device__ __forceinline__ float areg_lo() { float x; asm volatile("v_lshlrev_b32 %0, 16, v%c1" : "=v"(x) : "n"(220 + I)); return x; }
template <int I> __device__ __forceinline__ float areg_hi() { float x; asm volatile("v_and_b32 %0, 0xffff0000, v%c1" : "=v"(x) : "n"(220 + I)); return x; }
#define BUMP() __builtin_amdgcn_raw_buffer_store_b32(epoch + TAG_STEP, rp, lane == 0 ? EPOCH_OFF : HEAD_BYTES, 0, BUMP_AUX)
#define FLAG_SET() (__builtin_amdgcn_ballot_w64(((areg<1>() ^ (epoch + TAG_STEP)) | (areg<3>() ^ (epoch + TAG_STEP))) != 0) == 0)

__global__ void __launch_bounds__(NTHREADS, 2) __attribute__((amdgpu_num_vgpr(220)))
gqa_attn(const __hip_bfloat16* __restrict__ Q, const __hip_bfloat16* __restrict__ K, const __hip_bfloat16* __restrict__ V,
         __hip_bfloat16* __restrict__ O, unsigned char* __restrict__ ws) {
  __shared__ __attribute__((aligned(1024))) char smem[LDS_BYTES];
  LAS char* lds = (LAS char*)smem;
  const int lane = threadIdx.x & 63;
  const int wave = __builtin_amdgcn_readfirstlane(threadIdx.x >> 6);
  const int pair = blockIdx.x & (NPAIR - 1), role = blockIdx.x >> 7;
  const int n = role ? N1 : NTILE - N1;
  const int g = lane >> 4, c = lane & 15;
  const unsigned pt0 = (unsigned)__builtin_amdgcn_s_memrealtime(); unsigned ptx = pt0; int pn = 0;
#undef LBARRIER
#define LBARRIER() do { asm volatile("s_barrier" ::: "memory"); if (pn == PROBE || (PROBE == 99)) ptx = (unsigned)__builtin_amdgcn_s_memrealtime(); ++pn; } while (0)

  const int vo0 = g * 256 + ((c ^ (g << 1)) << 4), vo1 = vo0 ^ 128;
  const __amdgpu_buffer_rsrc_t rk = __builtin_amdgcn_make_buffer_rsrc((char*)K + (size_t)pair * (SK * 256), 0, KV_BYTES, 0x00020000);
  const __amdgpu_buffer_rsrc_t rv = __builtin_amdgcn_make_buffer_rsrc((char*)V + (size_t)pair * (SK * 256), 0, KV_BYTES, 0x00020000);
  const __amdgpu_buffer_rsrc_t rq = __builtin_amdgcn_make_buffer_rsrc((char*)Q + (size_t)pair * TILE_BYTES, 0, TILE_BYTES, 0x00020000);
  const int wo = (wave & 3) * 4096;
  auto unit = [&](const __amdgpu_buffer_rsrc_t r, int so, unsigned slot) {
    const unsigned dst = (unsigned)(uintptr_t)lds + (slot & (NS - 1)) * TILE_BYTES + wo;
    asm volatile("s_mov_b32 m0, %0\n\ts_nop 0\n\t"
                 "buffer_load_dwordx4 %1, %3, %4 offen " KV_POL " lds\n\t"
                 "buffer_load_dwordx4 %2, %3, %4 offen offset:1024 " KV_POL " lds\n\t"
                 "buffer_load_dwordx4 %1, %3, %4 offen offset:2048 " KV_POL " lds\n\t"
                 "buffer_load_dwordx4 %2, %3, %4 offen offset:3072 " KV_POL " lds"
                 :: "s"(dst), "v"(vo0), "v"(vo1), "s"(r), "s"(so) : "memory");
  };
  auto off = [&](int t) { return (t < N1 ? 2 * t + role : N1 + t) * TILE_BYTES + wo; };
  if (__builtin_expect(wave >= NLOAD, 0)) {
  const int head = wave - NLOAD;
  const unsigned char* wp = ws + WS_OFF + ((size_t)pair * 4 + head) * HEAD_BYTES;
  const unsigned ka0 = c * 256 + ((g ^ ((c & 7) << 1)) << 4);
  const int vr = 4 * g + (c >> 2);
  const unsigned va0 = vr * 256 + ((vr & 7) << 5) + (c & 3) * 8;
  u32x4 ones = {0x3F803F80u, 0x3F803F80u, 0x3F803F80u, 0x3F803F80u};
  asm volatile("" : "+v"(ones));

  int live = DRY ? 0 : 1;
#pragma unroll 1
  for (;; live = 1) {
    const __amdgpu_buffer_rsrc_t rp = __builtin_amdgcn_make_buffer_rsrc((void*)wp, 0, live ? HEAD_BYTES : 0, 0x00020000);
    const __amdgpu_buffer_rsrc_t ro = __builtin_amdgcn_make_buffer_rsrc((char*)O + ((size_t)pair * 4 + head) * (SQ * 256), 0, (live && !NOOUT) ? SQ * 256 : 0, 0x00020000);
    f32x4 o[8], lsum = {0.f, 0.f, 0.f, 0.f};
#pragma unroll
    for (int db = 0; db < 8; ++db) { o[db] = f32x4{0.f, 0.f, 0.f, 0.f}; asm volatile("" : "+v"(o[db])); }
    if (live) { RSTAMP(50); BARRIER(); } else { RSTAMP(49); }
    u32x4 q[4], kf[4][4];
#pragma unroll
    for (int st = 0; st < 4; ++st) q[st] = *(const LAS u32x4*)(lds + (TILE_BYTES + head * 4096) + (ka0 ^ (st << 6)));
#pragma unroll
    for (int sub = 0; sub < 4; ++sub)
#pragma unroll
      for (int st = 0; st < 4; ++st) kf[sub][st] = *(const LAS u32x4*)(lds + (ka0 ^ (st << 6)) + sub * 4096);
    SB();
#pragma unroll
    for (int st = 0; st < 4; ++st)
#pragma unroll
      for (int i = 0; i < 4; ++i) q[st][i] = pk(__uint_as_float(q[st][i] << 16) * C2, __uint_as_float(q[st][i] & 0xFFFF0000u) * C2);
    float m = -3.0e38f;
    f32x4 c0 = {0.f, 0.f, 0.f, 0.f};
    unsigned force = GE2;
    unsigned kb = 2 * TILE_BYTES;
    unsigned kx[4];
#pragma unroll
    for (int st = 0; st < 4; ++st) kx[st] = (ka0 + kb) ^ (st << 6);
#pragma unroll
    for (int sub = 0; sub < 4; ++sub)
#pragma unroll
      for (int st = 0; st < 4; ++st) asm volatile("" : "+v"(kf[sub][st]));
    unsigned epoch;
    int got = 0;
    if (live) BARRIER();
#pragma unroll 1
    for (int left = live ? n : 1; left != 0; --left) {
      f32x4 s[4];
      float e[4][4];
      u32x4 p[2];
      const unsigned va = va0 + (left == 1 ? kb : (kb + 3 * TILE_BYTES) & (RING_BYTES - 1));
      unsigned vx[8];
#define QK1(sub, st) do { s[sub] = mfma(kf[sub][st], q[st], (st) ? s[sub] : c0); kf[sub][st] = *(const LAS u32x4*)(lds + kx[st] + (sub) * 4096); } while (0)
#define EX(sub, r) e[sub][r] = __builtin_amdgcn_exp2f(s[sub][r])
#define CV(sub, h) p[(sub) >> 1][2 * ((sub) & 1) + (h)] = pk(e[sub][2 * (h)], e[sub][2 * (h) + 1])
#define VX(i) do { vx[i] = va ^ ((i) << 5); if (i) asm volatile("" : "+v"(vx[i])); } while (0)
      QK1(0, 0); VX(0); SB(); QK1(1, 0); VX(1); SB(); QK1(0, 1); VX(2); SB(); QK1(1, 1); VX(3); SB();
      QK1(0, 2); VX(4); SB(); QK1(1, 2); VX(5); SB(); QK1(0, 3); VX(6); SB(); QK1(1, 3); VX(7); SB();
      QK1(2, 0); SB(); QK1(3, 0); SB();
      QK1(2, 1); EX(0, 0); EX(0, 1); SB(); QK1(3, 1); EX(0, 2); EX(0, 3); SB();
      QK1(2, 2); EX(1, 0); EX(1, 1); SB(); QK1(3, 2); EX(1, 2); EX(1, 3); SB();
      QK1(2, 3); CV(0, 0); CV(0, 1); SB(); QK1(3, 3); CV(1, 0); CV(1, 1); SB();
      unsigned any = (p[0][0] | p[0][1] | force) | p[0][2] | p[0][3];
      asm volatile("" : "+v"(any) :: "memory");
      kb = (kb + 2 * TILE_BYTES) & (RING_BYTES - 1);
      SB();
      EX(2, 0); EX(2, 1); EX(2, 2); EX(2, 3); EX(3, 0); EX(3, 1); EX(3, 2); EX(3, 3); SB();
      CV(2, 0); CV(2, 1); CV(3, 0); CV(3, 1);
      any = (any | p[1][0] | p[1][1]) | p[1][2] | p[1][3];
      if (__builtin_expect(__builtin_amdgcn_ballot_w64((any & GE2) != 0) != 0, 0)) {
        float t = s[0][0];
#pragma unroll
        for (int sub = 0; sub < 4; ++sub)
#pragma unroll
          for (int r = 0; r < 4; ++r) t = fmaxf(t, s[sub][r]);
        const float mn = fmaxf(m, xmax4(t) - c0[0] + SLACK);
        const float alpha = __builtin_amdgcn_exp2f(m - mn), sub_ = c0[0] + mn;
        m = mn;
        const bool fold = fabsf(mn) <= 512.f;
        const float nm = fold ? -mn : 0.f;
        c0 = f32x4{nm, nm, nm, nm};
        force = fold ? 0u : GE2;
#pragma unroll
        for (int db = 0; db < 8; ++db) o[db] *= alpha;
        lsum *= alpha;
#pragma unroll
        for (int sub = 0; sub < 4; ++sub) {
          p[sub >> 1][2 * (sub & 1)] = pk(__builtin_amdgcn_exp2f(s[sub][0] - sub_), __builtin_amdgcn_exp2f(s[sub][1] - sub_));
          p[sub >> 1][2 * (sub & 1) + 1] = pk(__builtin_amdgcn_exp2f(s[sub][2] - sub_), __builtin_amdgcn_exp2f(s[sub][3] - sub_));
        }
      }
      if (__builtin_expect(left <= PK1 + 1, 0)) {
        if (left == PK1 + 1) {
          asm volatile("s_load_dword %0, %1, %2" : "=&s"(epoch) : "s"(wp), "n"(EPOCH_OFF) : "memory");
          if (!role) FLAG_LOAD();
        }
        if (left == PK2 + 1 && !role) {
          wait_vm<0>();
          asm volatile("" : "+s"(epoch));
          if (FLAG_SET()) { PC_LOAD_ALL(); got = 1; if (EARLY_BUMP) BUMP(); } else FLAG_LOAD();
        }
      }
      BARRIER_IF(live);
      u32x2 vf[16][2];
#define VRD(i) do { vf[i][0] = trd(lds + vx[(i) & 7] + ((i) >> 3) * 8192); vf[i][1] = trd(lds + vx[(i) & 7] + ((i) >> 3) * 8192 + 4096); } while (0)
#define PV1(i) o[(i) & 7] = mfma(u32x4{vf[i][0][0], vf[i][0][1], vf[i][1][0], vf[i][1][1]}, p[(i) >> 3], o[(i) & 7])
      VRD(0); VRD(1); VRD(2); VRD(3); VRD(4); VRD(5); SB();
      lsum = mfma(ones, p[0], lsum);
      lsum = mfma(ones, p[1], lsum);
#pragma unroll
      for (int st = 0; st < 4; ++st) { kx[st] = (ka0 + kb) ^ (st << 6); if (st) asm volatile("" : "+v"(kx[st])); }
      SB();
      VRD(6); SB(); PV1(0); PV1(1); VRD(7); VRD(8); SB(); PV1(2); PV1(3); VRD(9); VRD(10); SB();
      PV1(4); PV1(5); VRD(11); VRD(12); SB(); PV1(6); PV1(7); VRD(13); VRD(14); SB();
      PV1(8); PV1(9); VRD(15); SB(); PV1(10); PV1(11); PV1(12); PV1(13); PV1(14); PV1(15); SB();
    }
    asm volatile("" : "+s"(epoch));
    RSTAMP(52);
    if (role) {
      const unsigned tag = epoch + TAG_STEP;
      const float inv = __builtin_amdgcn_rcpf(lsum[0]);
#pragma unroll
      for (int i = 0; i < 4; ++i) {
        const f32x4 x = o[2 * i] * inv, y = o[2 * i + 1] * inv;
        __builtin_amdgcn_raw_buffer_store_b128(u32x4{pk(x[0], x[1]), pk(x[2], x[3]), pk(y[0], y[1]), pk(y[2], y[3])}, rp, lane * 16 + i * 1024, 0, 16);
      }
      if (live) asm volatile("s_waitcnt vmcnt(0)" ::: "memory");
      __builtin_amdgcn_raw_buffer_store_b128(u32x4{__float_as_uint(m), tag, __float_as_uint(lsum[0]), tag}, rp, g == 0 ? FLAG_OFF + lane * 16 : HEAD_BYTES, 0, 16);
      if (live) { DIAG_OUT(1); return; }
      continue;
    }
    int spins = 0;
    if (live) {
      if (!got) {
        wait_vm<0>();
#pragma unroll 1
        while (!FLAG_SET() && ++spins <= (1 << 20)) { FLAG_LOAD(); wait_vm<0>(); }
        if (spins > (1 << 20)) lsum[0] = __uint_as_float(0x7FC00000u);
        PC_LOAD_ALL();
        if (EARLY_BUMP) BUMP();
      }
      wait_vm<EARLY_BUMP>();
      RSTAMP(53);
    }
    {
      const float m2 = __uint_as_float(areg<0>()), l2 = __uint_as_float(areg<2>());
      const float mm = fmaxf(m, m2);
      const float b1 = __builtin_amdgcn_exp2f(m - mm), b2 = __builtin_amdgcn_exp2f(m2 - mm);
      const float inv = __builtin_amdgcn_rcpf(fmaf(b1, lsum[0], b2 * l2));
      LAS char* xo = lds + RING_BYTES + head * (16 * 272);
      const float w1 = b1 * inv, w2 = b2 * l2 * inv;
#define OUT1(i) do { const f32x4 y = {areg_lo<4 + 2 * (i)>(), areg_hi<4 + 2 * (i)>(), areg_lo<5 + 2 * (i)>(), areg_hi<5 + 2 * (i)>()}; \
        const f32x4 r = o[i] * w1 + y * w2; \
        *(LAS u32x2*)(xo + c * 272 + g * 8 + (i) * 32) = u32x2{pk(r[0], r[1]), pk(r[2], r[3])}; } while (0)
      OUT1(0); OUT1(1); OUT1(2); OUT1(3); OUT1(4); OUT1(5); OUT1(6); OUT1(7);
      asm volatile("" ::: "memory");
      u32x4 rows[4];
#pragma unroll
      for (int i = 0; i < 4; ++i) rows[i] = *(LAS u32x4*)(xo + (4 * i + g) * 272 + c * 16);
#pragma unroll
      for (int i = 0; i < 4; ++i) __builtin_amdgcn_raw_buffer_store_b128(rows[i], ro, lane * 16 + i * 1024, 0, OUT_AUX);
      if (!EARLY_BUMP) BUMP();
    }
    if (live) { DIAG_OUT(got ? 2 : spins ? 5 : 3); return; }
  }
  }
  {
    asm volatile("" :: "s"(rk), "s"(rv), "s"(rq));
    if (PROBE == 50) ptx = (unsigned)__builtin_amdgcn_s_memrealtime();
    unit(rk, off(0), 0); unit(rq, wo, 1); unit(rk, off(1), 2); unit(rv, off(0), 5);
#define PACE_WAIT()
    unsigned t0, t1;
    asm volatile("s_load_dword %0, %2, 0\n\ts_load_dword %1, %3, 0" : "=&s"(t0), "=&s"(t1)
                 : "s"(ws + WS_OFF + ((size_t)pair * 4 + wave) * HEAD_BYTES + EPOCH_OFF), "s"((TOUCH & 2) ? (char*)O + ((size_t)pair * 4 + wave) * (SQ * 256) : (char*)Q) : "memory");
    wait_vm<12>(); unit(rk, off(2), 4);
    wait_vm<12>(); unit(rv, off(1), 7); LBARRIER();
    wait_vm<12>(); unit(rk, off(3), 6); LBARRIER();
#pragma unroll 1
    for (int s = 2; s < n - 1; ++s) {
      const bool tail = s == n - 2;
      wait_vm<12>(); if (s < N1 - 2) PACE_WAIT(); unit(rv, off(s), 2 * s + 5);
      wait_vm<12>(); if (s < N1 - 2) PACE_WAIT(); unit(tail ? rv : rk, off(tail ? s + 1 : s + 2), 2 * s + 4); LBARRIER();
    }
    wait_vm<8>(); LBARRIER();
    wait_vm<4>(); LBARRIER();
    wait_vm<0>(); LBARRIER();
    asm volatile("s_waitcnt lgkmcnt(0)" :: "s"(t0), "s"(t1) : "memory");
    if (PROBE_BLK < 0 || (int)blockIdx.x == PROBE_BLK) { const unsigned due = pt0 + 4000 + (ptx - pt0); while ((int)((unsigned)__builtin_amdgcn_s_memrealtime() - due) < 0) __builtin_amdgcn_s_sleep(1); }
    DIAG_OUT(0);
    return;
  }

}

}

extern "C" void kernel_launch(void* const* d_in, const int* in_sizes, int n_in, void* d_out, int out_size, void* d_ws, size_t ws_size,
                              hipStream_t stream) {
  gqa_attn<<<2 * NPAIR, NTHREADS, 0, stream>>>((const __hip_bfloat16*)d_in[0], (const __hip_bfloat16*)d_in[1], (const __hip_bfloat16*)d_in[2],
                                                (__hip_bfloat16*)d_out, (unsigned char*)d_ws);
}
```

```cpp
#define TOUCH 1
#define OUT_AUX 18
#define PROBE 77
#define PROBE_BLK 0
#include <hip/hip_runtime.h>
#include <hip/hip_bf16.h>
#include <stdint.h>

#define N1 14
#define KV_POL "nt"
#define PK1 3
#define PK2 1
#define DRY 0
#define PFMT 1
#define KV_BYTES (SK * 256)
#define MAP 0
#define WS_OFF 0
#define PACE 0
#define PACE0 400
#define STOUCH 0
#define NLOAD 4
#define EARLY_BUMP 1
#define BUMP_AUX 16
#define NOOUT 0

namespace {

constexpr int B_ = 16, HKV = 8, SQ = 16, SK = 2048;
constexpr int TK = 64, NTILE = SK / TK;
constexpr int NPAIR = B_ * HKV;
constexpr int TILE_BYTES = TK * 256;
constexpr int NS = 8;
constexpr int RING_BYTES = NS * TILE_BYTES;
constexpr int LDS_BYTES = RING_BYTES + 4 * 16 * 272;
constexpr int NCONS = 4;
constexpr int NTHREADS = 64 * (NCONS + NLOAD);
constexpr float C2 = 0.08838834764831845f * 1.4426950408889634f;
constexpr float SLACK = 7.0f;
constexpr unsigned GE2 = 0x40004000u;
constexpr int NPC = PFMT ? 4 : 8;
constexpr int FLAG_OFF = NPC * 1024, EPOCH_OFF = FLAG_OFF + 256;
constexpr int HEAD_BYTES = EPOCH_OFF + 256;
constexpr unsigned TAG_STEP = 0x9E3779B1u;

typedef __attribute__((ext_vector_type(8))) __bf16 bf16x8;
typedef __attribute__((ext_vector_type(4))) __bf16 bf16x4;
typedef __attribute__((ext_vector_type(2))) __bf16 bf16x2;
typedef __attribute__((ext_vector_type(4))) float f32x4;
typedef __attribute__((ext_vector_type(2))) float f32x2;
typedef __attribute__((ext_vector_type(4))) unsigned u32x4;
typedef __attribute__((ext_vector_type(2))) unsigned u32x2;
#define LAS __attribute__((address_space(3)))

#define BARRIER() asm volatile("s_waitcnt lgkmcnt(0)\n\ts_barrier" ::: "memory")
#define BARRIER_IF(x) BARRIER()
#define LBARRIER() asm volatile("s_barrier" ::: "memory")
#define DIAG_OUT(code)
#define RSTAMP(i)
#define SB() __builtin_amdgcn_sched_barrier(0)

template <int N> __device__ __forceinline__ void wait_vm() { asm volatile("s_waitcnt vmcnt(%0)" ::"n"(N) : "memory"); }

__device__ __forceinline__ float xmax4(float x) {
  auto s = __builtin_amdgcn_permlane16_swap(__float_as_uint(x), __float_as_uint(x), false, false);
  x = fmaxf(__uint_as_float(s[0]), __uint_as_float(s[1]));
  auto t = __builtin_amdgcn_permlane32_swap(__float_as_uint(x), __float_as_uint(x), false, false);
  return fmaxf(__uint_as_float(t[0]), __uint_as_float(t[1]));
}
__device__ __forceinline__ unsigned pk(float a, float b) { f32x2 v = {a, b}; return __builtin_bit_cast(unsigned, __builtin_convertvector(v, bf16x2)); }
__device__ __forceinline__ f32x4 mfma(u32x4 a, u32x4 b, f32x4 c) {
  return __builtin_amdgcn_mfma_f32_16x16x32_bf16(__builtin_bit_cast(bf16x8, a), __builtin_bit_cast(bf16x8, b), c, 0, 0, 0);
}
__device__ __forceinline__ u32x2 trd(LAS char* p) { return __builtin_bit_cast(u32x2, __builtin_amdgcn_ds_read_tr16_b64_v4bf16((LAS bf16x4*)p)); }

#define A_FLAG "v220", "v221", "v222", "v223"
#define A_PC0 "v224", "v225", "v226", "v227", "v228", "v229", "v230", "v231", "v232", "v233", "v234", "v235", "v236", "v237", "v238", "v239"
#define A_PC1 "v240", "v241", "v242", "v243", "v244", "v245", "v246", "v247", "v248", "v249", "v250", "v251", "v252", "v253", "v254", "v255"
#define PC_LOAD0() \
  asm volatile("buffer_load_dwordx4 v[224:227], %0, %1, 0 offen sc1\n\t" \
               "buffer_load_dwordx4 v[228:231], %0, %1, 0 offen offset:1024 sc1\n\t" \
               "buffer_load_dwordx4 v[232:235], %0, %1, 0 offen offset:2048 sc1\n\t" \
               "buffer_load_dwordx4 v[236:239], %0, %1, 0 offen offset:3072 sc1" :: "v"(lane * 16), "s"(rp) : "memory", A_PC0)
#define PC_LOAD1() \
  asm volatile("buffer_load_dwordx4 v[240:243], %0, %1, %2 offen sc1\n\t" \
               "buffer_load_dwordx4 v[244:247], %0, %1, %2 offen offset:1024 sc1\n\t" \
               "buffer_load_dwordx4 v[248:251], %0, %1, %2 offen offset:2048 sc1\n\t" \
               "buffer_load_dwordx4 v[252:255], %0, %1, %2 offen offset:3072 sc1" :: "v"(lane * 16), "s"(rp), "s"(4096) : "memory", A_PC1)
#define PC_LOAD_ALL() PC_LOAD0()
#define FLAG_LOAD() asm volatile("buffer_load_dwordx4 v[220:223], %0, %1, %2 offen sc1" :: "v"(c * 16), "s"(rp), "s"(FLAG_OFF) : "memory", A_FLAG)
template <int I> __device__ __forceinline__ unsigned areg() { unsigned x; asm volatile("v_mov_b32 %0, v%c1" : "=v"(x) : "n"(220 + I)); return x; }
template <int I> __device__ __forceinline__ float areg_lo() { float x; asm volatile("v_lshlrev_b32 %0, 16, v%c1" : "=v"(x) : "n"(220 + I)); return x; }
template <int I> __device__ __forceinline__ float areg_hi() { float x; asm volatile("v_and_b32 %0, 0xffff0000, v%c1" : "=v"(x) : "n"(220 + I)); return x; }
#define BUMP() __builtin_amdgcn_raw_buffer_store_b32(epoch + TAG_STEP, rp, lane == 0 ? EPOCH_OFF : HEAD_BYTES, 0, BUMP_AUX)
#define FLAG_SET() (__builtin_amdgcn_ballot_w64(((areg<1>() ^ (epoch + TAG_STEP)) | (areg<3>() ^ (epoch + TAG_STEP))) != 0) == 0)

__global__ void __launch_bounds__(NTHREADS, 2) __attribute__((amdgpu_num_vgpr(220)))
gqa_attn(const __hip_bfloat16* __restrict__ Q, const __hip_bfloat16* __restrict__ K, const __hip_bfloat16* __restrict__ V,
         __hip_bfloat16* __restrict__ O, unsigned char* __restrict__ ws) {
  __shared__ __attribute__((aligned(1024))) char smem[LDS_BYTES];
  unsigned long long ptop; asm volatile("s_memrealtime %0\n\ts_waitcnt lgkmcnt(0)" : "=s"(ptop) :: "memory"); __builtin_amdgcn_sched_barrier(0);
  LAS char* lds = (LAS char*)smem;
  const int lane = threadIdx.x & 63;
  const int wave = __builtin_amdgcn_readfirstlane(threadIdx.x >> 6);
  const int pair = blockIdx.x & (NPAIR - 1), role = blockIdx.x >> 7;
  const int n = role ? N1 : NTILE - N1;
  const int g = lane >> 4, c = lane & 15;
  const unsigned pt0 = (unsigned)ptop; unsigned ptx = pt0; int pn = 0;
#undef LBARRIER
#define LBARRIER() do { asm volatile("s_barrier" ::: "memory"); if (pn == PROBE || (PROBE == 99)) ptx = (unsigned)__builtin_amdgcn_s_memrealtime(); ++pn; } while (0)

  const int vo0 = g * 256 + ((c ^ (g << 1)) << 4), vo1 = vo0 ^ 128;
  const __amdgpu_buffer_rsrc_t rk = __builtin_amdgcn_make_buffer_rsrc((char*)K + (size_t)pair * (SK * 256), 0, KV_BYTES, 0x00020000);
  const __amdgpu_buffer_rsrc_t rv = __builtin_amdgcn_make_buffer_rsrc((char*)V + (size_t)pair * (SK * 256), 0, KV_BYTES, 0x00020000);
  const __amdgpu_buffer_rsrc_t rq = __builtin_amdgcn_make_buffer_rsrc((char*)Q + (size_t)pair * TILE_BYTES, 0, TILE_BYTES, 0x00020000);
  const int wo = (wave & 3) * 4096;
  auto unit = [&](const __amdgpu_buffer_rsrc_t r, int so, unsigned slot) {
    const unsigned dst = (unsigned)(uintptr_t)lds + (slot & (NS - 1)) * TILE_BYTES + wo;
    asm volatile("s_mov_b32 m0, %0\n\ts_nop 0\n\t"
                 "buffer_load_dwordx4 %1, %3, %4 offen " KV_POL " lds\n\t"
                 "buffer_load_dwordx4 %2, %3, %4 offen offset:1024 " KV_POL " lds\n\t"
                 "buffer_load_dwordx4 %1, %3, %4 offen offset:2048 " KV_POL " lds\n\t"
                 "buffer_load_dwordx4 %2, %3, %4 offen offset:3072 " KV_POL " lds"
                 :: "s"(dst), "v"(vo0), "v"(vo1), "s"(r), "s"(so) : "memory");
  };
  auto off = [&](int t) { return (t < N1 ? 2 * t + role : N1 + t) * TILE_BYTES + wo; };
  if (__builtin_expect(wave >= NLOAD, 0)) {
  const int head = wave - NLOAD;
  const unsigned char* wp = ws + WS_OFF + ((size_t)pair * 4 + head) * HEAD_BYTES;
  const unsigned ka0 = c * 256 + ((g ^ ((c & 7) << 1)) << 4);
  const int vr = 4 * g + (c >> 2);
  const unsigned va0 = vr * 256 + ((vr & 7) << 5) + (c & 3) * 8;
  u32x4 ones = {0x3F803F80u, 0x3F803F80u, 0x3F803F80u, 0x3F803F80u};
  asm volatile("" : "+v"(ones));

  int live = DRY ? 0 : 1;
#pragma unroll 1
  for (;; live = 1) {
    const __amdgpu_buffer_rsrc_t rp = __builtin_amdgcn_make_buffer_rsrc((void*)wp, 0, live ? HEAD_BYTES : 0, 0x00020000);
    const __amdgpu_buffer_rsrc_t ro = __builtin_amdgcn_make_buffer_rsrc((char*)O + ((size_t)pair * 4 + head) * (SQ * 256), 0, (live && !NOOUT) ? SQ * 256 : 0, 0x00020000);
    f32x4 o[8], lsum = {0.f, 0.f, 0.f, 0.f};
#pragma unroll
    for (int db = 0; db < 8; ++db) { o[db] = f32x4{0.f, 0.f, 0.f, 0.f}; asm volatile("" : "+v"(o[db])); }
    if (live) { RSTAMP(50); BARRIER(); } else { RSTAMP(49); }
    u32x4 q[4], kf[4][4];
#pragma unroll
    for (int st = 0; st < 4; ++st) q[st] = *(const LAS u32x4*)(lds + (TILE_BYTES + head * 4096) + (ka0 ^ (st << 6)));
#pragma unroll
    for (int sub = 0; sub < 4; ++sub)
#pragma unroll
      for (int st = 0; st < 4; ++st) kf[sub][st] = *(const LAS u32x4*)(lds + (ka0 ^ (st << 6)) + sub * 4096);
    SB();
#pragma unroll
    for (int st = 0; st < 4; ++st)
#pragma unroll
      for (int i = 0; i < 4; ++i) q[st][i] = pk(__uint_as_float(q[st][i] << 16) * C2, __uint_as_float(q[st][i] & 0xFFFF0000u) * C2);
    float m = -3.0e38f;
    f32x4 c0 = {0.f, 0.f, 0.f, 0.f};
    unsigned force = GE2;
    unsigned kb = 2 * TILE_BYTES;
    unsigned kx[4];
#pragma unroll
    for (int st = 0; st < 4; ++st) kx[st] = (ka0 + kb) ^ (st << 6);
#pragma unroll
    for (int sub = 0; sub < 4; ++sub)
#pragma unroll
      for (int st = 0; st < 4; ++st) asm volatile("" : "+v"(kf[sub][st]));
    unsigned epoch;
    int got = 0;
    if (live) BARRIER();
#pragma unroll 1
    for (int left = live ? n : 1; left != 0; --left) {
      f32x4 s[4];
      float e[4][4];
      u32x4 p[2];
      const unsigned va = va0 + (left == 1 ? kb : (kb + 3 * TILE_BYTES) & (RING_BYTES - 1));
      unsigned vx[8];
#define QK1(sub, st) do { s[sub] = mfma(kf[sub][st], q[st], (st) ? s[sub] : c0); kf[sub][st] = *(const LAS u32x4*)(lds + kx[st] + (sub) * 4096); } while (0)
#define EX(sub, r) e[sub][r] = __builtin_amdgcn_exp2f(s[sub][r])
#define CV(sub, h) p[(sub) >> 1][2 * ((sub) & 1) + (h)] = pk(e[sub][2 * (h)], e[sub][2 * (h) + 1])
#define VX(i) do { vx[i] = va ^ ((i) << 5); if (i) asm volatile("" : "+v"(vx[i])); } while (0)
      QK1(0, 0); VX(0); SB(); QK1(1, 0); VX(1); SB(); QK1(0, 1); VX(2); SB(); QK1(1, 1); VX(3); SB();
      QK1(0, 2); VX(4); SB(); QK1(1, 2); VX(5); SB(); QK1(0, 3); VX(6); SB(); QK1(1, 3); VX(7); SB();
      QK1(2, 0); SB(); QK1(3, 0); SB();
      QK1(2, 1); EX(0, 0); EX(0, 1); SB(); QK1(3, 1); EX(0, 2); EX(0, 3); SB();
      QK1(2, 2); EX(1, 0); EX(1, 1); SB(); QK1(3, 2); EX(1, 2); EX(1, 3); SB();
      QK1(2, 3); CV(0, 0); CV(0, 1); SB(); QK1(3, 3); CV(1, 0); CV(1, 1); SB();
      unsigned any = (p[0][0] | p[0][1] | force) | p[0][2] | p[0][3];
      asm volatile("" : "+v"(any) :: "memory");
      kb = (kb + 2 * TILE_BYTES) & (RING_BYTES - 1);
      SB();
      EX(2, 0); EX(2, 1); EX(2, 2); EX(2, 3); EX(3, 0); EX(3, 1); EX(3, 2); EX(3, 3); SB();
      CV(2, 0); CV(2, 1); CV(3, 0); CV(3, 1);
      any = (any | p[1][0] | p[1][1]) | p[1][2] | p[1][3];
      if (__builtin_expect(__builtin_amdgcn_ballot_w64((any & GE2) != 0) != 0, 0)) {
        float t = s[0][0];
#pragma unroll
        for (int sub = 0; sub < 4; ++sub)
#pragma unroll
          for (int r = 0; r < 4; ++r) t = fmaxf(t, s[sub][r]);
        const float mn = fmaxf(m, xmax4(t) - c0[0] + SLACK);
        const float alpha = __builtin_amdgcn_exp2f(m - mn), sub_ = c0[0] + mn;
        m = mn;
        const bool fold = fabsf(mn) <= 512.f;
        const float nm = fold ? -mn : 0.f;
        c0 = f32x4{nm, nm, nm, nm};
        force = fold ? 0u : GE2;
#pragma unroll
        for (int db = 0; db < 8; ++db) o[db] *= alpha;
        lsum *= alpha;
#pragma unroll
        for (int sub = 0; sub < 4; ++sub) {
          p[sub >> 1][2 * (sub & 1)] = pk(__builtin_amdgcn_exp2f(s[sub][0] - sub_), __builtin_amdgcn_exp2f(s[sub][1] - sub_));
          p[sub >> 1][2 * (sub & 1) + 1] = pk(__builtin_amdgcn_exp2f(s[sub][2] - sub_), __builtin_amdgcn_exp2f(s[sub][3] - sub_));
        }
      }
      if (__builtin_expect(left <= PK1 + 1, 0)) {
        if (left == PK1 + 1) {
          asm volatile("s_load_dword %0, %1, %2" : "=&s"(epoch) : "s"(wp), "n"(EPOCH_OFF) : "memory");
          if (!role) FLAG_LOAD();
        }
        if (left == PK2 + 1 && !role) {
          wait_vm<0>();
          asm volatile("" : "+s"(epoch));
          if (FLAG_SET()) { PC_LOAD_ALL(); got = 1; if (EARLY_BUMP) BUMP(); } else FLAG_LOAD();
        }
      }
      BARRIER_IF(live);
      u32x2 vf[16][2];
#define VRD(i) do { vf[i][0] = trd(lds + vx[(i) & 7] + ((i) >> 3) * 8192); vf[i][1] = trd(lds + vx[(i) & 7] + ((i) >> 3) * 8192 + 4096); } while (0)
#define PV1(i) o[(i) & 7] = mfma(u32x4{vf[i][0][0], vf[i][0][1], vf[i][1][0], vf[i][1][1]}, p[(i) >> 3], o[(i) & 7])
      VRD(0); VRD(1); VRD(2); VRD(3); VRD(4); VRD(5); SB();
      lsum = mfma(ones, p[0], lsum);
      lsum = mfma(ones, p[1], lsum);
#pragma unroll
      for (int st = 0; st < 4; ++st) { kx[st] = (ka0 + kb) ^ (st << 6); if (st) asm volatile("" : "+v"(kx[st])); }
      SB();
      VRD(6); SB(); PV1(0); PV1(1); VRD(7); VRD(8); SB(); PV1(2); PV1(3); VRD(9); VRD(10); SB();
      PV1(4); PV1(5); VRD(11); VRD(12); SB(); PV1(6); PV1(7); VRD(13); VRD(14); SB();
      PV1(8); PV1(9); VRD(15); SB(); PV1(10); PV1(11); PV1(12); PV1(13); PV1(14); PV1(15); SB();
    }
    asm volatile("" : "+s"(epoch));
    RSTAMP(52);
    if (role) {
      const unsigned tag = epoch + TAG_STEP;
      const float inv = __builtin_amdgcn_rcpf(lsum[0]);
#pragma unroll
      for (int i = 0; i < 4; ++i) {
        const f32x4 x = o[2 * i] * inv, y = o[2 * i + 1] * inv;
        __builtin_amdgcn_raw_buffer_store_b128(u32x4{pk(x[0], x[1]), pk(x[2], x[3]), pk(y[0], y[1]), pk(y[2], y[3])}, rp, lane * 16 + i * 1024, 0, 16);
      }
      if (live) asm volatile("s_waitcnt vmcnt(0)" ::: "memory");
      __builtin_amdgcn_raw_buffer_store_b128(u32x4{__float_as_uint(m), tag, __float_as_uint(lsum[0]), tag}, rp, g == 0 ? FLAG_OFF + lane * 16 : HEAD_BYTES, 0, 16);
      if (live) { DIAG_OUT(1); return; }
      continue;
    }
    int spins = 0;
    if (live) {
      if (!got) {
        wait_vm<0>();
#pragma unroll 1
        while (!FLAG_SET() && ++spins <= (1 << 20)) { FLAG_LOAD(); wait_vm<0>(); }
        if (spins > (1 << 20)) lsum[0] = __uint_as_float(0x7FC00000u);
        PC_LOAD_ALL();
        if (EARLY_BUMP) BUMP();
      }
      wait_vm<EARLY_BUMP>();
      RSTAMP(53);
    }
    {
      const float m2 = __uint_as_float(areg<0>()), l2 = __uint_as_float(areg<2>());
      const float mm = fmaxf(m, m2);
      const float b1 = __builtin_amdgcn_exp2f(m - mm), b2 = __builtin_amdgcn_exp2f(m2 - mm);
      const float inv = __builtin_amdgcn_rcpf(fmaf(b1, lsum[0], b2 * l2));
      LAS char* xo = lds + RING_BYTES + head * (16 * 272);
      const float w1 = b1 * inv, w2 = b2 * l2 * inv;
#define OUT1(i) do { const f32x4 y = {areg_lo<4 + 2 * (i)>(), areg_hi<4 + 2 * (i)>(), areg_lo<5 + 2 * (i)>(), areg_hi<5 + 2 * (i)>()}; \
        const f32x4 r = o[i] * w1 + y * w2; \
        *(LAS u32x2*)(xo + c * 272 + g * 8 + (i) * 32) = u32x2{pk(r[0], r[1]), pk(r[2], r[3])}; } while (0)
      OUT1(0); OUT1(1); OUT1(2); OUT1(3); OUT1(4); OUT1(5); OUT1(6); OUT1(7);
      asm volatile("" ::: "memory");
      u32x4 rows[4];
#pragma unroll
      for (int i = 0; i < 4; ++i) rows[i] = *(LAS u32x4*)(xo + (4 * i + g) * 272 + c * 16);
#pragma unroll
      for (int i = 0; i < 4; ++i) __builtin_amdgcn_raw_buffer_store_b128(rows[i], ro, lane * 16 + i * 1024, 0, OUT_AUX);
      if (!EARLY_BUMP) BUMP();
    }
    if (live) { DIAG_OUT(got ? 2 : spins ? 5 : 3); return; }
  }
  }
  {
    asm volatile("" :: "s"(rk), "s"(rv), "s"(rq));
    if (PROBE == 50) ptx = (unsigned)__builtin_amdgcn_s_memrealtime();
    unit(rk, off(0), 0); unit(rq, wo, 1); unit(rk, off(1), 2); unit(rv, off(0), 5);
#define PACE_WAIT()
    unsigned t0, t1;
    asm volatile("s_load_dword %0, %2, 0\n\ts_load_dword %1, %3, 0" : "=&s"(t0), "=&s"(t1)
                 : "s"(ws + WS_OFF + ((size_t)pair * 4 + wave) * HEAD_BYTES + EPOCH_OFF), "s"((TOUCH & 2) ? (char*)O + ((size_t)pair * 4 + wave) * (SQ * 256) : (char*)Q) : "memory");
    wait_vm<12>(); unit(rk, off(2), 4);
    wait_vm<12>(); unit(rv, off(1), 7); LBARRIER();
    wait_vm<12>(); unit(rk, off(3), 6); LBARRIER();
#pragma unroll 1
    for (int s = 2; s < n - 1; ++s) {
      const bool tail = s == n - 2;
      wait_vm<12>(); if (s < N1 - 2) PACE_WAIT(); unit(rv, off(s), 2 * s + 5);
      wait_vm<12>(); if (s < N1 - 2) PACE_WAIT(); unit(tail ? rv : rk, off(tail ? s + 1 : s + 2), 2 * s + 4); LBARRIER();
    }
    wait_vm<8>(); LBARRIER();
    wait_vm<4>(); LBARRIER();
    wait_vm<0>(); LBARRIER();
    asm volatile("s_waitcnt lgkmcnt(0)" :: "s"(t0), "s"(t1) : "memory");
    if (PROBE_BLK < 0 || (int)blockIdx.x == PROBE_BLK) { const unsigned due = pt0 + 4000 + (ptx - pt0); while ((int)((unsigned)__builtin_amdgcn_s_memrealtime() - due) < 0) __builtin_amdgcn_s_sleep(1); }
    DIAG_OUT(0);
    return;
  }

}

}

extern "C" void kernel_launch(void* const* d_in, const int* in_sizes, int n_in, void* d_out, int out_size, void* d_ws, size_t ws_size,
                              hipStream_t stream) {
  gqa_attn<<<2 * NPAIR, NTHREADS, 0, stream>>>((const __hip_bfloat16*)d_in[0], (const __hip_bfloat16*)d_in[1], (const __hip_bfloat16*)d_in[2],
                                                (__hip_bfloat16*)d_out, (unsigned char*)d_ws);
}
```

```cpp
#define TOUCH 1
#define OUT_AUX 18
#define EXP 2
#include <hip/hip_runtime.h>
#include <hip/hip_bf16.h>
#include <stdint.h>

#define N1 14
#define KV_POL "nt"
#define PK1 3
#define PK2 1
#define DRY 0
#define PFMT 1
#define KV_BYTES (SK * 256)
#define MAP 0
#define WS_OFF 0
#define PACE 0
#define PACE0 400
#define STOUCH 0
#define NLOAD 4
#define EARLY_BUMP 1
#define BUMP_AUX 16
#define NOOUT 0

namespace {

constexpr int B_ = 16, HKV = 8, SQ = 16, SK = 2048;
constexpr int TK = 64, NTILE = SK / TK;
constexpr int NPAIR = B_ * HKV;
constexpr int TILE_BYTES = TK * 256;
constexpr int NS = 8;
constexpr int RING_BYTES = NS * TILE_BYTES;
constexpr int LDS_BYTES = RING_BYTES + 4 * 16 * 272;
constexpr int NCONS = 4;
constexpr int NTHREADS = 64 * (NCONS + NLOAD);
constexpr float C2 = 0.08838834764831845f * 1.4426950408889634f;
constexpr float SLACK = 7.0f;
constexpr unsigned GE2 = 0x40004000u;
constexpr int NPC = PFMT ? 4 : 8;
constexpr int FLAG_OFF = NPC * 1024, EPOCH_OFF = FLAG_OFF + 256;
constexpr int HEAD_BYTES = EPOCH_OFF + 256;
constexpr unsigned TAG_STEP = 0x9E3779B1u;

typedef __attribute__((ext_vector_type(8))) __bf16 bf16x8;
typedef __attribute__((ext_vector_type(4))) __bf16 bf16x4;
typedef __attribute__((ext_vector_type(2))) __bf16 bf16x2;
typedef __attribute__((ext_vector_type(4))) float f32x4;
typedef __attribute__((ext_vector_type(2))) float f32x2;
typedef __attribute__((ext_vector_type(4))) unsigned u32x4;
typedef __attribute__((ext_vector_type(2))) unsigned u32x2;
#define LAS __attribute__((address_space(3)))

#define BARRIER() asm volatile("s_waitcnt lgkmcnt(0)\n\ts_barrier" ::: "memory")
#define BARRIER_IF(x) BARRIER()
#define LBARRIER() asm volatile("s_barrier" ::: "memory")
#define DIAG_OUT(code)
#define RSTAMP(i)
#define SB() __builtin_amdgcn_sched_barrier(0)

template <int N> __device__ __forceinline__ void wait_vm() { asm volatile("s_waitcnt vmcnt(%0)" ::"n"(N) : "memory"); }

__device__ __forceinline__ float xmax4(float x) {
  auto s = __builtin_amdgcn_permlane16_swap(__float_as_uint(x), __float_as_uint(x), false, false);
  x = fmaxf(__uint_as_float(s[0]), __uint_as_float(s[1]));
  auto t = __builtin_amdgcn_permlane32_swap(__float_as_uint(x), __float_as_uint(x), false, false);
  return fmaxf(__uint_as_float(t[0]), __uint_as_float(t[1]));
}
__device__ __forceinline__ unsigned pk(float a, float b) { f32x2 v = {a, b}; return __builtin_bit_cast(unsigned, __builtin_convertvector(v, bf16x2)); }
__device__ __forceinline__ f32x4 mfma(u32x4 a, u32x4 b, f32x4 c) {
  return __builtin_amdgcn_mfma_f32_16x16x32_bf16(__builtin_bit_cast(bf16x8, a), __builtin_bit_cast(bf16x8, b), c, 0, 0, 0);
}
__device__ __forceinline__ u32x2 trd(LAS char* p) { return __builtin_bit_cast(u32x2, __builtin_amdgcn_ds_read_tr16_b64_v4bf16((LAS bf16x4*)p)); }

#define A_FLAG "v220", "v221", "v222", "v223"
#define A_PC0 "v224", "v225", "v226", "v227", "v228", "v229", "v230", "v231", "v232", "v233", "v234", "v235", "v236", "v237", "v238", "v239"
#define A_PC1 "v240", "v241", "v242", "v243", "v244", "v245", "v246", "v247", "v248", "v249", "v250", "v251", "v252", "v253", "v254", "v255"
#define PC_LOAD0() \
  asm volatile("buffer_load_dwordx4 v[224:227], %0, %1, 0 offen sc1\n\t" \
               "buffer_load_dwordx4 v[228:231], %0, %1, 0 offen offset:1024 sc1\n\t" \
               "buffer_load_dwordx4 v[232:235], %0, %1, 0 offen offset:2048 sc1\n\t" \
               "buffer_load_dwordx4 v[236:239], %0, %1, 0 offen offset:3072 sc1" :: "v"(lane * 16), "s"(rp) : "memory", A_PC0)
#define PC_LOAD1() \
  asm volatile("buffer_load_dwordx4 v[240:243], %0, %1, %2 offen sc1\n\t" \
               "buffer_load_dwordx4 v[244:247], %0, %1, %2 offen offset:1024 sc1\n\t" \
               "buffer_load_dwordx4 v[248:251], %0, %1, %2 offen offset:2048 sc1\n\t" \
               "buffer_load_dwordx4 v[252:255], %0, %1, %2 offen offset:3072 sc1" :: "v"(lane * 16), "s"(rp), "s"(4096) : "memory", A_PC1)
#define PC_LOAD_ALL() PC_LOAD0()
#define FLAG_LOAD() asm volatile("buffer_load_dwordx4 v[220:223], %0, %1, %2 offen sc1" :: "v"(c * 16), "s"(rp), "s"(FLAG_OFF) : "memory", A_FLAG)
template <int I> __device__ __forceinline__ unsigned areg() { unsigned x; asm volatile("v_mov_b32 %0, v%c1" : "=v"(x) : "n"(220 + I)); return x; }
template <int I> __device__ __forceinline__ float areg_lo() { float x; asm volatile("v_lshlrev_b32 %0, 16, v%c1" : "=v"(x) : "n"(220 + I)); return x; }
template <int I> __device__ __forceinline__ float areg_hi() { float x; asm volatile("v_and_b32 %0, 0xffff0000, v%c1" : "=v"(x) : "n"(220 + I)); return x; }
#define BUMP() __builtin_amdgcn_raw_buffer_store_b32(epoch + TAG_STEP, rp, lane == 0 ? EPOCH_OFF : HEAD_BYTES, 0, BUMP_AUX)
#define FLAG_SET() (__builtin_amdgcn_ballot_w64(((areg<1>() ^ (epoch + TAG_STEP)) | (areg<3>() ^ (epoch + TAG_STEP))) != 0) == 0)

__global__ void __launch_bounds__(NTHREADS, 2) __attribute__((amdgpu_num_vgpr(220)))
gqa_attn(const __hip_bfloat16* __restrict__ Q, const __hip_bfloat16* __restrict__ K, const __hip_bfloat16* __restrict__ V,
         __hip_bfloat16* __restrict__ O, unsigned char* __restrict__ ws) {
  __shared__ __attribute__((aligned(1024))) char smem[LDS_BYTES];
  const unsigned ptop = (unsigned)__builtin_amdgcn_s_memrealtime();
  LAS char* lds = (LAS char*)smem;
  const int lane = threadIdx.x & 63;
  const int wave = __builtin_amdgcn_readfirstlane(threadIdx.x >> 6);
  const int pair = blockIdx.x & (NPAIR - 1), role = blockIdx.x >> 7;
  const int n = role ? N1 : NTILE - N1;
  const int g = lane >> 4, c = lane & 15;

  const int vo0 = g * 256 + ((c ^ (g << 1)) << 4), vo1 = vo0 ^ 128;
  const __amdgpu_buffer_rsrc_t rk = __builtin_amdgcn_make_buffer_rsrc((char*)K + (size_t)pair * (SK * 256), 0, KV_BYTES, 0x00020000);
  const __amdgpu_buffer_rsrc_t rv = __builtin_amdgcn_make_buffer_rsrc((char*)V + (size_t)pair * (SK * 256), 0, KV_BYTES, 0x00020000);
  const __amdgpu_buffer_rsrc_t rq = __builtin_amdgcn_make_buffer_rsrc((char*)Q + (size_t)pair * TILE_BYTES, 0, TILE_BYTES, 0x00020000);
  const int wo = (wave & 3) * 4096;
  auto unit = [&](const __amdgpu_buffer_rsrc_t r, int so, unsigned slot) {
    const unsigned dst = (unsigned)(uintptr_t)lds + (slot & (NS - 1)) * TILE_BYTES + wo;
    asm volatile("s_mov_b32 m0, %0\n\ts_nop 0\n\t"
                 "buffer_load_dwordx4 %1, %3, %4 offen " KV_POL " lds\n\t"
                 "buffer_load_dwordx4 %2, %3, %4 offen offset:1024 " KV_POL " lds\n\t"
                 "buffer_load_dwordx4 %1, %3, %4 offen offset:2048 " KV_POL " lds\n\t"
                 "buffer_load_dwordx4 %2, %3, %4 offen offset:3072 " KV_POL " lds"
                 :: "s"(dst), "v"(vo0), "v"(vo1), "s"(r), "s"(so) : "memory");
  };
  auto off = [&](int t) { return (t < N1 ? 2 * t + role : N1 + t) * TILE_BYTES + wo; };
  if (__builtin_expect(wave >= NLOAD, 0)) {
  const int head = wave - NLOAD;
  const unsigned char* wp = ws + WS_OFF + ((size_t)pair * 4 + head) * HEAD_BYTES;
  const unsigned ka0 = c * 256 + ((g ^ ((c & 7) << 1)) << 4);
  const int vr = 4 * g + (c >> 2);
  const unsigned va0 = vr * 256 + ((vr & 7) << 5) + (c & 3) * 8;
  u32x4 ones = {0x3F803F80u, 0x3F803F80u, 0x3F803F80u, 0x3F803F80u};
  asm volatile("" : "+v"(ones));

  int live = DRY ? 0 : 1;
#pragma unroll 1
  for (;; live = 1) {
    const __amdgpu_buffer_rsrc_t rp = __builtin_amdgcn_make_buffer_rsrc((void*)wp, 0, live ? HEAD_BYTES : 0, 0x00020000);
    const __amdgpu_buffer_rsrc_t ro = __builtin_amdgcn_make_buffer_rsrc((char*)O + ((size_t)pair * 4 + head) * (SQ * 256), 0, (live && !NOOUT) ? SQ * 256 : 0, 0x00020000);
    f32x4 o[8], lsum = {0.f, 0.f, 0.f, 0.f};
#pragma unroll
    for (int db = 0; db < 8; ++db) { o[db] = f32x4{0.f, 0.f, 0.f, 0.f}; asm volatile("" : "+v"(o[db])); }
    if (live) { RSTAMP(50); BARRIER(); } else { RSTAMP(49); }
    u32x4 q[4], kf[4][4];
#pragma unroll
    for (int st = 0; st < 4; ++st) q[st] = *(const LAS u32x4*)(lds + (TILE_BYTES + head * 4096) + (ka0 ^ (st << 6)));
#pragma unroll
    for (int sub = 0; sub < 4; ++sub)
#pragma unroll
      for (int st = 0; st < 4; ++st) kf[sub][st] = *(const LAS u32x4*)(lds + (ka0 ^ (st << 6)) + sub * 4096);
    SB();
#pragma unroll
    for (int st = 0; st < 4; ++st)
#pragma unroll
      for (int i = 0; i < 4; ++i) q[st][i] = pk(__uint_as_float(q[st][i] << 16) * C2, __uint_as_float(q[st][i] & 0xFFFF0000u) * C2);
    float m = -3.0e38f;
    f32x4 c0 = {0.f, 0.f, 0.f, 0.f};
    unsigned force = GE2;
    unsigned kb = 2 * TILE_BYTES;
    unsigned kx[4];
#pragma unroll
    for (int st = 0; st < 4; ++st) kx[st] = (ka0 + kb) ^ (st << 6);
#pragma unroll
    for (int sub = 0; sub < 4; ++sub)
#pragma unroll
      for (int st = 0; st < 4; ++st) asm volatile("" : "+v"(kf[sub][st]));
    unsigned epoch;
    int got = 0;
    if (live) BARRIER();
#pragma unroll 1
    for (int left = live ? n : 1; left != 0; --left) {
      f32x4 s[4];
      float e[4][4];
      u32x4 p[2];
      const unsigned va = va0 + (left == 1 ? kb : (kb + 3 * TILE_BYTES) & (RING_BYTES - 1));
      unsigned vx[8];
#define QK1(sub, st) do { s[sub] = mfma(kf[sub][st], q[st], (st) ? s[sub] : c0); kf[sub][st] = *(const LAS u32x4*)(lds + kx[st] + (sub) * 4096); } while (0)
#define EX(sub, r) e[sub][r] = __builtin_amdgcn_exp2f(s[sub][r])
#define CV(sub, h) p[(sub) >> 1][2 * ((sub) & 1) + (h)] = pk(e[sub][2 * (h)], e[sub][2 * (h) + 1])
#define VX(i) do { vx[i] = va ^ ((i) << 5); if (i) asm volatile("" : "+v"(vx[i])); } while (0)
      QK1(0, 0); VX(0); SB(); QK1(1, 0); VX(1); SB(); QK1(0, 1); VX(2); SB(); QK1(1, 1); VX(3); SB();
      QK1(0, 2); VX(4); SB(); QK1(1, 2); VX(5); SB(); QK1(0, 3); VX(6); SB(); QK1(1, 3); VX(7); SB();
      QK1(2, 0); SB(); QK1(3, 0); SB();
      QK1(2, 1); EX(0, 0); EX(0, 1); SB(); QK1(3, 1); EX(0, 2); EX(0, 3); SB();
      QK1(2, 2); EX(1, 0); EX(1, 1); SB(); QK1(3, 2); EX(1, 2); EX(1, 3); SB();
      QK1(2, 3); CV(0, 0); CV(0, 1); SB(); QK1(3, 3); CV(1, 0); CV(1, 1); SB();
      unsigned any = (p[0][0] | p[0][1] | force) | p[0][2] | p[0][3];
      asm volatile("" : "+v"(any) :: "memory");
      kb = (kb + 2 * TILE_BYTES) & (RING_BYTES - 1);
      SB();
      EX(2, 0); EX(2, 1); EX(2, 2); EX(2, 3); EX(3, 0); EX(3, 1); EX(3, 2); EX(3, 3); SB();
      CV(2, 0); CV(2, 1); CV(3, 0); CV(3, 1);
      any = (any | p[1][0] | p[1][1]) | p[1][2] | p[1][3];
      if (__builtin_expect(__builtin_amdgcn_ballot_w64((any & GE2) != 0) != 0, 0)) {
        float t = s[0][0];
#pragma unroll
        for (int sub = 0; sub < 4; ++sub)
#pragma unroll
          for (int r = 0; r < 4; ++r) t = fmaxf(t, s[sub][r]);
        const float mn = fmaxf(m, xmax4(t) - c0[0] + SLACK);
        const float alpha = __builtin_amdgcn_exp2f(m - mn), sub_ = c0[0] + mn;
        m = mn;
        const bool fold = fabsf(mn) <= 512.f;
        const float nm = fold ? -mn : 0.f;
        c0 = f32x4{nm, nm, nm, nm};
        force = fold ? 0u : GE2;
#pragma unroll
        for (int db = 0; db < 8; ++db) o[db] *= alpha;
        lsum *= alpha;
#pragma unroll
        for (int sub = 0; sub < 4; ++sub) {
          p[sub >> 1][2 * (sub & 1)] = pk(__builtin_amdgcn_exp2f(s[sub][0] - sub_), __builtin_amdgcn_exp2f(s[sub][1] - sub_));
          p[sub >> 1][2 * (sub & 1) + 1] = pk(__builtin_amdgcn_exp2f(s[sub][2] - sub_), __builtin_amdgcn_exp2f(s[sub][3] - sub_));
        }
      }
      if (__builtin_expect(left <= PK1 + 1, 0)) {
        if (left == PK1 + 1) {
          asm volatile("s_load_dword %0, %1, %2" : "=&s"(epoch) : "s"(wp), "n"(EPOCH_OFF) : "memory");
          if (!role) FLAG_LOAD();
        }
        if (left == PK2 + 1 && !role) {
          wait_vm<0>();
          asm volatile("" : "+s"(epoch));
          if (FLAG_SET()) { PC_LOAD_ALL(); got = 1; if (EARLY_BUMP) BUMP(); } else FLAG_LOAD();
        }
      }
      BARRIER_IF(live);
      u32x2 vf[16][2];
#define VRD(i) do { vf[i][0] = trd(lds + vx[(i) & 7] + ((i) >> 3) * 8192); vf[i][1] = trd(lds + vx[(i) & 7] + ((i) >> 3) * 8192 + 4096); } while (0)
#define PV1(i) o[(i) & 7] = mfma(u32x4{vf[i][0][0], vf[i][0][1], vf[i][1][0], vf[i][1][1]}, p[(i) >> 3], o[(i) & 7])
      VRD(0); VRD(1); VRD(2); VRD(3); VRD(4); VRD(5); SB();
      lsum = mfma(ones, p[0], lsum);
      lsum = mfma(ones, p[1], lsum);
#pragma unroll
      for (int st = 0; st < 4; ++st) { kx[st] = (ka0 + kb) ^ (st << 6); if (st) asm volatile("" : "+v"(kx[st])); }
      SB();
      VRD(6); SB(); PV1(0); PV1(1); VRD(7); VRD(8); SB(); PV1(2); PV1(3); VRD(9); VRD(10); SB();
      PV1(4); PV1(5); VRD(11); VRD(12); SB(); PV1(6); PV1(7); VRD(13); VRD(14); SB();
      PV1(8); PV1(9); VRD(15); SB(); PV1(10); PV1(11); PV1(12); PV1(13); PV1(14); PV1(15); SB();
    }
    asm volatile("" : "+s"(epoch));
    RSTAMP(52);
    if (role) {
      const unsigned tag = epoch + TAG_STEP;
      const float inv = __builtin_amdgcn_rcpf(lsum[0]);
#pragma unroll
      for (int i = 0; i < 4; ++i) {
        const f32x4 x = o[2 * i] * inv, y = o[2 * i + 1] * inv;
        __builtin_amdgcn_raw_buffer_store_b128(u32x4{pk(x[0], x[1]), pk(x[2], x[3]), pk(y[0], y[1]), pk(y[2], y[3])}, rp, lane * 16 + i * 1024, 0, 16);
      }
      if (live) asm volatile("s_waitcnt vmcnt(0)" ::: "memory");
      __builtin_amdgcn_raw_buffer_store_b128(u32x4{__float_as_uint(m), tag, __float_as_uint(lsum[0]), tag}, rp, g == 0 ? FLAG_OFF + lane * 16 : HEAD_BYTES, 0, 16);
      if (live) { DIAG_OUT(1); return; }
      continue;
    }
    int spins = 0;
    if (live) {
      if (!got) {
        wait_vm<0>();
#pragma unroll 1
        while (!FLAG_SET() && ++spins <= (1 << 20)) { FLAG_LOAD(); wait_vm<0>(); }
        if (spins > (1 << 20)) lsum[0] = __uint_as_float(0x7FC00000u);
        PC_LOAD_ALL();
        if (EARLY_BUMP) BUMP();
      }
      wait_vm<EARLY_BUMP>();
      RSTAMP(53);
    }
    {
      const float m2 = __uint_as_float(areg<0>()), l2 = __uint_as_float(areg<2>());
      const float mm = fmaxf(m, m2);
      const float b1 = __builtin_amdgcn_exp2f(m - mm), b2 = __builtin_amdgcn_exp2f(m2 - mm);
      const float inv = __builtin_amdgcn_rcpf(fmaf(b1, lsum[0], b2 * l2));
      LAS char* xo = lds + RING_BYTES + head * (16 * 272);
      const float w1 = b1 * inv, w2 = b2 * l2 * inv;
#define OUT1(i) do { const f32x4 y = {areg_lo<4 + 2 * (i)>(), areg_hi<4 + 2 * (i)>(), areg_lo<5 + 2 * (i)>(), areg_hi<5 + 2 * (i)>()}; \
        const f32x4 r = o[i] * w1 + y * w2; \
        *(LAS u32x2*)(xo + c * 272 + g * 8 + (i) * 32) = u32x2{pk(r[0], r[1]), pk(r[2], r[3])}; } while (0)
      OUT1(0); OUT1(1); OUT1(2); OUT1(3); OUT1(4); OUT1(5); OUT1(6); OUT1(7);
      asm volatile("" ::: "memory");
      u32x4 rows[4];
#pragma unroll
      for (int i = 0; i < 4; ++i) rows[i] = *(LAS u32x4*)(xo + (4 * i + g) * 272 + c * 16);
#pragma unroll
      for (int i = 0; i < 4; ++i) __builtin_amdgcn_raw_buffer_store_b128(rows[i], ro, lane * 16 + i * 1024, 0, OUT_AUX);
      if (!EARLY_BUMP) BUMP();
    }
    if (live) { DIAG_OUT(got ? 2 : spins ? 5 : 3); return; }
  }
  }
  {
    unit(rk, off(0), 0); unit(rq, wo, 1); unit(rk, off(1), 2); unit(rv, off(0), 5);
#define PACE_WAIT()
    unsigned t0, t1;
    asm volatile("s_load_dword %0, %2, 0\n\ts_load_dword %1, %3, 0" : "=&s"(t0), "=&s"(t1)
                 : "s"(ws + WS_OFF + ((size_t)pair * 4 + wave) * HEAD_BYTES + EPOCH_OFF), "s"((TOUCH & 2) ? (char*)O + ((size_t)pair * 4 + wave) * (SQ * 256) : (char*)Q) : "memory");
    wait_vm<12>(); unit(rk, off(2), 4);
    wait_vm<12>(); unit(rv, off(1), 7); LBARRIER();
    wait_vm<12>(); unit(rk, off(3), 6); LBARRIER();
#pragma unroll 1
    for (int s = 2; s < n - 1; ++s) {
      const bool tail = s == n - 2;
      wait_vm<12>(); if (s < N1 - 2) PACE_WAIT(); unit(rv, off(s), 2 * s + 5);
      wait_vm<12>(); if (s < N1 - 2) PACE_WAIT(); unit(tail ? rv : rk, off(tail ? s + 1 : s + 2), 2 * s + 4); LBARRIER();
    }
    wait_vm<8>(); LBARRIER();
    wait_vm<4>(); LBARRIER();
    wait_vm<0>(); LBARRIER();
    asm volatile("s_waitcnt lgkmcnt(0)" :: "s"(t0), "s"(t1) : "memory");
    if (blockIdx.x == 0 && wave == 0) {
      unsigned* tsw = (unsigned*)(ws + (32u << 20));
      if (!(EXP & 1) && (EXP & 2)) { if (lane == 0) tsw[0] = ptop; }
      if (!(EXP & 2)) {
        const unsigned ref = (EXP & 1) ? __builtin_amdgcn_readfirstlane(__builtin_nontemporal_load(tsw)) : ptop;
        while ((int)((unsigned)__builtin_amdgcn_s_memrealtime() - (ref + 6000)) < 0) __builtin_amdgcn_s_sleep(1);
      }
    }
    return;
  }

}

__global__ void tiny_a(unsigned char* ws) { const unsigned t = (unsigned)__builtin_amdgcn_s_memrealtime(); if (threadIdx.x == 0) *(unsigned*)(ws + (32u << 20)) = t; }
__global__ void tiny_b(unsigned char* ws) { const unsigned ref = __builtin_amdgcn_readfirstlane(*(volatile unsigned*)(ws + (32u << 20)));
  while ((int)((unsigned)__builtin_amdgcn_s_memrealtime() - (ref + 6000)) < 0) __builtin_amdgcn_s_sleep(1); }
}

extern "C" void kernel_launch(void* const* d_in, const int* in_sizes, int n_in, void* d_out, int out_size, void* d_ws, size_t ws_size,
                              hipStream_t stream) {
  if (EXP & 1) tiny_a<<<1, 64, 0, stream>>>((unsigned char*)d_ws);
  gqa_attn<<<2 * NPAIR, NTHREADS, 0, stream>>>((const __hip_bfloat16*)d_in[0], (const __hip_bfloat16*)d_in[1], (const __hip_bfloat16*)d_in[2],
                                                (__hip_bfloat16*)d_out, (unsigned char*)d_ws);
  if (EXP & 2) tiny_b<<<1, 64, 0, stream>>>((unsigned char*)d_ws);
}
```

```cpp
#define PACE 116
#define PACE1 98
#define PACE0 400
#define N1 15
#include <hip/hip_runtime.h>
#include <hip/hip_bf16.h>
#include <stdint.h>

#define KV_POL "nt"
#define OUT_AUX 18
#define PK1 4
#define PSTEP 2
#define KV_BYTES (SK * 256)
#define MAP 0
#define WS_OFF 0
#define WIN 4
#define WSW 2
#define WSW1 2
#define NOCOMP 0
#define NOX 0
#define PREWARM 0
#define EARLYBAR 0
#define NLOAD 4
#define BARFIRST 0
#define PBASE 1
#define PEND 2
#define FPOS 1
#define TOUCH_AT 0
#define TOUCH_O
#define PST_AUX 16
#define PCNT 4
#define PLD_POL ""

namespace {

constexpr int B_ = 16, HKV = 8, SQ = 16, SK = 2048;
constexpr int TK = 64, NTILE = SK / TK;
constexpr int NPAIR = B_ * HKV;
constexpr int TILE_BYTES = TK * 256;
constexpr int NS = 8;
constexpr int RING_BYTES = NS * TILE_BYTES;
constexpr int XCH_BYTES = 4096 + 1024;
constexpr int LDS_BYTES = RING_BYTES + 4 * XCH_BYTES;
constexpr int NTHREADS = 512;
constexpr float C2 = 0.08838834764831845f * 1.4426950408889634f;
constexpr float SLACK = 7.0f;
constexpr unsigned GE2 = 0x40004000u;
constexpr int FLAG_OFF = 4096, EPOCH_OFF = FLAG_OFF + 256;
constexpr int HEAD_BYTES = EPOCH_OFF + 256;
constexpr unsigned TAG_STEP = 0x9E3779B1u;

typedef __attribute__((ext_vector_type(8))) __bf16 bf16x8;
typedef __attribute__((ext_vector_type(4))) __bf16 bf16x4;
typedef __attribute__((ext_vector_type(2))) __bf16 bf16x2;
typedef __attribute__((ext_vector_type(4))) float f32x4;
typedef __attribute__((ext_vector_type(2))) float f32x2;
typedef __attribute__((ext_vector_type(4))) unsigned u32x4;
typedef __attribute__((ext_vector_type(2))) unsigned u32x2;
#define LAS __attribute__((address_space(3)))

#define BARRIER() asm volatile("s_waitcnt lgkmcnt(0)\n\ts_barrier" ::: "memory")
#define LBARRIER() asm volatile("s_barrier" ::: "memory")
#define DIAG_OUT(code)
#define RSTAMP(i)
#define SB() __builtin_amdgcn_sched_barrier(0)

template <int N> __device__ __forceinline__ void wait_vm() { asm volatile("s_waitcnt vmcnt(%0)" ::"n"(N) : "memory"); }

__device__ __forceinline__ float xmax4(float x) {
  auto s = __builtin_amdgcn_permlane16_swap(__float_as_uint(x), __float_as_uint(x), false, false);
  x = fmaxf(__uint_as_float(s[0]), __uint_as_float(s[1]));
  auto t = __builtin_amdgcn_permlane32_swap(__float_as_uint(x), __float_as_uint(x), false, false);
  return fmaxf(__uint_as_float(t[0]), __uint_as_float(t[1]));
}
__device__ __forceinline__ unsigned pk(float a, float b) { f32x2 v = {a, b}; return __builtin_bit_cast(unsigned, __builtin_convertvector(v, bf16x2)); }
__device__ __forceinline__ f32x4 mfma(u32x4 a, u32x4 b, f32x4 c) {
  return __builtin_amdgcn_mfma_f32_16x16x32_bf16(__builtin_bit_cast(bf16x8, a), __builtin_bit_cast(bf16x8, b), c, 0, 0, 0);
}
__device__ __forceinline__ u32x2 trd(LAS char* p) { return __builtin_bit_cast(u32x2, __builtin_amdgcn_ds_read_tr16_b64_v4bf16((LAS bf16x4*)p)); }

__global__ void __launch_bounds__(NTHREADS, 2)
gqa_attn(const __hip_bfloat16* __restrict__ Q, const __hip_bfloat16* __restrict__ K, const __hip_bfloat16* __restrict__ V,
         __hip_bfloat16* __restrict__ O, unsigned char* __restrict__ ws) {
  __shared__ __attribute__((aligned(1024))) char smem[LDS_BYTES];
  LAS char* lds = (LAS char*)smem;
  const unsigned tstart = (unsigned)__builtin_amdgcn_s_memrealtime();
  const int lane = threadIdx.x & 63;
  const int wave = __builtin_amdgcn_readfirstlane(threadIdx.x >> 6);
  const int pair = blockIdx.x & (NPAIR - 1), role = blockIdx.x >> 7;
  const int n = role ? N1 : NTILE - N1;
  const int g = lane >> 4, c = lane & 15;

  if (__builtin_expect(wave >= NLOAD, 0)) {
    const int head = wave - NLOAD;
    const unsigned char* wp = ws + WS_OFF + ((size_t)pair * 4 + head) * HEAD_BYTES;
    const __amdgpu_buffer_rsrc_t rp = __builtin_amdgcn_make_buffer_rsrc((void*)wp, 0, HEAD_BYTES, 0x00020000);
    const __amdgpu_buffer_rsrc_t ro = __builtin_amdgcn_make_buffer_rsrc((char*)O + ((size_t)pair * 4 + head) * (SQ * 256), 0, SQ * 256, 0x00020000);
    const int po = lane * 16;
    const unsigned ka0 = c * 256 + ((g ^ ((c & 7) << 1)) << 4);
    const int vr = 4 * g + (c >> 2);
    const unsigned va0 = vr * 256 + ((vr & 7) << 5) + (c & 3) * 8;
    unsigned xi = RING_BYTES + head * XCH_BYTES + po, xw = RING_BYTES + head * XCH_BYTES + c * 272 + g * 8, xr = RING_BYTES + head * XCH_BYTES + g * 272 + c * 16;
    u32x4 ones = {0x3F803F80u, 0x3F803F80u, 0x3F803F80u, 0x3F803F80u};
    asm volatile("" : "+v"(ones), "+v"(xi), "+v"(xw), "+v"(xr));
    f32x4 o[8], lsum = {0.f, 0.f, 0.f, 0.f};
#pragma unroll
    for (int db = 0; db < 8; ++db) { o[db] = f32x4{0.f, 0.f, 0.f, 0.f}; asm volatile("" : "+v"(o[db])); }
    float nm = 3.0e38f;
    unsigned kb = 2 * TILE_BYTES;
    unsigned kx[4];
#pragma unroll
    for (int st = 0; st < 4; ++st) kx[st] = (ka0 + kb) ^ (st << 6);
    unsigned epoch;
    asm volatile("s_load_dword %0, %1, %2\n\ts_waitcnt lgkmcnt(0)" : "=&s"(epoch) : "s"(wp), "n"(EPOCH_OFF) : "memory");
    BARRIER();
    u32x4 q[4], kf[4][4];
#pragma unroll
    for (int st = 0; st < 4; ++st) q[st] = *(const LAS u32x4*)(lds + (TILE_BYTES + head * 4096) + (ka0 ^ (st << 6)));
#pragma unroll
    for (int sub = 0; sub < 4; ++sub)
#pragma unroll
      for (int st = 0; st < 4; ++st) kf[sub][st] = *(const LAS u32x4*)(lds + (ka0 ^ (st << 6)) + sub * 4096);
    asm volatile("" : "+v"(kf[0][0]), "+v"(kf[0][1]), "+v"(kf[0][2]), "+v"(kf[0][3]), "+v"(kf[1][0]), "+v"(kf[1][1]), "+v"(kf[1][2]), "+v"(kf[1][3]),
                 "+v"(kf[2][0]), "+v"(kf[2][1]), "+v"(kf[2][2]), "+v"(kf[2][3]), "+v"(kf[3][0]), "+v"(kf[3][1]), "+v"(kf[3][2]), "+v"(kf[3][3]),
                 "+v"(q[0]), "+v"(q[1]), "+v"(q[2]), "+v"(q[3]));
    BARRIER();
#pragma unroll 1
    for (int left = n; left != 0; --left) {
      f32x4 s[4], x[4];
      u32x4 p[2];
      u32x2 vf[16][2];
      const f32x4 zero = {0.f, 0.f, 0.f, 0.f};
      const unsigned va = va0 + (left == 1 ? kb : (kb + 3 * TILE_BYTES) & (RING_BYTES - 1));
      unsigned vx[8];
#define QK1(sub, st) do { s[sub] = mfma(kf[sub][st], q[st], (st) ? s[sub] : zero); kf[sub][st] = *(const LAS u32x4*)(lds + kx[st] + (sub) * 4096); } while (0)
#define FM(sub) x[sub] = s[sub] * C2 + nm
#define EX(sub, r) x[sub][r] = __builtin_amdgcn_exp2f(x[sub][r])
#define CV(sub, h) p[(sub) >> 1][2 * ((sub) & 1) + (h)] = pk(x[sub][2 * (h)], x[sub][2 * (h) + 1])
#define VX(i) do { vx[i] = va ^ ((i) << 5); if (i) asm volatile("" : "+v"(vx[i])); } while (0)
#define VRD(i) do { vf[i][0] = trd(lds + vx[(i) & 7] + ((i) >> 3) * 8192); vf[i][1] = trd(lds + vx[(i) & 7] + ((i) >> 3) * 8192 + 4096); } while (0)
#define PV1(i) o[(i) & 7] = mfma(u32x4{vf[i][0][0], vf[i][0][1], vf[i][1][0], vf[i][1][1]}, p[(i) >> 3], o[(i) & 7])
      QK1(0, 0); VX(0); SB(); QK1(1, 0); VX(1); SB(); QK1(0, 1); VX(2); SB(); QK1(1, 1); VX(3); SB();
      QK1(0, 2); VX(4); SB(); QK1(1, 2); VX(5); SB(); QK1(0, 3); VX(6); SB(); QK1(1, 3); VX(7); SB();
      QK1(2, 0); SB(); QK1(3, 0); FM(0); SB();
      QK1(2, 1); FM(1); EX(0, 0); SB(); QK1(3, 1); EX(0, 1); EX(0, 2); SB();
      QK1(2, 2); EX(0, 3); EX(1, 0); SB(); QK1(3, 2); EX(1, 1); EX(1, 2); SB();
      QK1(2, 3); EX(1, 3); CV(0, 0); SB(); QK1(3, 3); CV(0, 1); CV(1, 0); CV(1, 1); SB();
      unsigned any = (p[0][0] | p[0][1] | p[0][2]) | p[0][3];
      asm volatile("" : "+v"(any) :: "memory");
      kb = (kb + 2 * TILE_BYTES) & (RING_BYTES - 1);
      SB();
      FM(2); FM(3); SB();
      EX(2, 0); EX(2, 1); EX(2, 2); EX(2, 3); EX(3, 0); EX(3, 1); EX(3, 2); EX(3, 3); SB();
      CV(2, 0); CV(2, 1); CV(3, 0); CV(3, 1);
      any = (any | p[1][0] | p[1][1]) | p[1][2] | p[1][3];
      if (__builtin_expect(__builtin_amdgcn_ballot_w64((any & GE2) != 0) != 0, 0)) {
        float t = s[0][0];
#pragma unroll
        for (int sub = 0; sub < 4; ++sub)
#pragma unroll
          for (int r = 0; r < 4; ++r) t = fmaxf(t, s[sub][r]);
        const float nn = fminf(nm, -fmaf(xmax4(t), C2, SLACK));
        const float alpha = __builtin_amdgcn_exp2f(nn - nm);
        nm = nn;
#pragma unroll
        for (int db = 0; db < 8; ++db) o[db] *= alpha;
        lsum *= alpha;
#pragma unroll
        for (int sub = 0; sub < 4; ++sub) { FM(sub); EX(sub, 0); EX(sub, 1); EX(sub, 2); EX(sub, 3); CV(sub, 0); CV(sub, 1); }
      }
      BARRIER();
      VRD(0); VRD(1); VRD(2); VRD(3); VRD(4); VRD(5); SB();
      lsum = mfma(ones, p[0], lsum);
      lsum = mfma(ones, p[1], lsum);
#pragma unroll
      for (int st = 0; st < 4; ++st) { kx[st] = (ka0 + kb) ^ (st << 6); if (st) asm volatile("" : "+v"(kx[st])); }
      SB();
      VRD(6); SB(); PV1(0); PV1(1); VRD(7); VRD(8); SB(); PV1(2); PV1(3); VRD(9); VRD(10); SB();
      PV1(4); PV1(5); VRD(11); VRD(12); SB(); PV1(6); PV1(7); VRD(13); VRD(14); SB();
      PV1(8); PV1(9); VRD(15); SB(); PV1(10); PV1(11); PV1(12); PV1(13); PV1(14); PV1(15); SB();
    }
    RSTAMP(52);
    if (role) {
      const unsigned tag = epoch + TAG_STEP;
      const float inv = __builtin_amdgcn_rcpf(lsum[0]);
      u32x4 y[4];
#pragma unroll
      for (int i = 0; i < 4; ++i) {
        const f32x4 a = o[2 * i] * inv, b = o[2 * i + 1] * inv;
        y[i] = u32x4{pk(a[0], a[1]), pk(a[2], a[3]), pk(b[0], b[1]), pk(b[2], b[3])};
        if (i < PCNT) __builtin_amdgcn_raw_buffer_store_b128(y[i], rp, po + i * 1024, 0, PST_AUX);
      }
      asm volatile("s_waitcnt vmcnt(0)" :: "v"(y[0]), "v"(y[1]), "v"(y[2]), "v"(y[3]) : "memory");
      __builtin_amdgcn_raw_buffer_store_b128(u32x4{__float_as_uint(nm), tag, __float_as_uint(lsum[0]), tag}, rp, g == 0 ? FLAG_OFF + po : HEAD_BYTES, 0, 16);
      DIAG_OUT(1);
      return;
    }
    if (!NOX) BARRIER();
    RSTAMP(53);
    {
      const u32x4 st = *(LAS u32x4*)(lds + xi + 4096);
      u32x4 y[4];
#pragma unroll
      for (int i = 0; i < 4; ++i) y[i] = *(LAS u32x4*)(lds + xi + i * 1024);
      SB();
      const float nm2 = __uint_as_float(st[0]), l2 = __uint_as_float(st[2]);
      const float nn = fminf(nm, nm2);
      const float b1 = __builtin_amdgcn_exp2f(nn - nm), b2 = __builtin_amdgcn_exp2f(nn - nm2) * l2;
      const float inv = __builtin_amdgcn_rcpf(fmaf(b1, lsum[0], b2));
      const float w1 = b1 * inv, w2 = b2 * inv;
#pragma unroll
      for (int i = 0; i < 8; ++i) {
        const unsigned y0 = y[i >> 1][2 * (i & 1)], y1 = y[i >> 1][2 * (i & 1) + 1];
        const f32x4 u = {__uint_as_float(y0 << 16), __uint_as_float(y0 & 0xFFFF0000u), __uint_as_float(y1 << 16), __uint_as_float(y1 & 0xFFFF0000u)};
        const f32x4 r = o[i] * w1 + u * w2;
        *(LAS u32x2*)(lds + xw + i * 32) = u32x2{pk(r[0], r[1]), pk(r[2], r[3])};
      }
      asm volatile("" ::: "memory");
      u32x4 rows[4];
#pragma unroll
      for (int i = 0; i < 4; ++i) rows[i] = *(LAS u32x4*)(lds + xr + i * (4 * 272));
#pragma unroll
      for (int i = 0; i < 4; ++i) __builtin_amdgcn_raw_buffer_store_b128(rows[i], ro, po + i * 1024, 0, OUT_AUX);
    }
    DIAG_OUT(4);
    return;
  }

  const int vo0 = g * 256 + ((c ^ (g << 1)) << 4), vo1 = vo0 ^ 128;
  const __amdgpu_buffer_rsrc_t rk = __builtin_amdgcn_make_buffer_rsrc((char*)K + (size_t)pair * (SK * 256), 0, KV_BYTES, 0x00020000);
  const __amdgpu_buffer_rsrc_t rv = __builtin_amdgcn_make_buffer_rsrc((char*)V + (size_t)pair * (SK * 256), 0, KV_BYTES, 0x00020000);
  const __amdgpu_buffer_rsrc_t rq = __builtin_amdgcn_make_buffer_rsrc((char*)Q + (size_t)pair * TILE_BYTES, 0, TILE_BYTES, 0x00020000);
  const int wo = wave * 4096;
  auto unit = [&](const __amdgpu_buffer_rsrc_t r, int so, unsigned slot) {
    const unsigned dst = (unsigned)(uintptr_t)lds + (slot & (NS - 1)) * TILE_BYTES + wo;
    asm volatile("s_mov_b32 m0, %0\n\ts_nop 0\n\t"
                 "buffer_load_dwordx4 %1, %3, %4 offen " KV_POL " lds\n\t"
                 "buffer_load_dwordx4 %2, %3, %4 offen offset:1024 " KV_POL " lds\n\t"
                 "buffer_load_dwordx4 %1, %3, %4 offen offset:2048 " KV_POL " lds\n\t"
                 "buffer_load_dwordx4 %2, %3, %4 offen offset:3072 " KV_POL " lds"
                 :: "s"(dst), "v"(vo0), "v"(vo1), "s"(r), "s"(so) : "memory");
  };
  auto off = [&](int t) { return (t < N1 ? 2 * t + role : N1 + t) * TILE_BYTES + wo; };
#define UK(t) unit(rk, off(t), 2 * (t))
#define UV(t) unit(rv, off(t), 2 * (t) + 5)
  UK(0); unit(rq, wo, 1); UK(1); UV(0);
  const unsigned char* wp = ws + WS_OFF + ((size_t)pair * 4 + wave) * HEAD_BYTES;
  unsigned epoch;
  asm volatile("s_load_dword %0, %1, %2\n\ts_waitcnt lgkmcnt(0)" : "=&s"(epoch) : "s"(wp), "n"(EPOCH_OFF) : "memory");
  int s = 2;
  unsigned due = tstart + PACE0;
  const unsigned pstep = (role ? PACE1 : PACE) / 2;
#define PACE_WAIT() do { while ((int)((unsigned)__builtin_amdgcn_s_memrealtime() - due) < 0) __builtin_amdgcn_s_sleep(1); due += pstep; } while (0)
  wait_vm<12>(); UK(2);
  wait_vm<12>(); UV(1); LBARRIER();
  wait_vm<12>(); UK(3); LBARRIER();
#pragma unroll 1
  for (; s < n - 1; ++s) {
    const bool tail = s == n - 2;
    wait_vm<12>(); if (s < N1 - PEND) PACE_WAIT(); UV(s);
    wait_vm<12>(); if (s < N1 - PEND) PACE_WAIT(); unit(tail ? rv : rk, off(tail ? s + 1 : s + 2), 2 * s + 4); LBARRIER();
  }
  if (role || NOX || NOCOMP) {
    wait_vm<8>(); LBARRIER();
    wait_vm<4>(); LBARRIER();
    wait_vm<0>(); LBARRIER();
    DIAG_OUT(0);
    return;
  }
  const __amdgpu_buffer_rsrc_t rp = __builtin_amdgcn_make_buffer_rsrc((void*)wp, 0, HEAD_BYTES, 0x00020000);
  int spins = 1 << 20;
  unsigned long long tmp64;
#define FLAG_LOAD "buffer_load_dwordx4 v[32:35], %[fo], %[rp], 0 offen sc1\n\t"
#define FLAG_TEST "v_cmp_ne_u32 vcc, %[tag], v33\n\tv_cmp_ne_u32 %[t], %[tag], v35\n\ts_nop 3\n\ts_or_b64 vcc, vcc, %[t]\n\t"
#define PART_LOAD "buffer_load_dwordx4 v[16:19], %[po], %[rp], 0 offen sc1\n\t" \
                  "buffer_load_dwordx4 v[20:23], %[po], %[rp], 0 offen offset:1024 sc1\n\t" \
                  "buffer_load_dwordx4 v[24:27], %[po], %[rp], 0 offen offset:2048 sc1\n\t" \
                  "buffer_load_dwordx4 v[28:31], %[po], %[rp], 0 offen offset:3072 sc1\n\t"
  asm volatile(
      "s_waitcnt vmcnt(8)\n\ts_barrier\n\t"
      FLAG_LOAD
      "s_waitcnt vmcnt(5)\n\ts_barrier\n\t"
      "s_waitcnt vmcnt(0)\n\t"
      FLAG_TEST
      "s_cbranch_vccnz 1f\n\t"
      PART_LOAD
      "s_barrier\n"
      "3:\n\t"
      "ds_write_b128 %[xi], v[32:35] offset:4096\n\t"
      "s_waitcnt vmcnt(0)\n\t"
      "ds_write_b128 %[xi], v[16:19]\n\t"
      "ds_write_b128 %[xi], v[20:23] offset:1024\n\t"
      "ds_write_b128 %[xi], v[24:27] offset:2048\n\t"
      "ds_write_b128 %[xi], v[28:31] offset:3072\n\t"
      "s_waitcnt lgkmcnt(0)\n\ts_barrier\n\t"
      "buffer_store_dword v33, %[eo], %[rp], 0 offen sc1\n\t"
      "s_branch 4f\n"
      "1:\n\t"
      FLAG_LOAD
      "s_barrier\n"
      "2:\n\t"
      "s_waitcnt vmcnt(0)\n\t"
      FLAG_TEST
      "s_cbranch_vccz 5f\n\t"
      FLAG_LOAD
      "s_sub_u32 %[spins], %[spins], 1\n\t"
      "s_cbranch_scc0 2b\n\t"
      "s_waitcnt vmcnt(0)\n\t"
      "v_mov_b32 v34, 0x7fc00000\n"
      "5:\n\t"
      "s_sub_u32 %[spins], %[spins], 1\n\t"
      PART_LOAD
      "s_branch 3b\n"
      "4:"
      : [spins] "+s"(spins), [t] "=&s"(tmp64)
      : [tag] "s"(epoch + TAG_STEP), [fo] "v"(FLAG_OFF + c * 16), [po] "v"(lane * 16), [eo] "v"(lane == 0 ? EPOCH_OFF : HEAD_BYTES),
        [xi] "v"((unsigned)(uintptr_t)lds + RING_BYTES + wave * XCH_BYTES + lane * 16), [rp] "s"(rp)
      : "memory", "vcc", "scc", "v16", "v17", "v18", "v19", "v20", "v21", "v22", "v23", "v24", "v25", "v26", "v27", "v28", "v29", "v30", "v31", "v32", "v33", "v34", "v35");
  DIAG_OUT(spins == (1 << 20) ? 2 : spins == (1 << 20) - 1 ? 3 : 5);
}

}

extern "C" void kernel_launch(void* const* d_in, const int* in_sizes, int n_in, void* d_out, int out_size, void* d_ws, size_t ws_size,
                              hipStream_t stream) {
  gqa_attn<<<2 * NPAIR, NTHREADS, 0, stream>>>((const __hip_bfloat16*)d_in[0], (const __hip_bfloat16*)d_in[1], (const __hip_bfloat16*)d_in[2],
                                                (__hip_bfloat16*)d_out, (unsigned char*)d_ws);
}
```

```cpp
#define EARLYBAR 1
#include <hip/hip_runtime.h>
#include <hip/hip_bf16.h>
#include <stdint.h>

#define K_POL "nt"
#define V_POL "nt"
#define OUT_AUX 18
#define KV_BYTES (SK * 256)
#define PACE 0
#define PACE0 200
#define XD 2
#define NOCOMP 0
#define BARFIRST 0
#define PRIO 0
#define NLOAD 4

namespace {

constexpr int B_ = 16, HKV = 8, SQ = 16, SK = 2048;
constexpr int TK = 64, NT = SK / TK;
constexpr int NPAIR = B_ * HKV;
constexpr int KT_BYTES = TK * 256, VT_BYTES = TK * 128;
constexpr int NS = XD + 2;
constexpr int SLOT_BYTES = KT_BYTES + VT_BYTES;
constexpr int RING_BYTES = NS * SLOT_BYTES;
constexpr int Q_OFF = RING_BYTES;
constexpr int LDS_BYTES = RING_BYTES + KT_BYTES;
constexpr int NTHREADS = 512;
constexpr float C2 = 0.08838834764831845f * 1.4426950408889634f;
constexpr float SLACK = 7.0f;
constexpr unsigned GE2 = 0x40004000u;
static_assert(LDS_BYTES <= 163840 && LDS_BYTES > 81920, "one workgroup per CU");

typedef __attribute__((ext_vector_type(8))) __bf16 bf16x8;
typedef __attribute__((ext_vector_type(4))) __bf16 bf16x4;
typedef __attribute__((ext_vector_type(2))) __bf16 bf16x2;
typedef __attribute__((ext_vector_type(4))) float f32x4;
typedef __attribute__((ext_vector_type(2))) float f32x2;
typedef __attribute__((ext_vector_type(4))) unsigned u32x4;
typedef __attribute__((ext_vector_type(2))) unsigned u32x2;
#define LAS __attribute__((address_space(3)))

#define BARRIER() asm volatile("s_waitcnt lgkmcnt(0)\n\ts_barrier" ::: "memory")
#define LBARRIER() asm volatile("s_barrier" ::: "memory")
#define DIAG_OUT(code)
#define RSTAMP(i)
#define SB() __builtin_amdgcn_sched_barrier(0)

template <int N> __device__ __forceinline__ void wait_vm() { asm volatile("s_waitcnt vmcnt(%0)" ::"n"(N) : "memory"); }

__device__ __forceinline__ float xmax4(float x) {
  auto s = __builtin_amdgcn_permlane16_swap(__float_as_uint(x), __float_as_uint(x), false, false);
  x = fmaxf(__uint_as_float(s[0]), __uint_as_float(s[1]));
  auto t = __builtin_amdgcn_permlane32_swap(__float_as_uint(x), __float_as_uint(x), false, false);
  return fmaxf(__uint_as_float(t[0]), __uint_as_float(t[1]));
}
__device__ __forceinline__ unsigned pk(float a, float b) { f32x2 v = {a, b}; return __builtin_bit_cast(unsigned, __builtin_convertvector(v, bf16x2)); }
__device__ __forceinline__ f32x4 mfma(u32x4 a, u32x4 b, f32x4 c) {
  return __builtin_amdgcn_mfma_f32_16x16x32_bf16(__builtin_bit_cast(bf16x8, a), __builtin_bit_cast(bf16x8, b), c, 0, 0, 0);
}
__device__ __forceinline__ u32x2 trd(LAS char* p) { return __builtin_bit_cast(u32x2, __builtin_amdgcn_ds_read_tr16_b64_v4bf16((LAS bf16x4*)p)); }
__device__ __forceinline__ unsigned ring_next(unsigned x) { x += SLOT_BYTES; return x == (unsigned)RING_BYTES ? 0u : x; }

__global__ void __launch_bounds__(NTHREADS, 2)
gqa_attn(const __hip_bfloat16* __restrict__ Q, const __hip_bfloat16* __restrict__ K, const __hip_bfloat16* __restrict__ V,
         __hip_bfloat16* __restrict__ O
         ) {
  __shared__ __attribute__((aligned(1024))) char smem[LDS_BYTES];
  LAS char* lds = (LAS char*)smem;
  const int lane = threadIdx.x & 63;
  const int wave = __builtin_amdgcn_readfirstlane(threadIdx.x >> 6);
  const int pair = blockIdx.x & (NPAIR - 1), half = blockIdx.x >> 7;
  const int g = lane >> 4, c = lane & 15;

  if (__builtin_expect(wave >= NLOAD, 0)) {
    const int head = wave - NLOAD;
    if (PRIO) __builtin_amdgcn_s_setprio(PRIO);
    const __amdgpu_buffer_rsrc_t ro = __builtin_amdgcn_make_buffer_rsrc((char*)O + ((size_t)pair * 4 + head) * (SQ * 256), 0, SQ * 256, 0x00020000);
    const unsigned ka0 = c * 256 + ((g ^ ((c & 7) << 1)) << 4);
    const int vr = 4 * g + (c >> 2);
    const unsigned va0 = KT_BYTES + vr * 128 + (((vr >> 1) & 3) << 5) + (c & 3) * 8;
    unsigned xw = Q_OFF + head * 4096 + c * 144 + g * 8, xr = Q_OFF + head * 4096 + (lane >> 3) * 144 + (lane & 7) * 16;
    int oo = (lane >> 3) * 256 + half * 128 + (lane & 7) * 16;
    u32x4 ones = {0x3F803F80u, 0x3F803F80u, 0x3F803F80u, 0x3F803F80u};
    asm volatile("" : "+v"(ones), "+v"(xw), "+v"(xr), "+v"(oo));
    f32x4 o[4], lsum = {0.f, 0.f, 0.f, 0.f};
#pragma unroll
    for (int db = 0; db < 4; ++db) { o[db] = f32x4{0.f, 0.f, 0.f, 0.f}; asm volatile("" : "+v"(o[db])); }
    float nm = 3.0e38f;
    unsigned sb = SLOT_BYTES;
    unsigned kx[4];
#pragma unroll
    for (int st = 0; st < 4; ++st) kx[st] = (ka0 ^ (st << 6)) + sb;
    BARRIER();
    u32x4 q[4], kf[4][4];
#pragma unroll
    for (int st = 0; st < 4; ++st) q[st] = *(const LAS u32x4*)(lds + (Q_OFF + head * 4096) + (ka0 ^ (st << 6)));
#pragma unroll
    for (int sub = 0; sub < 4; ++sub)
#pragma unroll
      for (int st = 0; st < 4; ++st) kf[sub][st] = *(const LAS u32x4*)(lds + (ka0 ^ (st << 6)) + sub * 4096);
    asm volatile("" : "+v"(kf[0][0]), "+v"(kf[0][1]), "+v"(kf[0][2]), "+v"(kf[0][3]), "+v"(kf[1][0]), "+v"(kf[1][1]), "+v"(kf[1][2]), "+v"(kf[1][3]),
                 "+v"(kf[2][0]), "+v"(kf[2][1]), "+v"(kf[2][2]), "+v"(kf[2][3]), "+v"(kf[3][0]), "+v"(kf[3][1]), "+v"(kf[3][2]), "+v"(kf[3][3]),
                 "+v"(q[0]), "+v"(q[1]), "+v"(q[2]), "+v"(q[3]));
    BARRIER();
#pragma unroll 1
    for (int left = NT; left != 0; --left) {
      f32x4 s[4], x[4];
      u32x4 p[2];
      u32x2 vf[8][2];
      const f32x4 zero = {0.f, 0.f, 0.f, 0.f};
      sb = ring_next(sb);
      const unsigned va = va0 + sb;
      unsigned vx[4];
#define QK1(sub, st) do { s[sub] = mfma(kf[sub][st], q[st], (st) ? s[sub] : zero); kf[sub][st] = *(const LAS u32x4*)(lds + kx[st] + (sub) * 4096); } while (0)
#define FM(sub) x[sub] = s[sub] * C2 + nm
#define EX(sub, r) x[sub][r] = __builtin_amdgcn_exp2f(x[sub][r])
#define CV(sub, h) p[(sub) >> 1][2 * ((sub) & 1) + (h)] = pk(x[sub][2 * (h)], x[sub][2 * (h) + 1])
#define VX(i) do { vx[i] = va ^ ((i) << 5); if (i) asm volatile("" : "+v"(vx[i])); } while (0)
#define VRD(i) do { vf[i][0] = trd(lds + vx[(i) & 3] + ((i) >> 2) * 4096); vf[i][1] = trd(lds + vx[(i) & 3] + ((i) >> 2) * 4096 + 2048); } while (0)
#define PV1(i) o[(i) & 3] = mfma(u32x4{vf[i][0][0], vf[i][0][1], vf[i][1][0], vf[i][1][1]}, p[(i) >> 2], o[(i) & 3])
#define KX() do { kx[0] = ka0 + sb; kx[1] = kx[0] ^ 64; kx[2] = kx[0] ^ 128; kx[3] = kx[0] ^ 192; asm volatile("" : "+v"(kx[1]), "+v"(kx[2]), "+v"(kx[3])); } while (0)
      QK1(0, 0); SB(); QK1(1, 0); VX(0); SB(); QK1(0, 1); VX(1); SB(); QK1(1, 1); VX(2); SB();
      QK1(0, 2); VX(3); SB(); QK1(1, 2); SB(); QK1(0, 3); SB(); QK1(1, 3); SB();
      QK1(2, 0); SB(); QK1(3, 0); FM(0); SB();
      QK1(2, 1); FM(1); EX(0, 0); SB(); QK1(3, 1); EX(0, 1); EX(0, 2); SB();
      QK1(2, 2); EX(0, 3); EX(1, 0); SB(); QK1(3, 2); EX(1, 1); EX(1, 2); SB();
      QK1(2, 3); EX(1, 3); CV(0, 0); SB(); QK1(3, 3); CV(0, 1); CV(1, 0); CV(1, 1); SB();
      unsigned any = (p[0][0] | p[0][1] | p[0][2]) | p[0][3];
      asm volatile("" : "+v"(any) :: "memory");
      SB();
      FM(2); FM(3); SB();
      BARRIER();
      VRD(0); VRD(1); VRD(2); VRD(3); VRD(4); VRD(5); VRD(6); VRD(7);
      asm volatile("" ::: "memory");
      SB();
      EX(2, 0); EX(2, 1); EX(2, 2); EX(2, 3); EX(3, 0); EX(3, 1); EX(3, 2); EX(3, 3); SB();
      CV(2, 0); CV(2, 1); CV(3, 0); CV(3, 1);
      any = (any | p[1][0] | p[1][1]) | p[1][2] | p[1][3];
      if (__builtin_expect(__builtin_amdgcn_ballot_w64((any & GE2) != 0) != 0, 0)) {
        float t = s[0][0];
#pragma unroll
        for (int sub = 0; sub < 4; ++sub)
#pragma unroll
          for (int r = 0; r < 4; ++r) t = fmaxf(t, s[sub][r]);
        const float nn = fminf(nm, -fmaf(xmax4(t), C2, SLACK));
        const float alpha = __builtin_amdgcn_exp2f(nn - nm);
        nm = nn;
#pragma unroll
        for (int db = 0; db < 4; ++db) o[db] *= alpha;
        lsum *= alpha;
#pragma unroll
        for (int sub = 0; sub < 4; ++sub) { FM(sub); EX(sub, 0); EX(sub, 1); EX(sub, 2); EX(sub, 3); CV(sub, 0); CV(sub, 1); }
      }
      lsum = mfma(ones, p[0], lsum); PV1(0); PV1(1); PV1(2); PV1(3); SB();
      lsum = mfma(ones, p[1], lsum); KX(); SB(); PV1(4); PV1(5); PV1(6); PV1(7); SB();
    }
    RSTAMP(52);
    {
      const float inv = __builtin_amdgcn_rcpf(lsum[0]);
#pragma unroll
      for (int i = 0; i < 4; ++i) {
        const f32x4 r = o[i] * inv;
        *(LAS u32x2*)(lds + xw + i * 32) = u32x2{pk(r[0], r[1]), pk(r[2], r[3])};
      }
      asm volatile("" ::: "memory");
      const u32x4 r0 = *(LAS u32x4*)(lds + xr), r1 = *(LAS u32x4*)(lds + xr + 8 * 144);
      __builtin_amdgcn_raw_buffer_store_b128(r0, ro, oo, 0, OUT_AUX);
      __builtin_amdgcn_raw_buffer_store_b128(r1, ro, oo + 2048, 0, OUT_AUX);
    }
    DIAG_OUT(4);
    return;
  }

  const int ko0 = g * 256 + ((c ^ (g << 1)) << 4), ko1 = ko0 ^ 128;
  const int vo = (lane >> 3) * 256 + half * 128 + (((lane & 7) ^ ((lane >> 3) & 6)) << 4) + 1024;
  const __amdgpu_buffer_rsrc_t rk = __builtin_amdgcn_make_buffer_rsrc((char*)K + (size_t)pair * (SK * 256), 0, KV_BYTES, 0x00020000);
  const __amdgpu_buffer_rsrc_t rq = __builtin_amdgcn_make_buffer_rsrc((char*)Q + (size_t)pair * KT_BYTES, 0, KT_BYTES, 0x00020000);
  const __amdgpu_buffer_rsrc_t rv = __builtin_amdgcn_make_buffer_rsrc((char*)V + (size_t)pair * (SK * 256), 0, KV_BYTES, 0x00020000);
  const unsigned lbase = (unsigned)(uintptr_t)lds;
  auto ktile = [&](const __amdgpu_buffer_rsrc_t r, int so, unsigned dst) {
    asm volatile("s_mov_b32 m0, %0\n\ts_nop 0\n\t"
                 "buffer_load_dwordx4 %1, %3, %4 offen " K_POL " lds\n\t"
                 "buffer_load_dwordx4 %2, %3, %4 offen offset:1024 " K_POL " lds\n\t"
                 "buffer_load_dwordx4 %1, %3, %4 offen offset:2048 " K_POL " lds\n\t"
                 "buffer_load_dwordx4 %2, %3, %4 offen offset:3072 " K_POL " lds"
                 :: "s"(lbase + dst + wave * 4096), "v"(ko0), "v"(ko1), "s"(r), "s"(so + wave * 4096) : "memory");
  };
  auto vtile = [&](int so, unsigned dst) {
    asm volatile("s_mov_b32 m0, %0\n\ts_nop 0\n\t"
                 "buffer_load_dwordx4 %1, %3, %4 offen " V_POL " lds\n\t"
                 "buffer_load_dwordx4 %2, %3, %4 offen offset:1024 " V_POL " lds"
                 :: "s"(lbase + dst + KT_BYTES + wave * 2048), "v"(vo - 1024), "v"(vo), "s"(rv), "s"(so + wave * 4096) : "memory");
  };
#define PACE_WAIT()
  ktile(rk, 0, 0); ktile(rq, 0, Q_OFF); ktile(rk, KT_BYTES, SLOT_BYTES);
#pragma unroll
  for (int s = 2; s <= XD; ++s) { vtile((s - 2) * KT_BYTES, s * SLOT_BYTES); ktile(rk, s * KT_BYTES, s * SLOT_BYTES); }
  wait_vm<4 + 6 * (XD - 1)>(); LBARRIER();
  vtile((XD - 1) * KT_BYTES, (XD + 1) * SLOT_BYTES); ktile(rk, (XD + 1) * KT_BYTES, (XD + 1) * SLOT_BYTES);
  wait_vm<6 * XD>(); LBARRIER();
  unsigned dst = 0;
#pragma unroll 1
  for (int s = 2; s < NT - XD; ++s) {
    wait_vm<6 * XD - 2>(); PACE_WAIT(); vtile((s + XD - 2) * KT_BYTES, dst);
    wait_vm<6 * XD - 4>(); ktile(rk, (s + XD) * KT_BYTES, dst); LBARRIER();
    dst = ring_next(dst);
  }
  vtile((NT - 2) * KT_BYTES, dst);
  wait_vm<6 * XD - 4>(); LBARRIER();
  vtile((NT - 1) * KT_BYTES, ring_next(dst));
  if (XD >= 4) { wait_vm<16>(); LBARRIER(); }
  if (XD >= 3) { wait_vm<10>(); LBARRIER(); }
  wait_vm<4>(); LBARRIER();
  wait_vm<2>(); LBARRIER();
  wait_vm<0>(); LBARRIER();
  DIAG_OUT(0);
}

}

extern "C" void kernel_launch(void* const* d_in, const int* in_sizes, int n_in, void* d_out, int out_size, void* d_ws, size_t ws_size,
                              hipStream_t stream) {
  gqa_attn<<<2 * NPAIR, NTHREADS, 0, stream>>>((const __hip_bfloat16*)d_in[0], (const __hip_bfloat16*)d_in[1], (const __hip_bfloat16*)d_in[2],
                                                (__hip_bfloat16*)d_out
                                                );
}
```

```cpp
#define PACE 114
#define PACE1 98
#define PACE0 400
#define N1 15
#include <hip/hip_runtime.h>
#include <hip/hip_bf16.h>
#include <stdint.h>

#define KV_POL "nt"
#define OUT_AUX 18
#define PK1 4
#define PSTEP 2
#define KV_BYTES (SK * 256)
#define MAP 0
#define WS_OFF 0
#define WIN 4
#define WSW 2
#define WSW1 2
#define NOCOMP 0
#define NOX 0
#define PREWARM 0
#define EARLYBAR 0
#define NLOAD 4
#define BARFIRST 0
#define PBASE 1
#define PEND 2
#define FPOS 1
#define TOUCH_AT 0
#define TOUCH_O
#define PST_AUX 16
#define PCNT 4
#define PLD_POL ""

namespace {

constexpr int B_ = 16, HKV = 8, SQ = 16, SK = 2048;
constexpr int TK = 64, NTILE = SK / TK;
constexpr int NPAIR = B_ * HKV;
constexpr int TILE_BYTES = TK * 256;
constexpr int NS = 8;
constexpr int RING_BYTES = NS * TILE_BYTES;
constexpr int XCH_BYTES = 4096 + 1024;
constexpr int LDS_BYTES = RING_BYTES + 4 * XCH_BYTES;
constexpr int NTHREADS = 512;
constexpr float C2 = 0.08838834764831845f * 1.4426950408889634f;
constexpr float SLACK = 7.0f;
constexpr unsigned GE2 = 0x40004000u;
constexpr int FLAG_OFF = 4096, EPOCH_OFF = FLAG_OFF + 256;
constexpr int HEAD_BYTES = EPOCH_OFF + 256;
constexpr unsigned TAG_STEP = 0x9E3779B1u;

typedef __attribute__((ext_vector_type(8))) __bf16 bf16x8;
typedef __attribute__((ext_vector_type(4))) __bf16 bf16x4;
typedef __attribute__((ext_vector_type(2))) __bf16 bf16x2;
typedef __attribute__((ext_vector_type(4))) float f32x4;
typedef __attribute__((ext_vector_type(2))) float f32x2;
typedef __attribute__((ext_vector_type(4))) unsigned u32x4;
typedef __attribute__((ext_vector_type(2))) unsigned u32x2;
#define LAS __attribute__((address_space(3)))

#define BARRIER() asm volatile("s_waitcnt lgkmcnt(0)\n\ts_barrier" ::: "memory")
#define LBARRIER() asm volatile("s_barrier" ::: "memory")
#define DIAG_OUT(code)
#define RSTAMP(i)
#define SB() __builtin_amdgcn_sched_barrier(0)

template <int N> __device__ __forceinline__ void wait_vm() { asm volatile("s_waitcnt vmcnt(%0)" ::"n"(N) : "memory"); }

__device__ __forceinline__ float xmax4(float x) {
  auto s = __builtin_amdgcn_permlane16_swap(__float_as_uint(x), __float_as_uint(x), false, false);
  x = fmaxf(__uint_as_float(s[0]), __uint_as_float(s[1]));
  auto t = __builtin_amdgcn_permlane32_swap(__float_as_uint(x), __float_as_uint(x), false, false);
  return fmaxf(__uint_as_float(t[0]), __uint_as_float(t[1]));
}
__device__ __forceinline__ unsigned pk(float a, float b) { f32x2 v = {a, b}; return __builtin_bit_cast(unsigned, __builtin_convertvector(v, bf16x2)); }
__device__ __forceinline__ f32x4 mfma(u32x4 a, u32x4 b, f32x4 c) {
  return __builtin_amdgcn_mfma_f32_16x16x32_bf16(__builtin_bit_cast(bf16x8, a), __builtin_bit_cast(bf16x8, b), c, 0, 0, 0);
}
__device__ __forceinline__ u32x2 trd(LAS char* p) { return __builtin_bit_cast(u32x2, __builtin_amdgcn_ds_read_tr16_b64_v4bf16((LAS bf16x4*)p)); }

__global__ void __launch_bounds__(NTHREADS, 2)
gqa_attn(const __hip_bfloat16* __restrict__ Q, const __hip_bfloat16* __restrict__ K, const __hip_bfloat16* __restrict__ V,
         __hip_bfloat16* __restrict__ O, unsigned char* __restrict__ ws) {
  __shared__ __attribute__((aligned(1024))) char smem[LDS_BYTES];
  LAS char* lds = (LAS char*)smem;
  const unsigned tstart = (unsigned)__builtin_amdgcn_s_memrealtime();
  const int lane = threadIdx.x & 63;
  const int wave = __builtin_amdgcn_readfirstlane(threadIdx.x >> 6);
  const int pair = blockIdx.x & (NPAIR - 1), role = blockIdx.x >> 7;
  const int n = role ? N1 : NTILE - N1;
  const int g = lane >> 4, c = lane & 15;

  if (__builtin_expect(wave >= NLOAD, 0)) {
    const int head = wave - NLOAD;
    const unsigned char* wp = ws + WS_OFF + ((size_t)pair * 4 + head) * HEAD_BYTES;
    const __amdgpu_buffer_rsrc_t rp = __builtin_amdgcn_make_buffer_rsrc((void*)wp, 0, HEAD_BYTES, 0x00020000);
    const __amdgpu_buffer_rsrc_t ro = __builtin_amdgcn_make_buffer_rsrc((char*)O + ((size_t)pair * 4 + head) * (SQ * 256), 0, SQ * 256, 0x00020000);
    const int po = lane * 16;
    const unsigned ka0 = c * 256 + ((g ^ ((c & 7) << 1)) << 4);
    const int vr = 4 * g + (c >> 2);
    const unsigned va0 = vr * 256 + ((vr & 7) << 5) + (c & 3) * 8;
    unsigned xi = RING_BYTES + head * XCH_BYTES + po, xw = RING_BYTES + head * XCH_BYTES + c * 272 + g * 8, xr = RING_BYTES + head * XCH_BYTES + g * 272 + c * 16;
    u32x4 ones = {0x3F803F80u, 0x3F803F80u, 0x3F803F80u, 0x3F803F80u};
    asm volatile("" : "+v"(ones), "+v"(xi), "+v"(xw), "+v"(xr));
    f32x4 o[8], lsum = {0.f, 0.f, 0.f, 0.f};
#pragma unroll
    for (int db = 0; db < 8; ++db) { o[db] = f32x4{0.f, 0.f, 0.f, 0.f}; asm volatile("" : "+v"(o[db])); }
    float nm = 3.0e38f;
    unsigned kb = 2 * TILE_BYTES;
    unsigned kx[4];
#pragma unroll
    for (int st = 0; st < 4; ++st) kx[st] = (ka0 + kb) ^ (st << 6);
    unsigned epoch;
    asm volatile("s_load_dword %0, %1, %2\n\ts_waitcnt lgkmcnt(0)" : "=&s"(epoch) : "s"(wp), "n"(EPOCH_OFF) : "memory");
    BARRIER();
    u32x4 q[4], kf[4][4];
#pragma unroll
    for (int st = 0; st < 4; ++st) q[st] = *(const LAS u32x4*)(lds + (TILE_BYTES + head * 4096) + (ka0 ^ (st << 6)));
#pragma unroll
    for (int sub = 0; sub < 4; ++sub)
#pragma unroll
      for (int st = 0; st < 4; ++st) kf[sub][st] = *(const LAS u32x4*)(lds + (ka0 ^ (st << 6)) + sub * 4096);
    asm volatile("" : "+v"(kf[0][0]), "+v"(kf[0][1]), "+v"(kf[0][2]), "+v"(kf[0][3]), "+v"(kf[1][0]), "+v"(kf[1][1]), "+v"(kf[1][2]), "+v"(kf[1][3]),
                 "+v"(kf[2][0]), "+v"(kf[2][1]), "+v"(kf[2][2]), "+v"(kf[2][3]), "+v"(kf[3][0]), "+v"(kf[3][1]), "+v"(kf[3][2]), "+v"(kf[3][3]),
                 "+v"(q[0]), "+v"(q[1]), "+v"(q[2]), "+v"(q[3]));
    BARRIER();
#pragma unroll 1
    for (int left = n; left != 0; --left) {
      f32x4 s[4], x[4];
      u32x4 p[2];
      u32x2 vf[16][2];
      const f32x4 zero = {0.f, 0.f, 0.f, 0.f};
      const unsigned va = va0 + (left == 1 ? kb : (kb + 3 * TILE_BYTES) & (RING_BYTES - 1));
      unsigned vx[8];
#define QK1(sub, st) do { s[sub] = mfma(kf[sub][st], q[st], (st) ? s[sub] : zero); kf[sub][st] = *(const LAS u32x4*)(lds + kx[st] + (sub) * 4096); } while (0)
#define FM(sub) x[sub] = s[sub] * C2 + nm
#define EX(sub, r) x[sub][r] = __builtin_amdgcn_exp2f(x[sub][r])
#define CV(sub, h) p[(sub) >> 1][2 * ((sub) & 1) + (h)] = pk(x[sub][2 * (h)], x[sub][2 * (h) + 1])
#define VX(i) do { vx[i] = va ^ ((i) << 5); if (i) asm volatile("" : "+v"(vx[i])); } while (0)
#define VRD(i) do { vf[i][0] = trd(lds + vx[(i) & 7] + ((i) >> 3) * 8192); vf[i][1] = trd(lds + vx[(i) & 7] + ((i) >> 3) * 8192 + 4096); } while (0)
#define PV1(i) o[(i) & 7] = mfma(u32x4{vf[i][0][0], vf[i][0][1], vf[i][1][0], vf[i][1][1]}, p[(i) >> 3], o[(i) & 7])
      QK1(0, 0); VX(0); SB(); QK1(1, 0); VX(1); SB(); QK1(0, 1); VX(2); SB(); QK1(1, 1); VX(3); SB();
      QK1(0, 2); VX(4); SB(); QK1(1, 2); VX(5); SB(); QK1(0, 3); VX(6); SB(); QK1(1, 3); VX(7); SB();
      QK1(2, 0); SB(); QK1(3, 0); FM(0); SB();
      QK1(2, 1); FM(1); EX(0, 0); SB(); QK1(3, 1); EX(0, 1); EX(0, 2); SB();
      QK1(2, 2); EX(0, 3); EX(1, 0); SB(); QK1(3, 2); EX(1, 1); EX(1, 2); SB();
      QK1(2, 3); EX(1, 3); CV(0, 0); SB(); QK1(3, 3); CV(0, 1); CV(1, 0); CV(1, 1); SB();
      unsigned any = (p[0][0] | p[0][1] | p[0][2]) | p[0][3];
      asm volatile("" : "+v"(any) :: "memory");
      kb = (kb + 2 * TILE_BYTES) & (RING_BYTES - 1);
      SB();
      FM(2); FM(3); SB();
      EX(2, 0); EX(2, 1); EX(2, 2); EX(2, 3); EX(3, 0); EX(3, 1); EX(3, 2); EX(3, 3); SB();
      CV(2, 0); CV(2, 1); CV(3, 0); CV(3, 1);
      any = (any | p[1][0] | p[1][1]) | p[1][2] | p[1][3];
      if (__builtin_expect(__builtin_amdgcn_ballot_w64((any & GE2) != 0) != 0, 0)) {
        float t = s[0][0];
#pragma unroll
        for (int sub = 0; sub < 4; ++sub)
#pragma unroll
          for (int r = 0; r < 4; ++r) t = fmaxf(t, s[sub][r]);
        const float nn = fminf(nm, -fmaf(xmax4(t), C2, SLACK));
        const float alpha = __builtin_amdgcn_exp2f(nn - nm);
        nm = nn;
#pragma unroll
        for (int db = 0; db < 8; ++db) o[db] *= alpha;
        lsum *= alpha;
#pragma unroll
        for (int sub = 0; sub < 4; ++sub) { FM(sub); EX(sub, 0); EX(sub, 1); EX(sub, 2); EX(sub, 3); CV(sub, 0); CV(sub, 1); }
      }
      BARRIER();
      VRD(0); VRD(1); VRD(2); VRD(3); VRD(4); VRD(5); SB();
      lsum = mfma(ones, p[0], lsum);
      lsum = mfma(ones, p[1], lsum);
#pragma unroll
      for (int st = 0; st < 4; ++st) { kx[st] = (ka0 + kb) ^ (st << 6); if (st) asm volatile("" : "+v"(kx[st])); }
      SB();
      VRD(6); SB(); PV1(0); PV1(1); VRD(7); VRD(8); SB(); PV1(2); PV1(3); VRD(9); VRD(10); SB();
      PV1(4); PV1(5); VRD(11); VRD(12); SB(); PV1(6); PV1(7); VRD(13); VRD(14); SB();
      PV1(8); PV1(9); VRD(15); SB(); PV1(10); PV1(11); PV1(12); PV1(13); PV1(14); PV1(15); SB();
    }
    RSTAMP(52);
    if (role) {
      const unsigned tag = epoch + TAG_STEP;
      const float inv = __builtin_amdgcn_rcpf(lsum[0]);
      u32x4 y[4];
#pragma unroll
      for (int i = 0; i < 4; ++i) {
        const f32x4 a = o[2 * i] * inv, b = o[2 * i + 1] * inv;
        y[i] = u32x4{pk(a[0], a[1]), pk(a[2], a[3]), pk(b[0], b[1]), pk(b[2], b[3])};
        if (i < PCNT) __builtin_amdgcn_raw_buffer_store_b128(y[i], rp, po + i * 1024, 0, PST_AUX);
      }
      asm volatile("s_waitcnt vmcnt(0)" :: "v"(y[0]), "v"(y[1]), "v"(y[2]), "v"(y[3]) : "memory");
      __builtin_amdgcn_raw_buffer_store_b128(u32x4{__float_as_uint(nm), tag, __float_as_uint(lsum[0]), tag}, rp, g == 0 ? FLAG_OFF + po : HEAD_BYTES, 0, 16);
      DIAG_OUT(1);
      return;
    }
    if (!NOX) BARRIER();
    RSTAMP(53);
    {
      const u32x4 st = *(LAS u32x4*)(lds + xi + 4096);
      u32x4 y[4];
#pragma unroll
      for (int i = 0; i < 4; ++i) y[i] = *(LAS u32x4*)(lds + xi + i * 1024);
      SB();
      const float nm2 = __uint_as_float(st[0]), l2 = __uint_as_float(st[2]);
      const float nn = fminf(nm, nm2);
      const float b1 = __builtin_amdgcn_exp2f(nn - nm), b2 = __builtin_amdgcn_exp2f(nn - nm2) * l2;
      const float inv = __builtin_amdgcn_rcpf(fmaf(b1, lsum[0], b2));
      const float w1 = b1 * inv, w2 = b2 * inv;
#pragma unroll
      for (int i = 0; i < 8; ++i) {
        const unsigned y0 = y[i >> 1][2 * (i & 1)], y1 = y[i >> 1][2 * (i & 1) + 1];
        const f32x4 u = {__uint_as_float(y0 << 16), __uint_as_float(y0 & 0xFFFF0000u), __uint_as_float(y1 << 16), __uint_as_float(y1 & 0xFFFF0000u)};
        const f32x4 r = o[i] * w1 + u * w2;
        *(LAS u32x2*)(lds + xw + i * 32) = u32x2{pk(r[0], r[1]), pk(r[2], r[3])};
      }
      asm volatile("" ::: "memory");
      u32x4 rows[4];
#pragma unroll
      for (int i = 0; i < 4; ++i) rows[i] = *(LAS u32x4*)(lds + xr + i * (4 * 272));
#pragma unroll
      for (int i = 0; i < 4; ++i) __builtin_amdgcn_raw_buffer_store_b128(rows[i], ro, po + i * 1024, 0, OUT_AUX);
    }
    DIAG_OUT(4);
    return;
  }

  const int vo0 = g * 256 + ((c ^ (g << 1)) << 4), vo1 = vo0 ^ 128;
  const __amdgpu_buffer_rsrc_t rk = __builtin_amdgcn_make_buffer_rsrc((char*)K + (size_t)pair * (SK * 256), 0, KV_BYTES, 0x00020000);
  const __amdgpu_buffer_rsrc_t rv = __builtin_amdgcn_make_buffer_rsrc((char*)V + (size_t)pair * (SK * 256), 0, KV_BYTES, 0x00020000);
  const __amdgpu_buffer_rsrc_t rq = __builtin_amdgcn_make_buffer_rsrc((char*)Q + (size_t)pair * TILE_BYTES, 0, TILE_BYTES, 0x00020000);
  const int wo = wave * 4096;
  auto unit = [&](const __amdgpu_buffer_rsrc_t r, int so, unsigned slot) {
    const unsigned dst = (unsigned)(uintptr_t)lds + (slot & (NS - 1)) * TILE_BYTES + wo;
    asm volatile("s_mov_b32 m0, %0\n\ts_nop 0\n\t"
                 "buffer_load_dwordx4 %1, %3, %4 offen " KV_POL " lds\n\t"
                 "buffer_load_dwordx4 %2, %3, %4 offen offset:1024 " KV_POL " lds\n\t"
                 "buffer_load_dwordx4 %1, %3, %4 offen offset:2048 " KV_POL " lds\n\t"
                 "buffer_load_dwordx4 %2, %3, %4 offen offset:3072 " KV_POL " lds"
                 :: "s"(dst), "v"(vo0), "v"(vo1), "s"(r), "s"(so) : "memory");
  };
  auto off = [&](int t) { return (t < N1 ? 2 * t + role : N1 + t) * TILE_BYTES + wo; };
#define UK(t) unit(rk, off(t), 2 * (t))
#define UV(t) unit(rv, off(t), 2 * (t) + 5)
  UK(0); unit(rq, wo, 1); UK(1); UV(0);
  const unsigned char* wp = ws + WS_OFF + ((size_t)pair * 4 + wave) * HEAD_BYTES;
  unsigned epoch;
  asm volatile("s_load_dword %0, %1, %2\n\ts_waitcnt lgkmcnt(0)" : "=&s"(epoch) : "s"(wp), "n"(EPOCH_OFF) : "memory");
  int s = 2;
  unsigned due = tstart + PACE0;
  const unsigned pstep = (role ? PACE1 : PACE) / 2;
#define PACE_WAIT() do { while ((int)((unsigned)__builtin_amdgcn_s_memrealtime() - due) < 0) __builtin_amdgcn_s_sleep(1); due += pstep; } while (0)
  wait_vm<12>(); UK(2);
  wait_vm<12>(); UV(1); LBARRIER();
  wait_vm<12>(); UK(3); LBARRIER();
#pragma unroll 1
  for (; s < n - 1; ++s) {
    const bool tail = s == n - 2;
    wait_vm<12>(); if (s < N1 - PEND) PACE_WAIT(); UV(s);
    wait_vm<12>(); if (s < N1 - PEND) PACE_WAIT(); unit(tail ? rv : rk, off(tail ? s + 1 : s + 2), 2 * s + 4); LBARRIER();
  }
  if (role || NOX || NOCOMP) {
    wait_vm<8>(); LBARRIER();
    wait_vm<4>(); LBARRIER();
    wait_vm<0>(); LBARRIER();
    DIAG_OUT(0);
    return;
  }
  const __amdgpu_buffer_rsrc_t rp = __builtin_amdgcn_make_buffer_rsrc((void*)wp, 0, HEAD_BYTES, 0x00020000);
  int spins = 1 << 20;
  unsigned long long tmp64;
#define FLAG_LOAD "buffer_load_dwordx4 v[32:35], %[fo], %[rp], 0 offen sc1\n\t"
#define FLAG_TEST "v_cmp_ne_u32 vcc, %[tag], v33\n\tv_cmp_ne_u32 %[t], %[tag], v35\n\ts_nop 3\n\ts_or_b64 vcc, vcc, %[t]\n\t"
#define PART_LOAD "buffer_load_dwordx4 v[16:19], %[po], %[rp], 0 offen sc1\n\t" \
                  "buffer_load_dwordx4 v[20:23], %[po], %[rp], 0 offen offset:1024 sc1\n\t" \
                  "buffer_load_dwordx4 v[24:27], %[po], %[rp], 0 offen offset:2048 sc1\n\t" \
                  "buffer_load_dwordx4 v[28:31], %[po], %[rp], 0 offen offset:3072 sc1\n\t"
  asm volatile(
      "s_waitcnt vmcnt(8)\n\ts_barrier\n\t"
      FLAG_LOAD
      "s_waitcnt vmcnt(5)\n\ts_barrier\n\t"
      "s_waitcnt vmcnt(0)\n\t"
      FLAG_TEST
      "s_cbranch_vccnz 1f\n\t"
      PART_LOAD
      "s_barrier\n"
      "3:\n\t"
      "ds_write_b128 %[xi], v[32:35] offset:4096\n\t"
      "s_waitcnt vmcnt(0)\n\t"
      "ds_write_b128 %[xi], v[16:19]\n\t"
      "ds_write_b128 %[xi], v[20:23] offset:1024\n\t"
      "ds_write_b128 %[xi], v[24:27] offset:2048\n\t"
      "ds_write_b128 %[xi], v[28:31] offset:3072\n\t"
      "s_waitcnt lgkmcnt(0)\n\ts_barrier\n\t"
      "buffer_store_dword v33, %[eo], %[rp], 0 offen sc1\n\t"
      "s_branch 4f\n"
      "1:\n\t"
      FLAG_LOAD
      "s_barrier\n"
      "2:\n\t"
      "s_waitcnt vmcnt(0)\n\t"
      FLAG_TEST
      "s_cbranch_vccz 5f\n\t"
      FLAG_LOAD
      "s_sub_u32 %[spins], %[spins], 1\n\t"
      "s_cbranch_scc0 2b\n\t"
      "s_waitcnt vmcnt(0)\n\t"
      "v_mov_b32 v34, 0x7fc00000\n"
      "5:\n\t"
      "s_sub_u32 %[spins], %[spins], 1\n\t"
      PART_LOAD
      "s_branch 3b\n"
      "4:"
      : [spins] "+s"(spins), [t] "=&s"(tmp64)
      : [tag] "s"(epoch + TAG_STEP), [fo] "v"(FLAG_OFF + c * 16), [po] "v"(lane * 16), [eo] "v"(lane == 0 ? EPOCH_OFF : HEAD_BYTES),
        [xi] "v"((unsigned)(uintptr_t)lds + RING_BYTES + wave * XCH_BYTES + lane * 16), [rp] "s"(rp)
      : "memory", "vcc", "scc", "v16", "v17", "v18", "v19", "v20", "v21", "v22", "v23", "v24", "v25", "v26", "v27", "v28", "v29", "v30", "v31", "v32", "v33", "v34", "v35");
  DIAG_OUT(spins == (1 << 20) ? 2 : spins == (1 << 20) - 1 ? 3 : 5);
}

}

extern "C" void kernel_launch(void* const* d_in, const int* in_sizes, int n_in, void* d_out, int out_size, void* d_ws, size_t ws_size,
                              hipStream_t stream) {
  gqa_attn<<<2 * NPAIR, NTHREADS, 0, stream>>>((const __hip_bfloat16*)d_in[0], (const __hip_bfloat16*)d_in[1], (const __hip_bfloat16*)d_in[2],
                                                (__hip_bfloat16*)d_out, (unsigned char*)d_ws);
}
```

```cpp
#define PACE 108
#define PACE0 380
#define PACE_LAST 0
#include <hip/hip_runtime.h>
#include <hip/hip_bf16.h>
#include <stdint.h>

#define NA 12
#define K2_POL "nt"
#define OUT_AUX 18
#define KV_BYTES (SK * 256)
#define NOCOMP 0
#define NOX 0
#define FLAG_LAG 2
#define NLOAD 4
#define L1 1
#define L2 2
#define L3 2
#define ST_POL "sc1"
#define LD_POL "sc1"
#define BARFIRST 0

namespace {

constexpr int B_ = 16, HKV = 8, SQ = 16, SK = 2048;
constexpr int TK = 64, NT = SK / TK;
constexpr int NB = NT - 2 * NA, NI = NA + NB;
constexpr int NPAIR = B_ * HKV;
constexpr int KT_BYTES = TK * 256, VH_BYTES = TK * 128;
constexpr int SLOT_BYTES = KT_BYTES + 2 * VH_BYTES;
constexpr int RING_BYTES = 4 * SLOT_BYTES;
constexpr int Q_OFF = RING_BYTES;
constexpr int XCH_BYTES = 4096;
constexpr int LDS_BYTES = RING_BYTES + 4 * XCH_BYTES;
constexpr int NTHREADS = 576;
constexpr float C2 = 0.08838834764831845f * 1.4426950408889634f;
constexpr float SLACK = 7.0f;
constexpr unsigned GE2 = 0x40004000u;
constexpr int FLAG_OFF = 8192, EPOCH_OFF = FLAG_OFF + 1024;
constexpr int BOX_BYTES = EPOCH_OFF + 256;
constexpr unsigned TAG_STEP = 0x9E3779B1u;
static_assert(NB >= 6 && NA >= 4, "the swap needs a few tiles to hide under");

typedef __attribute__((ext_vector_type(8))) __bf16 bf16x8;
typedef __attribute__((ext_vector_type(4))) __bf16 bf16x4;
typedef __attribute__((ext_vector_type(2))) __bf16 bf16x2;
typedef __attribute__((ext_vector_type(4))) float f32x4;
typedef __attribute__((ext_vector_type(2))) float f32x2;
typedef __attribute__((ext_vector_type(4))) unsigned u32x4;
typedef __attribute__((ext_vector_type(2))) unsigned u32x2;
#define LAS __attribute__((address_space(3)))

#define BARRIER() asm volatile("s_waitcnt lgkmcnt(0)\n\ts_barrier" ::: "memory")
#define LBARRIER() asm volatile("s_barrier" ::: "memory")
#define DIAG_OUT(code)
#define RSTAMP(i)
#define SB() __builtin_amdgcn_sched_barrier(0)

template <int N> __device__ __forceinline__ void wait_vm() { asm volatile("s_waitcnt vmcnt(%0)" ::"n"(N) : "memory"); }

__device__ __forceinline__ float xmax4(float x) {
  auto s = __builtin_amdgcn_permlane16_swap(__float_as_uint(x), __float_as_uint(x), false, false);
  x = fmaxf(__uint_as_float(s[0]), __uint_as_float(s[1]));
  auto t = __builtin_amdgcn_permlane32_swap(__float_as_uint(x), __float_as_uint(x), false, false);
  return fmaxf(__uint_as_float(t[0]), __uint_as_float(t[1]));
}
__device__ __forceinline__ unsigned pk(float a, float b) { f32x2 v = {a, b}; return __builtin_bit_cast(unsigned, __builtin_convertvector(v, bf16x2)); }
__device__ __forceinline__ f32x4 mfma(u32x4 a, u32x4 b, f32x4 c) {
  return __builtin_amdgcn_mfma_f32_16x16x32_bf16(__builtin_bit_cast(bf16x8, a), __builtin_bit_cast(bf16x8, b), c, 0, 0, 0);
}
__device__ __forceinline__ u32x2 trd(LAS char* p) { return __builtin_bit_cast(u32x2, __builtin_amdgcn_ds_read_tr16_b64_v4bf16((LAS bf16x4*)p)); }

__global__ void __launch_bounds__(NTHREADS, 3)
gqa_attn(const __hip_bfloat16* __restrict__ Q, const __hip_bfloat16* __restrict__ K, const __hip_bfloat16* __restrict__ V,
         __hip_bfloat16* __restrict__ O, unsigned char* __restrict__ ws) {
  __shared__ __attribute__((aligned(1024))) char smem[LDS_BYTES];
  LAS char* lds = (LAS char*)smem;
  const unsigned tstart = (unsigned)__builtin_amdgcn_s_memrealtime();
  const int lane = threadIdx.x & 63;
  const int wave = __builtin_amdgcn_readfirstlane(threadIdx.x >> 6);
  const int pair = blockIdx.x & (NPAIR - 1), half = blockIdx.x >> 7;
  const int g = lane >> 4, c = lane & 15;

  if (__builtin_expect(wave >= NLOAD, 0)) {
    if (wave == 8) {
      if (NOX || NOCOMP) return;
      const unsigned char* wp = ws + (size_t)pair * (2 * BOX_BYTES);
      const int bo = half * BOX_BYTES, bi = (half ^ 1) * BOX_BYTES;
      unsigned ep_out, ep_in;
      asm volatile("s_load_dword %0, %2, %3\n\ts_load_dword %1, %2, %4\n\ts_waitcnt lgkmcnt(0)" : "=&s"(ep_out), "=&s"(ep_in) : "s"(wp), "s"(bo + EPOCH_OFF), "s"(bi + EPOCH_OFF) : "memory");
      const __amdgpu_buffer_rsrc_t rp = __builtin_amdgcn_make_buffer_rsrc((void*)wp, 0, 2 * BOX_BYTES, 0x00020000);
      const unsigned xb = (unsigned)(uintptr_t)lds + Q_OFF;
#pragma unroll 1
      for (int j = 0; j < NA + 3; ++j) asm volatile("s_barrier" ::: "memory");
      int spins = 1 << 20;
      unsigned long long tmp64;
#define BAR "s_barrier\n\t"
#define BAR0
#define BAR1 BAR
#define BAR2 BAR BAR
#define BAR3 BAR BAR BAR
#define BAR4 BAR BAR BAR BAR
#define BAR5 BAR BAR BAR BAR BAR
#define BAR6 BAR BAR BAR BAR BAR BAR
#define BARS_(n) BAR##n
#define BARS(n) BARS_(n)
      static_assert(L1 + L2 + L3 + 3 == NB, "barrier count");
#define FLAG_LOAD "buffer_load_dwordx4 v[48:51], %[po], %[rp], %[fi] offen sc1\n\t"
#define FLAG_TEST "v_cmp_ne_u32 vcc, %[tin], v49\n\tv_cmp_ne_u32 %[t], %[tin], v51\n\ts_nop 3\n\ts_or_b64 vcc, vcc, %[t]\n\t"
#define PART_IO(op, s0, s1, pol) \
      op " v[16:19], %[po], %[rp], " s0 " offen " pol "\n\t" op " v[20:23], %[po], %[rp], " s0 " offen offset:1024 " pol "\n\t" \
      op " v[24:27], %[po], %[rp], " s0 " offen offset:2048 " pol "\n\t" op " v[28:31], %[po], %[rp], " s0 " offen offset:3072 " pol "\n\t" \
      op " v[32:35], %[po], %[rp], " s1 " offen " pol "\n\t" op " v[36:39], %[po], %[rp], " s1 " offen offset:1024 " pol "\n\t" \
      op " v[40:43], %[po], %[rp], " s1 " offen offset:2048 " pol "\n\t" op " v[44:47], %[po], %[rp], " s1 " offen offset:3072 " pol "\n\t"
      asm volatile(
          "ds_read_b128 v[16:19], %[xi]\n\tds_read_b128 v[20:23], %[xi] offset:1024\n\t"
          "ds_read_b128 v[24:27], %[xi] offset:4096\n\tds_read_b128 v[28:31], %[xi] offset:5120\n\t"
          "ds_read_b128 v[32:35], %[xi] offset:8192\n\tds_read_b128 v[36:39], %[xi] offset:9216\n\t"
          "ds_read_b128 v[40:43], %[xi] offset:12288\n\tds_read_b128 v[44:47], %[xi] offset:13312\n\t"
          "ds_read_b128 v[48:51], %[xs]\n\t"
          "s_waitcnt lgkmcnt(0)\n\t"
          PART_IO("buffer_store_dwordx4", "%[bo]", "%[bo2]", ST_POL)
          "v_mov_b32 v49, %[tout]\n\tv_mov_b32 v51, %[tout]\n\t"
          BARS(L1)
          "s_waitcnt vmcnt(0)\n\t"
          "buffer_store_dwordx4 v[48:51], %[po], %[rp], %[fo] offen sc1\n\t"
          BARS(L2)
          FLAG_LOAD
          BARS(L3)
          "s_waitcnt vmcnt(0)\n\t"
          FLAG_TEST
          "s_cbranch_vccnz 1f\n\t"
          PART_IO("buffer_load_dwordx4", "%[bi]", "%[bi2]", LD_POL)
          BAR
          "3:\n\t"
          "ds_write_b128 %[xs], v[48:51]\n\t"
          "s_waitcnt vmcnt(0)\n\t"
          "ds_write_b128 %[xi], v[16:19]\n\tds_write_b128 %[xi], v[20:23] offset:1024\n\t"
          "ds_write_b128 %[xi], v[24:27] offset:4096\n\tds_write_b128 %[xi], v[28:31] offset:5120\n\t"
          "ds_write_b128 %[xi], v[32:35] offset:8192\n\tds_write_b128 %[xi], v[36:39] offset:9216\n\t"
          "ds_write_b128 %[xi], v[40:43] offset:12288\n\tds_write_b128 %[xi], v[44:47] offset:13312\n\t"
          "s_waitcnt lgkmcnt(0)\n\t" BAR
          "buffer_store_dword v49, %[eo], %[rp], %[bi] offen sc1\n\t"
          "s_branch 4f\n"
          "1:\n\t"
          FLAG_LOAD
          BAR
          "2:\n\t"
          "s_waitcnt vmcnt(0)\n\t"
          FLAG_TEST
          "s_cbranch_vccz 5f\n\t"
          FLAG_LOAD
          "s_sub_u32 %[spins], %[spins], 1\n\t"
          "s_cbranch_scc0 2b\n\t"
          "s_waitcnt vmcnt(0)\n\t"
          "v_mov_b32 v50, 0x7fc00000\n"
          "5:\n\t"
          "s_sub_u32 %[spins], %[spins], 1\n\t"
          PART_IO("buffer_load_dwordx4", "%[bi]", "%[bi2]", LD_POL)
          "s_branch 3b\n"
          "4:"
          : [spins] "+s"(spins), [t] "=&s"(tmp64)
          : [tin] "s"(ep_in + TAG_STEP), [tout] "s"(ep_out + TAG_STEP), [bi] "s"(bi), [bi2] "s"(bi + 4096), [fi] "s"(bi + FLAG_OFF),
            [bo] "s"(bo), [bo2] "s"(bo + 4096), [fo] "s"(bo + FLAG_OFF), [po] "v"(lane * 16), [eo] "v"(lane == 0 ? EPOCH_OFF : 2 * BOX_BYTES),
            [xi] "v"(xb + lane * 16), [xs] "v"(xb + g * XCH_BYTES + 2048 + c * 16), [rp] "s"(rp)
          : "memory", "vcc", "scc", "v16", "v17", "v18", "v19", "v20", "v21", "v22", "v23", "v24", "v25", "v26", "v27", "v28", "v29", "v30", "v31",
            "v32", "v33", "v34", "v35", "v36", "v37", "v38", "v39", "v40", "v41", "v42", "v43", "v44", "v45", "v46", "v47", "v48", "v49", "v50", "v51");
      DIAG_OUT(spins == (1 << 20) ? 2 : spins == (1 << 20) - 1 ? 3 : 5);
      return;
    }
    const int head = wave - NLOAD;
    const __amdgpu_buffer_rsrc_t ro = __builtin_amdgcn_make_buffer_rsrc((char*)O + ((size_t)pair * 4 + head) * (SQ * 256), 0, SQ * 256, 0x00020000);
    const unsigned ka0 = c * 256 + ((g ^ ((c & 7) << 1)) << 4);
    const int vr = 4 * g + (c >> 2);
    const unsigned va0 = KT_BYTES + vr * 128 + (((vr >> 1) & 3) << 5) + (c & 3) * 8;
    unsigned xi = Q_OFF + head * XCH_BYTES + lane * 16;
    u32x4 ones = {0x3F803F80u, 0x3F803F80u, 0x3F803F80u, 0x3F803F80u};
    asm volatile("" : "+v"(ones), "+v"(xi));
    f32x4 o[8], lsum = {0.f, 0.f, 0.f, 0.f};
#pragma unroll
    for (int db = 0; db < 8; ++db) { o[db] = f32x4{0.f, 0.f, 0.f, 0.f}; asm volatile("" : "+v"(o[db])); }
    float nm = 3.0e38f;
    unsigned sb = SLOT_BYTES;
    unsigned kx[4];
#pragma unroll
    for (int st = 0; st < 4; ++st) kx[st] = (ka0 ^ (st << 6)) + sb;
    BARRIER();
    u32x4 q[4], kf[4][4];
#pragma unroll
    for (int st = 0; st < 4; ++st) q[st] = *(const LAS u32x4*)(lds + (Q_OFF + head * 4096) + (ka0 ^ (st << 6)));
#pragma unroll
    for (int sub = 0; sub < 4; ++sub)
#pragma unroll
      for (int st = 0; st < 4; ++st) kf[sub][st] = *(const LAS u32x4*)(lds + (ka0 ^ (st << 6)) + sub * 4096);
    asm volatile("" : "+v"(kf[0][0]), "+v"(kf[0][1]), "+v"(kf[0][2]), "+v"(kf[0][3]), "+v"(kf[1][0]), "+v"(kf[1][1]), "+v"(kf[1][2]), "+v"(kf[1][3]),
                 "+v"(kf[2][0]), "+v"(kf[2][1]), "+v"(kf[2][2]), "+v"(kf[2][3]), "+v"(kf[3][0]), "+v"(kf[3][1]), "+v"(kf[3][2]), "+v"(kf[3][3]),
                 "+v"(q[0]), "+v"(q[1]), "+v"(q[2]), "+v"(q[3]));
    BARRIER();
    int left = NI;
    asm volatile("" : "+s"(left));
#pragma unroll 1
    do {
      f32x4 s[4], x[4];
      u32x4 p[2];
      u32x2 vf[8][2];
      const f32x4 zero = {0.f, 0.f, 0.f, 0.f};
      sb = (sb + SLOT_BYTES) & (RING_BYTES - 1);
      unsigned vx[4];
#define QK1(sub, st) do { s[sub] = mfma(kf[sub][st], q[st], (st) ? s[sub] : zero); kf[sub][st] = *(const LAS u32x4*)(lds + kx[st] + (sub) * 4096); } while (0)
#define FM(sub) x[sub] = s[sub] * C2 + nm
#define EX(sub, r) x[sub][r] = __builtin_amdgcn_exp2f(x[sub][r])
#define CV(sub, h) p[(sub) >> 1][2 * ((sub) & 1) + (h)] = pk(x[sub][2 * (h)], x[sub][2 * (h) + 1])
#define VX(i) do { vx[i] = (i) ? vx[0] ^ ((i) << 5) : va0 + sb; asm volatile("" : "+v"(vx[i])); } while (0)
#define VRD(i, off) do { vf[i][0] = trd(lds + vx[(i) & 3] + ((i) >> 2) * 4096 + (off)); vf[i][1] = trd(lds + vx[(i) & 3] + ((i) >> 2) * 4096 + 2048 + (off)); } while (0)
#define PV1(i, ob) o[(ob) + ((i) & 3)] = mfma(u32x4{vf[i][0][0], vf[i][0][1], vf[i][1][0], vf[i][1][1]}, p[(i) >> 2], o[(ob) + ((i) & 3)])
      QK1(0, 0); SB(); QK1(1, 0); VX(0); SB(); QK1(0, 1); VX(1); SB(); QK1(1, 1); VX(2); SB();
      QK1(0, 2); VX(3); SB(); QK1(1, 2); SB(); QK1(0, 3); SB(); QK1(1, 3); SB();
      QK1(2, 0); SB(); QK1(3, 0); FM(0); SB();
      QK1(2, 1); FM(1); EX(0, 0); SB(); QK1(3, 1); EX(0, 1); EX(0, 2); SB();
      QK1(2, 2); EX(0, 3); EX(1, 0); SB(); QK1(3, 2); EX(1, 1); EX(1, 2); SB();
      QK1(2, 3); EX(1, 3); CV(0, 0); SB(); QK1(3, 3); CV(0, 1); CV(1, 0); CV(1, 1); SB();
      unsigned any = (p[0][0] | p[0][1] | p[0][2]) | p[0][3];
      asm volatile("" : "+v"(any) :: "memory");
      SB();
      FM(2); FM(3); SB();
      EX(2, 0); EX(2, 1); EX(2, 2); EX(2, 3); EX(3, 0); EX(3, 1); EX(3, 2); EX(3, 3); SB();
      CV(2, 0); CV(2, 1); CV(3, 0); CV(3, 1);
      any = (any | p[1][0] | p[1][1]) | p[1][2] | p[1][3];
      if (__builtin_expect(__builtin_amdgcn_ballot_w64((any & GE2) != 0) != 0, 0)) {
        float t = s[0][0];
#pragma unroll
        for (int sub = 0; sub < 4; ++sub)
#pragma unroll
          for (int r = 0; r < 4; ++r) t = fmaxf(t, s[sub][r]);
        const float nn = fminf(nm, -fmaf(xmax4(t), C2, SLACK));
        const float alpha = __builtin_amdgcn_exp2f(nn - nm);
        nm = nn;
#pragma unroll
        for (int db = 0; db < 8; ++db) o[db] *= alpha;
        lsum *= alpha;
#pragma unroll
        for (int sub = 0; sub < 4; ++sub) { FM(sub); EX(sub, 0); EX(sub, 1); EX(sub, 2); EX(sub, 3); CV(sub, 0); CV(sub, 1); }
      }
      BARRIER();
      VRD(0, 0); VRD(1, 0); VRD(2, 0); VRD(3, 0); VRD(4, 0); SB();
      lsum = mfma(ones, p[0], lsum);
      lsum = mfma(ones, p[1], lsum);
      kx[0] = ka0 + sb; kx[1] = kx[0] ^ 64; kx[2] = kx[0] ^ 128; kx[3] = kx[0] ^ 192;
      asm volatile("" : "+v"(kx[0]), "+v"(kx[1]), "+v"(kx[2]), "+v"(kx[3]));
      SB();
      PV1(0, 0); SB(); VRD(5, 0); SB(); PV1(1, 0); SB(); VRD(6, 0); SB(); PV1(2, 0); SB(); VRD(7, 0); SB(); PV1(3, 0); PV1(4, 0); PV1(5, 0); PV1(6, 0); PV1(7, 0); SB();
      if (__builtin_expect(left > NB, 0)) {
        VRD(0, VH_BYTES); VRD(1, VH_BYTES); VRD(2, VH_BYTES); VRD(3, VH_BYTES); VRD(4, VH_BYTES); SB();
        PV1(0, 4); SB(); VRD(5, VH_BYTES); SB(); PV1(1, 4); SB(); VRD(6, VH_BYTES); SB(); PV1(2, 4); SB(); VRD(7, VH_BYTES); SB(); PV1(3, 4); PV1(4, 4); PV1(5, 4); PV1(6, 4); PV1(7, 4); SB();
        if (left == NB + 1) {
          *(LAS f32x4*)(lds + xi + 2048) = f32x4{nm, 0.f, lsum[0], 0.f};
#pragma unroll
          for (int i = 0; i < 2; ++i) {
            const f32x4 a = o[4 + 2 * i], b = o[5 + 2 * i];
            *(LAS u32x4*)(lds + xi + i * 1024) = u32x4{pk(a[0], a[1]), pk(a[2], a[3]), pk(b[0], b[1]), pk(b[2], b[3])};
          }
        }
      }
    } while (--left != 0);
    RSTAMP(52);
    {
      asm volatile("" : "+v"(xi));
      const unsigned ln = (xi >> 4) & 63, xb = Q_OFF + head * XCH_BYTES;
      const unsigned xw = xb + (ln & 15) * 144 + (ln >> 4) * 8, xr = xb + (ln >> 3) * 144 + (ln & 7) * 16;
      const int oo = (ln >> 3) * 256 + half * 128 + (ln & 7) * 16;
      const f32x4 st = *(LAS f32x4*)(lds + (xi & ~0x300u) + 2048);
      const u32x4 yy[2] = {*(LAS u32x4*)(lds + xi), *(LAS u32x4*)(lds + xi + 1024)};
      SB();
      f32x4 y[4];
#pragma unroll
      for (int i = 0; i < 4; ++i) {
        const unsigned y0 = yy[i >> 1][2 * (i & 1)], y1 = yy[i >> 1][2 * (i & 1) + 1];
        y[i] = f32x4{__uint_as_float(y0 << 16), __uint_as_float(y0 & 0xFFFF0000u), __uint_as_float(y1 << 16), __uint_as_float(y1 & 0xFFFF0000u)};
      }
      const float nn = fminf(nm, st[0]);
      const float b1 = __builtin_amdgcn_exp2f(nn - nm), b2 = __builtin_amdgcn_exp2f(nn - st[0]);
      const float inv = __builtin_amdgcn_rcpf(fmaf(b1, lsum[0], b2 * st[2]));
      const float w1 = b1 * inv, w2 = b2 * inv;
#pragma unroll
      for (int i = 0; i < 4; ++i) {
        const f32x4 r = o[i] * w1 + y[i] * w2;
        *(LAS u32x2*)(lds + xw + i * 32) = u32x2{pk(r[0], r[1]), pk(r[2], r[3])};
      }
      asm volatile("" ::: "memory");
      const u32x4 r0 = *(LAS u32x4*)(lds + xr), r1 = *(LAS u32x4*)(lds + xr + 8 * 144);
      __builtin_amdgcn_raw_buffer_store_b128(r0, ro, oo, 0, OUT_AUX);
      __builtin_amdgcn_raw_buffer_store_b128(r1, ro, oo + 2048, 0, OUT_AUX);
    }
    DIAG_OUT(4);
    return;
  }

  const int ko0 = g * 256 + ((c ^ (g << 1)) << 4), ko1 = ko0 ^ 128;
  const int vo0 = (lane >> 3) * 256 + half * 128 + (((lane & 7) ^ ((lane >> 3) & 6)) << 4);
  const int vo1 = vo0 + 1024, vo2 = vo0 ^ 128, vo3 = vo2 + 1024;
  const __amdgpu_buffer_rsrc_t rk = __builtin_amdgcn_make_buffer_rsrc((char*)K + (size_t)pair * (SK * 256), 0, KV_BYTES, 0x00020000);
  const __amdgpu_buffer_rsrc_t rq = __builtin_amdgcn_make_buffer_rsrc((char*)Q + (size_t)pair * KT_BYTES, 0, KT_BYTES, 0x00020000);
  const __amdgpu_buffer_rsrc_t rv = __builtin_amdgcn_make_buffer_rsrc((char*)V + (size_t)pair * (SK * 256), 0, KV_BYTES, 0x00020000);
  const unsigned lbase = (unsigned)(uintptr_t)lds;
#define KTILE(POL, r, so, dst) \
    asm volatile("s_mov_b32 m0, %0\n\ts_nop 0\n\t" \
                 "buffer_load_dwordx4 %1, %3, %4 offen " POL " lds\n\t" \
                 "buffer_load_dwordx4 %2, %3, %4 offen offset:1024 " POL " lds\n\t" \
                 "buffer_load_dwordx4 %1, %3, %4 offen offset:2048 " POL " lds\n\t" \
                 "buffer_load_dwordx4 %2, %3, %4 offen offset:3072 " POL " lds" \
                 :: "s"(lbase + (dst) + wave * 4096), "v"(ko0), "v"(ko1), "s"(r), "s"((so) + wave * 4096) : "memory")
#define VHALF(a, b, so, dst) \
    asm volatile("s_mov_b32 m0, %0\n\ts_nop 0\n\t" \
                 "buffer_load_dwordx4 %1, %3, %4 offen nt lds\n\t" \
                 "buffer_load_dwordx4 %2, %3, %4 offen offset:1024 nt lds" \
                 :: "s"(lbase + (dst) + wave * 2048), "v"(a), "v"(b), "s"(rv), "s"((so) + wave * 4096) : "memory")
#define SLOT(s) (((s) & 3) * SLOT_BYTES)
#define OFF1(t) ((2 * (t) + half) * KT_BYTES)
#define OFF2(t) ((NA + (t)) * KT_BYTES)
#define K1(t) KTILE("nt", rk, OFF1(t), SLOT(t))
#define K2(t) KTILE(K2_POL, rk, OFF2(t), SLOT(t))
#define V1(t) do { VHALF(vo0, vo1, OFF1(t), SLOT((t) + 2) + KT_BYTES); VHALF(vo2, vo3, OFF1(t), SLOT((t) + 2) + KT_BYTES + VH_BYTES); } while (0)
#define V2(t) VHALF(vo0, vo1, OFF2(t), SLOT((t) + 2) + KT_BYTES)
  unsigned due = tstart + PACE0;
#define PACE_WAIT(d) do { while ((int)((unsigned)__builtin_amdgcn_s_memrealtime() - due) < 0) __builtin_amdgcn_s_sleep(1); due += (d); } while (0)
#define PACE_WAIT2(d)
  K1(0); KTILE(K2_POL, rq, 0, Q_OFF); K1(1); V1(0);
  wait_vm<12>(); K1(2);
  wait_vm<12>(); V1(1); LBARRIER();
  wait_vm<12>(); K1(3); LBARRIER();
#pragma unroll 1
  for (int s = 2; s < NA - 2; ++s) {
    wait_vm<12>(); PACE_WAIT(PACE / 2); V1(s);
    wait_vm<12>(); PACE_WAIT(PACE / 2); K1(s + 2); LBARRIER();
  }
  wait_vm<12>(); PACE_WAIT(PACE / 2); V1(NA - 2); wait_vm<12>(); PACE_WAIT(PACE / 4); K2(NA); LBARRIER();
  wait_vm<12>(); PACE_WAIT2(PACE / 2); V1(NA - 1); wait_vm<12>(); PACE_WAIT2(PACE / 4); K2(NA + 1); LBARRIER();
  wait_vm<12>(); V2(NA); wait_vm<10>(); K2(NA + 2); LBARRIER();
  wait_vm<10>(); V2(NA + 1); wait_vm<8>(); K2(NA + 3); LBARRIER();
#pragma unroll 1
  for (int s = NA + 2; s < NI - 2; ++s) {
    wait_vm<10>(); V2(s);
    wait_vm<8>(); K2(s + 2); LBARRIER();
  }
  V2(NI - 2);
  wait_vm<8>(); LBARRIER();
  V2(NI - 1);
  wait_vm<4>(); LBARRIER();
  wait_vm<2>(); LBARRIER();
  wait_vm<0>(); LBARRIER();
  DIAG_OUT(0);
}

}

extern "C" void kernel_launch(void* const* d_in, const int* in_sizes, int n_in, void* d_out, int out_size, void* d_ws, size_t ws_size,
                              hipStream_t stream) {
  gqa_attn<<<2 * NPAIR, NTHREADS, 0, stream>>>((const __hip_bfloat16*)d_in[0], (const __hip_bfloat16*)d_in[1], (const __hip_bfloat16*)d_in[2],
                                                (__hip_bfloat16*)d_out, (unsigned char*)d_ws);
}
```

```cpp
#define MAP 4
#define PBASE 0
#define PACE0 380
#include <hip/hip_runtime.h>
#include <hip/hip_bf16.h>
#include <stdint.h>

#define NA 12
#define K2_POL "nt"
#define OUT_AUX 18
#define KV_BYTES (SK * 256)
#define PACE 108
#define NOCOMP 0
#define NOX 0
#define FLAG_LAG 2
#define NLOAD 4
#define PRIO 0
#define PACEK 250
#define PACEM 2000
#define MINSP 0
#define SLIP 0
#define STOUCH 0
#define ORD 0
#define ROT 0
#define L1 1
#define L2 2
#define L3 2
#define ST_POL "sc1"
#define LD_POL "sc1"
#define BARFIRST 0
#define PACE_LAST 0

namespace {

constexpr int B_ = 16, HKV = 8, SQ = 16, SK = 2048;
constexpr int TK = 64, NT = SK / TK;
constexpr int NB = NT - 2 * NA, NI = NA + NB;
constexpr int NPAIR = B_ * HKV;
constexpr int KT_BYTES = TK * 256, VH_BYTES = TK * 128;
constexpr int SLOT_BYTES = KT_BYTES + 2 * VH_BYTES;
constexpr int RING_BYTES = 4 * SLOT_BYTES;
constexpr int Q_OFF = RING_BYTES;
constexpr int XCH_BYTES = 4096;
constexpr int LDS_BYTES = RING_BYTES + 4 * XCH_BYTES;
constexpr int NTHREADS = 576;
constexpr float C2 = 0.08838834764831845f * 1.4426950408889634f;
constexpr float SLACK = 7.0f;
constexpr unsigned GE2 = 0x40004000u;
constexpr int FLAG_OFF = 8192, EPOCH_OFF = FLAG_OFF + 1024;
constexpr int BOX_BYTES = EPOCH_OFF + 256;
constexpr unsigned TAG_STEP = 0x9E3779B1u;
static_assert(NB >= 6 && NA >= 4, "the swap needs a few tiles to hide under");

typedef __attribute__((ext_vector_type(8))) __bf16 bf16x8;
typedef __attribute__((ext_vector_type(4))) __bf16 bf16x4;
typedef __attribute__((ext_vector_type(2))) __bf16 bf16x2;
typedef __attribute__((ext_vector_type(4))) float f32x4;
typedef __attribute__((ext_vector_type(2))) float f32x2;
typedef __attribute__((ext_vector_type(4))) unsigned u32x4;
typedef __attribute__((ext_vector_type(2))) unsigned u32x2;
#define LAS __attribute__((address_space(3)))

#define BARRIER() asm volatile("s_waitcnt lgkmcnt(0)\n\ts_barrier" ::: "memory")
#define LBARRIER() asm volatile("s_barrier" ::: "memory")
#define DIAG_OUT(code)
#define RSTAMP(i)
#define SB() __builtin_amdgcn_sched_barrier(0)

template <int N> __device__ __forceinline__ void wait_vm() { asm volatile("s_waitcnt vmcnt(%0)" ::"n"(N) : "memory"); }

__device__ __forceinline__ float xmax4(float x) {
  auto s = __builtin_amdgcn_permlane16_swap(__float_as_uint(x), __float_as_uint(x), false, false);
  x = fmaxf(__uint_as_float(s[0]), __uint_as_float(s[1]));
  auto t = __builtin_amdgcn_permlane32_swap(__float_as_uint(x), __float_as_uint(x), false, false);
  return fmaxf(__uint_as_float(t[0]), __uint_as_float(t[1]));
}
__device__ __forceinline__ unsigned pk(float a, float b) { f32x2 v = {a, b}; return __builtin_bit_cast(unsigned, __builtin_convertvector(v, bf16x2)); }
__device__ __forceinline__ f32x4 mfma(u32x4 a, u32x4 b, f32x4 c) {
  return __builtin_amdgcn_mfma_f32_16x16x32_bf16(__builtin_bit_cast(bf16x8, a), __builtin_bit_cast(bf16x8, b), c, 0, 0, 0);
}
__device__ __forceinline__ u32x2 trd(LAS char* p) { return __builtin_bit_cast(u32x2, __builtin_amdgcn_ds_read_tr16_b64_v4bf16((LAS bf16x4*)p)); }

__global__ void __launch_bounds__(NTHREADS, 3)
gqa_attn(const __hip_bfloat16* __restrict__ Q, const __hip_bfloat16* __restrict__ K, const __hip_bfloat16* __restrict__ V,
         __hip_bfloat16* __restrict__ O, unsigned char* __restrict__ ws) {
  __shared__ __attribute__((aligned(1024))) char smem[LDS_BYTES];
  LAS char* lds = (LAS char*)smem;
  const unsigned tstart = (unsigned)__builtin_amdgcn_s_memrealtime();
  const int lane = threadIdx.x & 63;
  const int wave = __builtin_amdgcn_readfirstlane(threadIdx.x >> 6);
  const int pair = blockIdx.x & (NPAIR - 1), half = blockIdx.x >> 7;
  const int g = lane >> 4, c = lane & 15;

  if (__builtin_expect(wave >= NLOAD, 0)) {
    if (wave == 8) {
      if (NOX || NOCOMP) return;
      const unsigned char* wp = ws + (size_t)pair * (2 * BOX_BYTES);
      const int bo = half * BOX_BYTES, bi = (half ^ 1) * BOX_BYTES;
      unsigned ep_out, ep_in;
      asm volatile("s_load_dword %0, %2, %3\n\ts_load_dword %1, %2, %4\n\ts_waitcnt lgkmcnt(0)" : "=&s"(ep_out), "=&s"(ep_in) : "s"(wp), "s"(bo + EPOCH_OFF), "s"(bi + EPOCH_OFF) : "memory");
      const __amdgpu_buffer_rsrc_t rp = __builtin_amdgcn_make_buffer_rsrc((void*)wp, 0, 2 * BOX_BYTES, 0x00020000);
      const unsigned xb = (unsigned)(uintptr_t)lds + Q_OFF;
#pragma unroll 1
      for (int j = 0; j < NA + 3; ++j) asm volatile("s_barrier" ::: "memory");
      int spins = 1 << 20;
      unsigned long long tmp64;
#define BAR "s_barrier\n\t"
#define BAR0
#define BAR1 BAR
#define BAR2 BAR BAR
#define BAR3 BAR BAR BAR
#define BAR4 BAR BAR BAR BAR
#define BAR5 BAR BAR BAR BAR BAR
#define BAR6 BAR BAR BAR BAR BAR BAR
#define BARS_(n) BAR##n
#define BARS(n) BARS_(n)
      static_assert(L1 + L2 + L3 + 3 == NB, "barrier count");
#define FLAG_LOAD "buffer_load_dwordx4 v[48:51], %[po], %[rp], %[fi] offen sc1\n\t"
#define FLAG_TEST "v_cmp_ne_u32 vcc, %[tin], v49\n\tv_cmp_ne_u32 %[t], %[tin], v51\n\ts_nop 3\n\ts_or_b64 vcc, vcc, %[t]\n\t"
#define PART_IO(op, s0, s1, pol) \
      op " v[16:19], %[po], %[rp], " s0 " offen " pol "\n\t" op " v[20:23], %[po], %[rp], " s0 " offen offset:1024 " pol "\n\t" \
      op " v[24:27], %[po], %[rp], " s0 " offen offset:2048 " pol "\n\t" op " v[28:31], %[po], %[rp], " s0 " offen offset:3072 " pol "\n\t" \
      op " v[32:35], %[po], %[rp], " s1 " offen " pol "\n\t" op " v[36:39], %[po], %[rp], " s1 " offen offset:1024 " pol "\n\t" \
      op " v[40:43], %[po], %[rp], " s1 " offen offset:2048 " pol "\n\t" op " v[44:47], %[po], %[rp], " s1 " offen offset:3072 " pol "\n\t"
      asm volatile(
          "ds_read_b128 v[16:19], %[xi]\n\tds_read_b128 v[20:23], %[xi] offset:1024\n\t"
          "ds_read_b128 v[24:27], %[xi] offset:4096\n\tds_read_b128 v[28:31], %[xi] offset:5120\n\t"
          "ds_read_b128 v[32:35], %[xi] offset:8192\n\tds_read_b128 v[36:39], %[xi] offset:9216\n\t"
          "ds_read_b128 v[40:43], %[xi] offset:12288\n\tds_read_b128 v[44:47], %[xi] offset:13312\n\t"
          "ds_read_b128 v[48:51], %[xs]\n\t"
          "s_waitcnt lgkmcnt(0)\n\t"
          PART_IO("buffer_store_dwordx4", "%[bo]", "%[bo2]", ST_POL)
          "v_mov_b32 v49, %[tout]\n\tv_mov_b32 v51, %[tout]\n\t"
          BARS(L1)
          "s_waitcnt vmcnt(0)\n\t"
          "buffer_store_dwordx4 v[48:51], %[po], %[rp], %[fo] offen sc1\n\t"
          BARS(L2)
          FLAG_LOAD
          BARS(L3)
          "s_waitcnt vmcnt(0)\n\t"
          FLAG_TEST
          "s_cbranch_vccnz 1f\n\t"
          PART_IO("buffer_load_dwordx4", "%[bi]", "%[bi2]", LD_POL)
          BAR
          "3:\n\t"
          "ds_write_b128 %[xs], v[48:51]\n\t"
          "s_waitcnt vmcnt(0)\n\t"
          "ds_write_b128 %[xi], v[16:19]\n\tds_write_b128 %[xi], v[20:23] offset:1024\n\t"
          "ds_write_b128 %[xi], v[24:27] offset:4096\n\tds_write_b128 %[xi], v[28:31] offset:5120\n\t"
          "ds_write_b128 %[xi], v[32:35] offset:8192\n\tds_write_b128 %[xi], v[36:39] offset:9216\n\t"
          "ds_write_b128 %[xi], v[40:43] offset:12288\n\tds_write_b128 %[xi], v[44:47] offset:13312\n\t"
          "s_waitcnt lgkmcnt(0)\n\t" BAR
          "buffer_store_dword v49, %[eo], %[rp], %[bi] offen sc1\n\t"
          "s_branch 4f\n"
          "1:\n\t"
          FLAG_LOAD
          BAR
          "2:\n\t"
          "s_waitcnt vmcnt(0)\n\t"
          FLAG_TEST
          "s_cbranch_vccz 5f\n\t"
          FLAG_LOAD
          "s_sub_u32 %[spins], %[spins], 1\n\t"
          "s_cbranch_scc0 2b\n\t"
          "s_waitcnt vmcnt(0)\n\t"
          "v_mov_b32 v50, 0x7fc00000\n"
          "5:\n\t"
          "s_sub_u32 %[spins], %[spins], 1\n\t"
          PART_IO("buffer_load_dwordx4", "%[bi]", "%[bi2]", LD_POL)
          "s_branch 3b\n"
          "4:"
          : [spins] "+s"(spins), [t] "=&s"(tmp64)
          : [tin] "s"(ep_in + TAG_STEP), [tout] "s"(ep_out + TAG_STEP), [bi] "s"(bi), [bi2] "s"(bi + 4096), [fi] "s"(bi + FLAG_OFF),
            [bo] "s"(bo), [bo2] "s"(bo + 4096), [fo] "s"(bo + FLAG_OFF), [po] "v"(lane * 16), [eo] "v"(lane == 0 ? EPOCH_OFF : 2 * BOX_BYTES),
            [xi] "v"(xb + lane * 16), [xs] "v"(xb + g * XCH_BYTES + 2048 + c * 16), [rp] "s"(rp)
          : "memory", "vcc", "scc", "v16", "v17", "v18", "v19", "v20", "v21", "v22", "v23", "v24", "v25", "v26", "v27", "v28", "v29", "v30", "v31",
            "v32", "v33", "v34", "v35", "v36", "v37", "v38", "v39", "v40", "v41", "v42", "v43", "v44", "v45", "v46", "v47", "v48", "v49", "v50", "v51");
      DIAG_OUT(spins == (1 << 20) ? 2 : spins == (1 << 20) - 1 ? 3 : 5);
      return;
    }
    const int head = wave - NLOAD;
    if (PRIO) __builtin_amdgcn_s_setprio(PRIO);
    const __amdgpu_buffer_rsrc_t ro = __builtin_amdgcn_make_buffer_rsrc((char*)O + ((size_t)pair * 4 + head) * (SQ * 256), 0, SQ * 256, 0x00020000);
    const unsigned ka0 = c * 256 + ((g ^ ((c & 7) << 1)) << 4);
    const int vr = 4 * g + (c >> 2);
    const unsigned va0 = KT_BYTES + vr * 128 + (((vr >> 1) & 3) << 5) + (c & 3) * 8;
    unsigned xi = Q_OFF + head * XCH_BYTES + lane * 16;
    u32x4 ones = {0x3F803F80u, 0x3F803F80u, 0x3F803F80u, 0x3F803F80u};
    asm volatile("" : "+v"(ones), "+v"(xi));
    f32x4 o[8], lsum = {0.f, 0.f, 0.f, 0.f};
#pragma unroll
    for (int db = 0; db < 8; ++db) { o[db] = f32x4{0.f, 0.f, 0.f, 0.f}; asm volatile("" : "+v"(o[db])); }
    float nm = 3.0e38f;
    unsigned sb = SLOT_BYTES;
    unsigned kx[4];
#pragma unroll
    for (int st = 0; st < 4; ++st) kx[st] = (ka0 ^ (st << 6)) + sb;
    BARRIER();
    u32x4 q[4], kf[4][4];
#pragma unroll
    for (int st = 0; st < 4; ++st) q[st] = *(const LAS u32x4*)(lds + (Q_OFF + head * 4096) + (ka0 ^ (st << 6)));
#pragma unroll
    for (int sub = 0; sub < 4; ++sub)
#pragma unroll
      for (int st = 0; st < 4; ++st) kf[sub][st] = *(const LAS u32x4*)(lds + (ka0 ^ (st << 6)) + sub * 4096);
    asm volatile("" : "+v"(kf[0][0]), "+v"(kf[0][1]), "+v"(kf[0][2]), "+v"(kf[0][3]), "+v"(kf[1][0]), "+v"(kf[1][1]), "+v"(kf[1][2]), "+v"(kf[1][3]),
                 "+v"(kf[2][0]), "+v"(kf[2][1]), "+v"(kf[2][2]), "+v"(kf[2][3]), "+v"(kf[3][0]), "+v"(kf[3][1]), "+v"(kf[3][2]), "+v"(kf[3][3]),
                 "+v"(q[0]), "+v"(q[1]), "+v"(q[2]), "+v"(q[3]));
    BARRIER();
    int left = NI;
    asm volatile("" : "+s"(left));
#pragma unroll 1
    do {
      f32x4 s[4], x[4];
      u32x4 p[2];
      u32x2 vf[8][2];
      const f32x4 zero = {0.f, 0.f, 0.f, 0.f};
      sb = (sb + SLOT_BYTES) & (RING_BYTES - 1);
      unsigned vx[4];
#define QK1(sub, st) do { s[sub] = mfma(kf[sub][st], q[st], (st) ? s[sub] : zero); kf[sub][st] = *(const LAS u32x4*)(lds + kx[st] + (sub) * 4096); } while (0)
#define FM(sub) x[sub] = s[sub] * C2 + nm
#define EX(sub, r) x[sub][r] = __builtin_amdgcn_exp2f(x[sub][r])
#define CV(sub, h) p[(sub) >> 1][2 * ((sub) & 1) + (h)] = pk(x[sub][2 * (h)], x[sub][2 * (h) + 1])
#define VX(i) do { vx[i] = (i) ? vx[0] ^ ((i) << 5) : va0 + sb; asm volatile("" : "+v"(vx[i])); } while (0)
#define VRD(i, off) do { vf[i][0] = trd(lds + vx[(i) & 3] + ((i) >> 2) * 4096 + (off)); vf[i][1] = trd(lds + vx[(i) & 3] + ((i) >> 2) * 4096 + 2048 + (off)); } while (0)
#define PV1(i, ob) o[(ob) + ((i) & 3)] = mfma(u32x4{vf[i][0][0], vf[i][0][1], vf[i][1][0], vf[i][1][1]}, p[(i) >> 2], o[(ob) + ((i) & 3)])
      QK1(0, 0); SB(); QK1(1, 0); VX(0); SB(); QK1(0, 1); VX(1); SB(); QK1(1, 1); VX(2); SB();
      QK1(0, 2); VX(3); SB(); QK1(1, 2); SB(); QK1(0, 3); SB(); QK1(1, 3); SB();
      QK1(2, 0); SB(); QK1(3, 0); FM(0); SB();
      QK1(2, 1); FM(1); EX(0, 0); SB(); QK1(3, 1); EX(0, 1); EX(0, 2); SB();
      QK1(2, 2); EX(0, 3); EX(1, 0); SB(); QK1(3, 2); EX(1, 1); EX(1, 2); SB();
      QK1(2, 3); EX(1, 3); CV(0, 0); SB(); QK1(3, 3); CV(0, 1); CV(1, 0); CV(1, 1); SB();
      unsigned any = (p[0][0] | p[0][1] | p[0][2]) | p[0][3];
      asm volatile("" : "+v"(any) :: "memory");
      SB();
      FM(2); FM(3); SB();
      EX(2, 0); EX(2, 1); EX(2, 2); EX(2, 3); EX(3, 0); EX(3, 1); EX(3, 2); EX(3, 3); SB();
      CV(2, 0); CV(2, 1); CV(3, 0); CV(3, 1);
      any = (any | p[1][0] | p[1][1]) | p[1][2] | p[1][3];
      if (__builtin_expect(__builtin_amdgcn_ballot_w64((any & GE2) != 0) != 0, 0)) {
        float t = s[0][0];
#pragma unroll
        for (int sub = 0; sub < 4; ++sub)
#pragma unroll
          for (int r = 0; r < 4; ++r) t = fmaxf(t, s[sub][r]);
        const float nn = fminf(nm, -fmaf(xmax4(t), C2, SLACK));
        const float alpha = __builtin_amdgcn_exp2f(nn - nm);
        nm = nn;
#pragma unroll
        for (int db = 0; db < 8; ++db) o[db] *= alpha;
        lsum *= alpha;
#pragma unroll
        for (int sub = 0; sub < 4; ++sub) { FM(sub); EX(sub, 0); EX(sub, 1); EX(sub, 2); EX(sub, 3); CV(sub, 0); CV(sub, 1); }
      }
      BARRIER();
      VRD(0, 0); SB(); VRD(1, 0); SB(); VRD(2, 0); SB(); VRD(3, 0); SB(); VRD(4, 0); SB(); VRD(5, 0); SB();
      lsum = mfma(ones, p[0], lsum);
      lsum = mfma(ones, p[1], lsum);
      SB();
#define KX(i) do { kx[i] = (i) ? kx[0] ^ ((i) << 6) : ka0 + sb; asm volatile("" : "+v"(kx[i])); } while (0)
      PV1(0, 0); SB(); VRD(6, 0); SB(); PV1(1, 0); SB(); VRD(7, 0); SB(); PV1(2, 0); SB(); PV1(3, 0); KX(0); SB();
      PV1(4, 0); KX(1); SB(); PV1(5, 0); KX(2); SB(); PV1(6, 0); KX(3); SB(); PV1(7, 0); SB();
      if (__builtin_expect(left > NB, 0)) {
        VRD(0, VH_BYTES); SB(); VRD(1, VH_BYTES); SB(); VRD(2, VH_BYTES); SB(); VRD(3, VH_BYTES); SB(); VRD(4, VH_BYTES); SB();
        PV1(0, 4); SB(); VRD(5, VH_BYTES); SB(); PV1(1, 4); SB(); VRD(6, VH_BYTES); SB(); PV1(2, 4); SB(); VRD(7, VH_BYTES); SB(); PV1(3, 4); PV1(4, 4); PV1(5, 4); PV1(6, 4); PV1(7, 4); SB();
        if (left == NB + 1) {
          *(LAS f32x4*)(lds + xi + 2048) = f32x4{nm, 0.f, lsum[0], 0.f};
#pragma unroll
          for (int i = 0; i < 2; ++i) {
            const f32x4 a = o[4 + 2 * i], b = o[5 + 2 * i];
            *(LAS u32x4*)(lds + xi + i * 1024) = u32x4{pk(a[0], a[1]), pk(a[2], a[3]), pk(b[0], b[1]), pk(b[2], b[3])};
          }
        }
      }
    } while (--left != 0);
    RSTAMP(52);
    {
      asm volatile("" : "+v"(xi));
      const unsigned ln = (xi >> 4) & 63, xb = Q_OFF + head * XCH_BYTES;
      const unsigned xw = xb + (ln & 15) * 144 + (ln >> 4) * 8, xr = xb + (ln >> 3) * 144 + (ln & 7) * 16;
      const int oo = (ln >> 3) * 256 + half * 128 + (ln & 7) * 16;
      const f32x4 st = *(LAS f32x4*)(lds + (xi & ~0x300u) + 2048);
      const u32x4 yy[2] = {*(LAS u32x4*)(lds + xi), *(LAS u32x4*)(lds + xi + 1024)};
      SB();
      f32x4 y[4];
#pragma unroll
      for (int i = 0; i < 4; ++i) {
        const unsigned y0 = yy[i >> 1][2 * (i & 1)], y1 = yy[i >> 1][2 * (i & 1) + 1];
        y[i] = f32x4{__uint_as_float(y0 << 16), __uint_as_float(y0 & 0xFFFF0000u), __uint_as_float(y1 << 16), __uint_as_float(y1 & 0xFFFF0000u)};
      }
      const float nn = fminf(nm, st[0]);
      const float b1 = __builtin_amdgcn_exp2f(nn - nm), b2 = __builtin_amdgcn_exp2f(nn - st[0]);
      const float inv = __builtin_amdgcn_rcpf(fmaf(b1, lsum[0], b2 * st[2]));
      const float w1 = b1 * inv, w2 = b2 * inv;
#pragma unroll
      for (int i = 0; i < 4; ++i) {
        const f32x4 r = o[i] * w1 + y[i] * w2;
        *(LAS u32x2*)(lds + xw + i * 32) = u32x2{pk(r[0], r[1]), pk(r[2], r[3])};
      }
      asm volatile("" ::: "memory");
      const u32x4 r0 = *(LAS u32x4*)(lds + xr), r1 = *(LAS u32x4*)(lds + xr + 8 * 144);
      __builtin_amdgcn_raw_buffer_store_b128(r0, ro, oo, 0, OUT_AUX);
      __builtin_amdgcn_raw_buffer_store_b128(r1, ro, oo + 2048, 0, OUT_AUX);
    }
    DIAG_OUT(4);
    return;
  }

  const int ko0 = g * 256 + ((c ^ (g << 1)) << 4), ko1 = ko0 ^ 128;
  const int vo0 = (lane >> 3) * 256 + half * 128 + (((lane & 7) ^ ((lane >> 3) & 6)) << 4);
  const int vo1 = vo0 + 1024, vo2 = vo0 ^ 128, vo3 = vo2 + 1024;
  const __amdgpu_buffer_rsrc_t rk = __builtin_amdgcn_make_buffer_rsrc((char*)K + (size_t)pair * (SK * 256), 0, KV_BYTES, 0x00020000);
  const __amdgpu_buffer_rsrc_t rq = __builtin_amdgcn_make_buffer_rsrc((char*)Q + (size_t)pair * KT_BYTES, 0, KT_BYTES, 0x00020000);
  const __amdgpu_buffer_rsrc_t rv = __builtin_amdgcn_make_buffer_rsrc((char*)V + (size_t)pair * (SK * 256), 0, KV_BYTES, 0x00020000);
  const unsigned lbase = (unsigned)(uintptr_t)lds;
#define KTILE(POL, r, so, dst) \
    asm volatile("s_mov_b32 m0, %0\n\ts_nop 0\n\t" \
                 "buffer_load_dwordx4 %1, %3, %4 offen " POL " lds\n\t" \
                 "buffer_load_dwordx4 %2, %3, %4 offen offset:1024 " POL " lds\n\t" \
                 "buffer_load_dwordx4 %1, %3, %4 offen offset:2048 " POL " lds\n\t" \
                 "buffer_load_dwordx4 %2, %3, %4 offen offset:3072 " POL " lds" \
                 :: "s"(lbase + (dst) + wave * 4096), "v"(ko0), "v"(ko1), "s"(r), "s"((so) + wave * 4096) : "memory")
#define VHALF(a, b, so, dst) \
    asm volatile("s_mov_b32 m0, %0\n\ts_nop 0\n\t" \
                 "buffer_load_dwordx4 %1, %3, %4 offen nt lds\n\t" \
                 "buffer_load_dwordx4 %2, %3, %4 offen offset:1024 nt lds" \
                 :: "s"(lbase + (dst) + wave * 2048), "v"(a), "v"(b), "s"(rv), "s"((so) + wave * 4096) : "memory")
#define SLOT(s) (((s) & 3) * SLOT_BYTES)
  const int rot = 0;
#define OFF1(t) (((2 * (t) + half + rot) & (NT - 1)) * KT_BYTES)
#define OFF2(t) (((NA + (t) + rot) & (NT - 1)) * KT_BYTES)
#define K1(t) KTILE("nt", rk, OFF1(t), SLOT(t))
#define K2(t) KTILE(K2_POL, rk, OFF2(t), SLOT(t))
#define V1(t) do { VHALF(vo0, vo1, OFF1(t), SLOT((t) + 2) + KT_BYTES); VHALF(vo2, vo3, OFF1(t), SLOT((t) + 2) + KT_BYTES + VH_BYTES); } while (0)
#define V2(t) VHALF(vo0, vo1, OFF2(t), SLOT((t) + 2) + KT_BYTES)
  unsigned due = tstart + PACE0;
#define PACE_WAIT(d) do { while ((int)((unsigned)__builtin_amdgcn_s_memrealtime() - due) < 0) __builtin_amdgcn_s_sleep(1); due += (d); } while (0)
#define PACE_WAIT2(d)
  K1(0); KTILE(K2_POL, rq, 0, Q_OFF); K1(1); V1(0);
  wait_vm<12>(); K1(2);
  wait_vm<12>(); V1(1); LBARRIER();
  wait_vm<12>(); K1(3); LBARRIER();
#pragma unroll 1
  for (int s = 2; s < NA - 2; ++s) {
    wait_vm<12>(); PACE_WAIT(PACE / 2); V1(s);
    wait_vm<12>(); PACE_WAIT(PACE / 2); K1(s + 2); LBARRIER();
  }
  wait_vm<12>(); PACE_WAIT(PACE / 2); V1(NA - 2); wait_vm<12>(); PACE_WAIT(PACE / 4); K2(NA); LBARRIER();
  wait_vm<12>(); PACE_WAIT2(PACE / 2); V1(NA - 1); wait_vm<12>(); PACE_WAIT2(PACE / 4); K2(NA + 1); LBARRIER();
  wait_vm<12>(); V2(NA); wait_vm<10>(); K2(NA + 2); LBARRIER();
  wait_vm<10>(); V2(NA + 1); wait_vm<8>(); K2(NA + 3); LBARRIER();
#pragma unroll 1
  for (int s = NA + 2; s < NI - 2; ++s) {
    wait_vm<10>(); V2(s);
    wait_vm<8>(); K2(s + 2); LBARRIER();
  }
  V2(NI - 2);
  wait_vm<8>(); LBARRIER();
  V2(NI - 1);
  wait_vm<4>(); LBARRIER();
  wait_vm<2>(); LBARRIER();
  wait_vm<0>(); LBARRIER();
  DIAG_OUT(0);
}

}

extern "C" void kernel_launch(void* const* d_in, const int* in_sizes, int n_in, void* d_out, int out_size, void* d_ws, size_t ws_size,
                              hipStream_t stream) {
  gqa_attn<<<2 * NPAIR, NTHREADS, 0, stream>>>((const __hip_bfloat16*)d_in[0], (const __hip_bfloat16*)d_in[1], (const __hip_bfloat16*)d_in[2],
                                                (__hip_bfloat16*)d_out, (unsigned char*)d_ws);
}
```

```cpp
#define TRANS 0
#define BARFIRST 0
#include <hip/hip_runtime.h>
#include <hip/hip_bf16.h>
#include <stdint.h>

#define NA 12
#define K2_POL "nt"
#define OUT_AUX 18
#define KV_BYTES (SK * 256)
#define PACE 108
#define PACE0 360
#define NOCOMP 0
#define NOX 0
#define FLAG_LAG 2
#define NLOAD 4
#define PREREAD 0
#define PRE_POL ""
#define P2ORD 0
#define PRO 3
#define PROBAR 1
#define VTOUCH 1
#define PRIO 0
#define PACEK 250
#define PACEM 2000
#define MINSP 0
#define SLIP 0
#define STOUCH 0
#define ORD 0
#define MAP 4
#define ROT 0
#define PBASE 0
#define L1 1
#define L2 3
#define L3 1
#define ST_POL "sc1"
#define LD_POL "sc1"
#define PACE_LAST 0

namespace {

constexpr int B_ = 16, HKV = 8, SQ = 16, SK = 2048;
constexpr int TK = 64, NT = SK / TK;
constexpr int NB = NT - 2 * NA, NI = NA + NB;
constexpr int NPAIR = B_ * HKV;
constexpr int KT_BYTES = TK * 256, VH_BYTES = TK * 128;
constexpr int SLOT_BYTES = KT_BYTES + 2 * VH_BYTES;
constexpr int RING_BYTES = 4 * SLOT_BYTES;
constexpr int Q_OFF = RING_BYTES;
constexpr int XCH_BYTES = 4096;
constexpr int LDS_BYTES = RING_BYTES + 4 * XCH_BYTES;
constexpr int NTHREADS = 576;
constexpr float C2 = 0.08838834764831845f * 1.4426950408889634f;
constexpr float SLACK = 7.0f;
constexpr unsigned GE2 = 0x40004000u;
constexpr int FLAG_OFF = 8192, EPOCH_OFF = FLAG_OFF + 1024;
constexpr int BOX_BYTES = EPOCH_OFF + 256;
constexpr unsigned TAG_STEP = 0x9E3779B1u;
static_assert(NB >= 6 && NA >= 4, "the swap needs a few tiles to hide under");

typedef __attribute__((ext_vector_type(8))) __bf16 bf16x8;
typedef __attribute__((ext_vector_type(4))) __bf16 bf16x4;
typedef __attribute__((ext_vector_type(2))) __bf16 bf16x2;
typedef __attribute__((ext_vector_type(4))) float f32x4;
typedef __attribute__((ext_vector_type(2))) float f32x2;
typedef __attribute__((ext_vector_type(4))) unsigned u32x4;
typedef __attribute__((ext_vector_type(2))) unsigned u32x2;
#define LAS __attribute__((address_space(3)))

#define BARRIER() asm volatile("s_waitcnt lgkmcnt(0)\n\ts_barrier" ::: "memory")
#define LBARRIER() asm volatile("s_barrier" ::: "memory")
#define DIAG_OUT(code)
#define RSTAMP(i)
#define SB() __builtin_amdgcn_sched_barrier(0)

template <int N> __device__ __forceinline__ void wait_vm() { asm volatile("s_waitcnt vmcnt(%0)" ::"n"(N) : "memory"); }

__device__ __forceinline__ float xmax4(float x) {
  auto s = __builtin_amdgcn_permlane16_swap(__float_as_uint(x), __float_as_uint(x), false, false);
  x = fmaxf(__uint_as_float(s[0]), __uint_as_float(s[1]));
  auto t = __builtin_amdgcn_permlane32_swap(__float_as_uint(x), __float_as_uint(x), false, false);
  return fmaxf(__uint_as_float(t[0]), __uint_as_float(t[1]));
}
__device__ __forceinline__ unsigned pk(float a, float b) { f32x2 v = {a, b}; return __builtin_bit_cast(unsigned, __builtin_convertvector(v, bf16x2)); }
__device__ __forceinline__ f32x4 mfma(u32x4 a, u32x4 b, f32x4 c) {
  return __builtin_amdgcn_mfma_f32_16x16x32_bf16(__builtin_bit_cast(bf16x8, a), __builtin_bit_cast(bf16x8, b), c, 0, 0, 0);
}
__device__ __forceinline__ u32x2 trd(LAS char* p) { return __builtin_bit_cast(u32x2, __builtin_amdgcn_ds_read_tr16_b64_v4bf16((LAS bf16x4*)p)); }

__global__ void __launch_bounds__(NTHREADS, 3)
gqa_attn(const __hip_bfloat16* __restrict__ Q, const __hip_bfloat16* __restrict__ K, const __hip_bfloat16* __restrict__ V,
         __hip_bfloat16* __restrict__ O, unsigned char* __restrict__ ws) {
  __shared__ __attribute__((aligned(1024))) char smem[LDS_BYTES];
  LAS char* lds = (LAS char*)smem;
  const unsigned tstart = (unsigned)__builtin_amdgcn_s_memrealtime();
  const int lane = threadIdx.x & 63;
  const int wave = __builtin_amdgcn_readfirstlane(threadIdx.x >> 6);
  const int pair = blockIdx.x & (NPAIR - 1), half = blockIdx.x >> 7;
  const int g = lane >> 4, c = lane & 15;

  if (__builtin_expect(wave >= NLOAD, 0)) {
    if (wave == 8) {
      if (NOX || NOCOMP) return;
      const unsigned char* wp = ws + (size_t)pair * (2 * BOX_BYTES);
      const int bo = half * BOX_BYTES, bi = (half ^ 1) * BOX_BYTES;
      unsigned ep_out, ep_in;
      asm volatile("s_load_dword %0, %2, %3\n\ts_load_dword %1, %2, %4\n\ts_waitcnt lgkmcnt(0)" : "=&s"(ep_out), "=&s"(ep_in) : "s"(wp), "s"(bo + EPOCH_OFF), "s"(bi + EPOCH_OFF) : "memory");
      const __amdgpu_buffer_rsrc_t rp = __builtin_amdgcn_make_buffer_rsrc((void*)wp, 0, 2 * BOX_BYTES, 0x00020000);
      const unsigned xb = (unsigned)(uintptr_t)lds + Q_OFF;
      {
#pragma unroll 1
        for (int j = 0; j < 6; ++j) asm volatile("s_barrier" ::: "memory");
        unsigned t0, t1;
        asm volatile("buffer_load_dword %0, off, %2, %3 offset:128\n\t"
                     "s_barrier\n\ts_barrier\n\ts_barrier\n\ts_waitcnt vmcnt(0)"
                     : "=&v"(t0), "=&v"(t1) : "s"(rp), "s"(bo + EPOCH_OFF), "v"(0), "s"((char*)O + (size_t)pair * (4 * SQ * 256)) : "memory");
#pragma unroll 1
        for (int j = 9; j < NA + 3; ++j) asm volatile("s_barrier" ::: "memory");
      }
      int spins = 1 << 20;
      unsigned long long tmp64;
#define BAR "s_barrier\n\t"
#define TS(n)
#define TS_CLOB
#define BAR0
#define BAR1 BAR
#define BAR2 BAR BAR
#define BAR3 BAR BAR BAR
#define BAR4 BAR BAR BAR BAR
#define BAR5 BAR BAR BAR BAR BAR
#define BAR6 BAR BAR BAR BAR BAR BAR
#define BARS_(n) BAR##n
#define BARS(n) BARS_(n)
      static_assert(L1 + L2 + L3 + 3 == NB, "barrier count");
#define FLAG_LOAD "buffer_load_dwordx4 v[48:51], %[po], %[rp], %[fi] offen sc1\n\t"
#define FLAG_TEST "v_cmp_ne_u32 vcc, %[tin], v49\n\tv_cmp_ne_u32 %[t], %[tin], v51\n\ts_nop 3\n\ts_or_b64 vcc, vcc, %[t]\n\t"
#define PART_IO(op, s0, s1, pol) \
      op " v[16:19], %[po], %[rp], " s0 " offen " pol "\n\t" op " v[20:23], %[po], %[rp], " s0 " offen offset:1024 " pol "\n\t" \
      op " v[24:27], %[po], %[rp], " s0 " offen offset:2048 " pol "\n\t" op " v[28:31], %[po], %[rp], " s0 " offen offset:3072 " pol "\n\t" \
      op " v[32:35], %[po], %[rp], " s1 " offen " pol "\n\t" op " v[36:39], %[po], %[rp], " s1 " offen offset:1024 " pol "\n\t" \
      op " v[40:43], %[po], %[rp], " s1 " offen offset:2048 " pol "\n\t" op " v[44:47], %[po], %[rp], " s1 " offen offset:3072 " pol "\n\t"
      asm volatile(
          TS(60)
          "ds_read_b128 v[16:19], %[xi]\n\tds_read_b128 v[20:23], %[xi] offset:1024\n\t"
          "ds_read_b128 v[24:27], %[xi] offset:4096\n\tds_read_b128 v[28:31], %[xi] offset:5120\n\t"
          "ds_read_b128 v[32:35], %[xi] offset:8192\n\tds_read_b128 v[36:39], %[xi] offset:9216\n\t"
          "ds_read_b128 v[40:43], %[xi] offset:12288\n\tds_read_b128 v[44:47], %[xi] offset:13312\n\t"
          "ds_read_b128 v[48:51], %[xs]\n\t"
          "s_waitcnt lgkmcnt(0)\n\t"
          PART_IO("buffer_store_dwordx4", "%[bo]", "%[bo2]", ST_POL)
          "v_mov_b32 v49, %[tout]\n\tv_mov_b32 v51, %[tout]\n\t" TS(62)
          BARS(L1) TS(64)
          "s_waitcnt vmcnt(0)\n\t" TS(66)
          "buffer_store_dwordx4 v[48:51], %[po], %[rp], %[fo] offen sc1\n\t"
          BARS(L2)
          FLAG_LOAD
          BARS(L3) TS(68)
          "s_waitcnt vmcnt(0)\n\t" TS(70)
          FLAG_TEST
          "s_cbranch_vccnz 1f\n\t"
          PART_IO("buffer_load_dwordx4", "%[bi]", "%[bi2]", LD_POL)
          BAR
          "3:\n\t"
          "ds_write_b128 %[xs], v[48:51]\n\t" TS(72)
          "s_waitcnt vmcnt(0)\n\t" TS(74)
          "ds_write_b128 %[xi], v[16:19]\n\tds_write_b128 %[xi], v[20:23] offset:1024\n\t"
          "ds_write_b128 %[xi], v[24:27] offset:4096\n\tds_write_b128 %[xi], v[28:31] offset:5120\n\t"
          "ds_write_b128 %[xi], v[32:35] offset:8192\n\tds_write_b128 %[xi], v[36:39] offset:9216\n\t"
          "ds_write_b128 %[xi], v[40:43] offset:12288\n\tds_write_b128 %[xi], v[44:47] offset:13312\n\t"
          "s_waitcnt lgkmcnt(0)\n\t" BAR
          "buffer_store_dword v49, %[eo], %[rp], %[bi] offen sc1\n\t"
          "s_branch 4f\n"
          "1:\n\t"
          FLAG_LOAD
          BAR
          "2:\n\t"
          "s_waitcnt vmcnt(0)\n\t"
          FLAG_TEST
          "s_cbranch_vccz 5f\n\t"
          FLAG_LOAD
          "s_sub_u32 %[spins], %[spins], 1\n\t"
          "s_cbranch_scc0 2b\n\t"
          "s_waitcnt vmcnt(0)\n\t"
          "v_mov_b32 v50, 0x7fc00000\n"
          "5:\n\t"
          "s_sub_u32 %[spins], %[spins], 1\n\t"
          PART_IO("buffer_load_dwordx4", "%[bi]", "%[bi2]", LD_POL)
          "s_branch 3b\n"
          "4:"
          : [spins] "+s"(spins), [t] "=&s"(tmp64)
          : [tin] "s"(ep_in + TAG_STEP), [tout] "s"(ep_out + TAG_STEP), [bi] "s"(bi), [bi2] "s"(bi + 4096), [fi] "s"(bi + FLAG_OFF),
            [bo] "s"(bo), [bo2] "s"(bo + 4096), [fo] "s"(bo + FLAG_OFF), [po] "v"(lane * 16), [eo] "v"(lane == 0 ? EPOCH_OFF : 2 * BOX_BYTES),
            [xi] "v"(xb + lane * 16), [xs] "v"(xb + g * XCH_BYTES + 2048 + c * 16), [rp] "s"(rp)
          : "memory", "vcc", "scc", "v16", "v17", "v18", "v19", "v20", "v21", "v22", "v23", "v24", "v25", "v26", "v27", "v28", "v29", "v30", "v31",
            "v32", "v33", "v34", "v35", "v36", "v37", "v38", "v39", "v40", "v41", "v42", "v43", "v44", "v45", "v46", "v47", "v48", "v49", "v50", "v51" TS_CLOB);
      DIAG_OUT(spins == (1 << 20) ? 2 : spins == (1 << 20) - 1 ? 3 : 5);
      return;
    }
    const int head = wave - NLOAD;
    if (PRIO) __builtin_amdgcn_s_setprio(PRIO);
    const __amdgpu_buffer_rsrc_t ro = __builtin_amdgcn_make_buffer_rsrc((char*)O + ((size_t)pair * 4 + head) * (SQ * 256), 0, SQ * 256, 0x00020000);
    const unsigned ka0 = c * 256 + ((g ^ ((c & 7) << 1)) << 4);
    const int vr = 4 * g + (c >> 2);
    const unsigned va0 = KT_BYTES + vr * 128 + (((vr >> 1) & 3) << 5) + (c & 3) * 8;
    unsigned xi = Q_OFF + head * XCH_BYTES + lane * 16;
    u32x4 ones = {0x3F803F80u, 0x3F803F80u, 0x3F803F80u, 0x3F803F80u};
    asm volatile("" : "+v"(ones), "+v"(xi));
    f32x4 o[8], lsum = {0.f, 0.f, 0.f, 0.f};
#pragma unroll
    for (int db = 0; db < 8; ++db) { o[db] = f32x4{0.f, 0.f, 0.f, 0.f}; asm volatile("" : "+v"(o[db])); }
    float nm = 3.0e38f;
    unsigned sb = SLOT_BYTES;
    unsigned kx[4];
#pragma unroll
    for (int st = 0; st < 4; ++st) kx[st] = (ka0 ^ (st << 6)) + sb;
    BARRIER();
    u32x4 q[4], kf[4][4];
#pragma unroll
    for (int st = 0; st < 4; ++st) q[st] = *(const LAS u32x4*)(lds + (Q_OFF + head * 4096) + (ka0 ^ (st << 6)));
#pragma unroll
    for (int sub = 0; sub < 4; ++sub)
#pragma unroll
      for (int st = 0; st < 4; ++st) kf[sub][st] = *(const LAS u32x4*)(lds + (ka0 ^ (st << 6)) + sub * 4096);
    asm volatile("" : "+v"(kf[0][0]), "+v"(kf[0][1]), "+v"(kf[0][2]), "+v"(kf[0][3]), "+v"(kf[1][0]), "+v"(kf[1][1]), "+v"(kf[1][2]), "+v"(kf[1][3]),
                 "+v"(kf[2][0]), "+v"(kf[2][1]), "+v"(kf[2][2]), "+v"(kf[2][3]), "+v"(kf[3][0]), "+v"(kf[3][1]), "+v"(kf[3][2]), "+v"(kf[3][3]),
                 "+v"(q[0]), "+v"(q[1]), "+v"(q[2]), "+v"(q[3]));
    BARRIER();
    int left = NI;
    asm volatile("" : "+s"(left));
#pragma unroll 1
    do {
      f32x4 s[4], x[4];
      u32x4 p[2];
      u32x2 vf[8][2];
      const f32x4 zero = {0.f, 0.f, 0.f, 0.f};
      sb = (sb + SLOT_BYTES) & (RING_BYTES - 1);
      unsigned vx[4];
#define QK1(sub, st) do { s[sub] = mfma(kf[sub][st], q[st], (st) ? s[sub] : zero); kf[sub][st] = *(const LAS u32x4*)(lds + kx[st] + (sub) * 4096); } while (0)
#define FM(sub) x[sub] = s[sub] * C2 + nm
#define EX(sub, r) x[sub][r] = __builtin_amdgcn_exp2f(x[sub][r])
#define CV(sub, h) p[(sub) >> 1][2 * ((sub) & 1) + (h)] = pk(x[sub][2 * (h)], x[sub][2 * (h) + 1])
#define VX(i) do { vx[i] = (i) ? vx[0] ^ ((i) << 5) : va0 + sb; asm volatile("" : "+v"(vx[i])); } while (0)
#define VRD(i, off) do { vf[i][0] = trd(lds + vx[(i) & 3] + ((i) >> 2) * 4096 + (off)); vf[i][1] = trd(lds + vx[(i) & 3] + ((i) >> 2) * 4096 + 2048 + (off)); } while (0)
#define PV1(i, ob) o[(ob) + ((i) & 3)] = mfma(u32x4{vf[i][0][0], vf[i][0][1], vf[i][1][0], vf[i][1][1]}, p[(i) >> 2], o[(ob) + ((i) & 3)])
      QK1(0, 0); SB(); QK1(1, 0); VX(0); SB(); QK1(0, 1); VX(1); SB(); QK1(1, 1); VX(2); SB();
      QK1(0, 2); VX(3); SB(); QK1(1, 2); SB(); QK1(0, 3); SB(); QK1(1, 3); SB();
      QK1(2, 0); SB(); QK1(3, 0); FM(0); SB();
      QK1(2, 1); FM(1); EX(0, 0); SB(); QK1(3, 1); EX(0, 1); EX(0, 2); SB();
      QK1(2, 2); EX(0, 3); EX(1, 0); SB(); QK1(3, 2); EX(1, 1); EX(1, 2); SB();
      QK1(2, 3); EX(1, 3); CV(0, 0); SB(); QK1(3, 3); CV(0, 1); CV(1, 0); CV(1, 1); SB();
      unsigned any = (p[0][0] | p[0][1] | p[0][2]) | p[0][3];
      asm volatile("" : "+v"(any) :: "memory");
      SB();
      FM(2); FM(3); SB();
      EX(2, 0); EX(2, 1); EX(2, 2); EX(2, 3); EX(3, 0); EX(3, 1); EX(3, 2); EX(3, 3); SB();
      CV(2, 0); CV(2, 1); CV(3, 0); CV(3, 1);
      any = (any | p[1][0] | p[1][1]) | p[1][2] | p[1][3];
      if (__builtin_expect(__builtin_amdgcn_ballot_w64((any & GE2) != 0) != 0, 0)) {
        float t = s[0][0];
#pragma unroll
        for (int sub = 0; sub < 4; ++sub)
#pragma unroll
          for (int r = 0; r < 4; ++r) t = fmaxf(t, s[sub][r]);
        const float nn = fminf(nm, -fmaf(xmax4(t), C2, SLACK));
        const float alpha = __builtin_amdgcn_exp2f(nn - nm);
        nm = nn;
#pragma unroll
        for (int db = 0; db < 8; ++db) o[db] *= alpha;
        lsum *= alpha;
#pragma unroll
        for (int sub = 0; sub < 4; ++sub) { FM(sub); EX(sub, 0); EX(sub, 1); EX(sub, 2); EX(sub, 3); CV(sub, 0); CV(sub, 1); }
      }
      BARRIER();
      VRD(0, 0); SB(); VRD(1, 0); SB(); VRD(2, 0); SB(); VRD(3, 0); SB(); VRD(4, 0); SB(); VRD(5, 0); SB();
      lsum = mfma(ones, p[0], lsum);
      lsum = mfma(ones, p[1], lsum);
      SB();
#define KX(i) do { kx[i] = (i) ? kx[0] ^ ((i) << 6) : ka0 + sb; asm volatile("" : "+v"(kx[i])); } while (0)
      PV1(0, 0); SB(); VRD(6, 0); SB(); PV1(1, 0); SB(); VRD(7, 0); SB(); PV1(2, 0); SB(); PV1(3, 0); KX(0); SB();
      PV1(4, 0); KX(1); SB(); PV1(5, 0); KX(2); SB(); PV1(6, 0); KX(3); SB(); PV1(7, 0); SB();
      if (__builtin_expect(left > NB, 0)) {
        VRD(0, VH_BYTES); SB(); VRD(1, VH_BYTES); SB(); VRD(2, VH_BYTES); SB(); VRD(3, VH_BYTES); SB(); VRD(4, VH_BYTES); SB();
        PV1(0, 4); SB(); VRD(5, VH_BYTES); SB(); PV1(1, 4); SB(); VRD(6, VH_BYTES); SB(); PV1(2, 4); SB(); VRD(7, VH_BYTES); SB(); PV1(3, 4); PV1(4, 4); PV1(5, 4); PV1(6, 4); PV1(7, 4); SB();
        if (left == NB + 1) {
          *(LAS f32x4*)(lds + xi + 2048) = f32x4{nm, 0.f, lsum[0], 0.f};
#pragma unroll
          for (int i = 0; i < 2; ++i) {
            const f32x4 a = o[4 + 2 * i], b = o[5 + 2 * i];
            *(LAS u32x4*)(lds + xi + i * 1024) = u32x4{pk(a[0], a[1]), pk(a[2], a[3]), pk(b[0], b[1]), pk(b[2], b[3])};
          }
        }
      }
    } while (--left != 0);
    RSTAMP(52);
    {
      asm volatile("" : "+v"(xi));
      const unsigned ln = (xi >> 4) & 63, xb = Q_OFF + head * XCH_BYTES;
      const unsigned xw = xb + (ln & 15) * 144 + (ln >> 4) * 8, xr = xb + (ln >> 3) * 144 + (ln & 7) * 16;
      const int oo = (ln >> 3) * 256 + half * 128 + (ln & 7) * 16;
      const f32x4 st = *(LAS f32x4*)(lds + (xi & ~0x300u) + 2048);
      const u32x4 yy[2] = {*(LAS u32x4*)(lds + xi), *(LAS u32x4*)(lds + xi + 1024)};
      SB();
      f32x4 y[4];
#pragma unroll
      for (int i = 0; i < 4; ++i) {
        const unsigned y0 = yy[i >> 1][2 * (i & 1)], y1 = yy[i >> 1][2 * (i & 1) + 1];
        y[i] = f32x4{__uint_as_float(y0 << 16), __uint_as_float(y0 & 0xFFFF0000u), __uint_as_float(y1 << 16), __uint_as_float(y1 & 0xFFFF0000u)};
      }
      const float nn = fminf(nm, st[0]);
      const float b1 = __builtin_amdgcn_exp2f(nn - nm), b2 = __builtin_amdgcn_exp2f(nn - st[0]);
      const float inv = __builtin_amdgcn_rcpf(fmaf(b1, lsum[0], b2 * st[2]));
      const float w1 = b1 * inv, w2 = b2 * inv;
#pragma unroll
      for (int i = 0; i < 4; ++i) {
        const f32x4 r = o[i] * w1 + y[i] * w2;
        *(LAS u32x2*)(lds + xw + i * 32) = u32x2{pk(r[0], r[1]), pk(r[2], r[3])};
      }
      asm volatile("" ::: "memory");
      const u32x4 r0 = *(LAS u32x4*)(lds + xr), r1 = *(LAS u32x4*)(lds + xr + 8 * 144);
      __builtin_amdgcn_raw_buffer_store_b128(r0, ro, oo, 0, OUT_AUX);
      __builtin_amdgcn_raw_buffer_store_b128(r1, ro, oo + 2048, 0, OUT_AUX);
    }
    DIAG_OUT(4);
    return;
  }

  const int ko0 = g * 256 + ((c ^ (g << 1)) << 4), ko1 = ko0 ^ 128;
  const int vo0 = (lane >> 3) * 256 + half * 128 + (((lane & 7) ^ ((lane >> 3) & 6)) << 4);
  const int vo1 = vo0 + 1024, vo2 = vo0 ^ 128, vo3 = vo2 + 1024;
  const __amdgpu_buffer_rsrc_t rk = __builtin_amdgcn_make_buffer_rsrc((char*)K + (size_t)pair * (SK * 256), 0, KV_BYTES, 0x00020000);
  const __amdgpu_buffer_rsrc_t rq = __builtin_amdgcn_make_buffer_rsrc((char*)Q + (size_t)pair * KT_BYTES, 0, KT_BYTES, 0x00020000);
  const __amdgpu_buffer_rsrc_t rv = __builtin_amdgcn_make_buffer_rsrc((char*)V + (size_t)pair * (SK * 256), 0, KV_BYTES, 0x00020000);
  const unsigned lbase = (unsigned)(uintptr_t)lds;
#define KTILE(POL, r, so, dst) \
    asm volatile("s_mov_b32 m0, %0\n\ts_nop 0\n\t" \
                 "buffer_load_dwordx4 %1, %3, %4 offen " POL " lds\n\t" \
                 "buffer_load_dwordx4 %2, %3, %4 offen offset:1024 " POL " lds\n\t" \
                 "buffer_load_dwordx4 %1, %3, %4 offen offset:2048 " POL " lds\n\t" \
                 "buffer_load_dwordx4 %2, %3, %4 offen offset:3072 " POL " lds" \
                 :: "s"(lbase + (dst) + wave * 4096), "v"(ko0), "v"(ko1), "s"(r), "s"((so) + wave * 4096) : "memory")
#define VHALF(a, b, so, dst) \
    asm volatile("s_mov_b32 m0, %0\n\ts_nop 0\n\t" \
                 "buffer_load_dwordx4 %1, %3, %4 offen nt lds\n\t" \
                 "buffer_load_dwordx4 %2, %3, %4 offen offset:1024 nt lds" \
                 :: "s"(lbase + (dst) + wave * 2048), "v"(a), "v"(b), "s"(rv), "s"((so) + wave * 4096) : "memory")
#define SLOT(s) (((s) & 3) * SLOT_BYTES)
  const int rot = 0;
#define OFF1(t) (((2 * (t) + half + rot) & (NT - 1)) * KT_BYTES)
#define OFF2(t) (((NA + (t) + rot) & (NT - 1)) * KT_BYTES)
#define K1(t) KTILE("nt", rk, OFF1(t), SLOT(t))
#define K2(t) KTILE(K2_POL, rk, OFF2(t), SLOT(t))
#define V1(t) do { VHALF(vo0, vo1, OFF1(t), SLOT((t) + 2) + KT_BYTES); VHALF(vo2, vo3, OFF1(t), SLOT((t) + 2) + KT_BYTES + VH_BYTES); } while (0)
#define V2(t) VHALF(vo0, vo1, OFF2(t), SLOT((t) + 2) + KT_BYTES)
  unsigned due = tstart + PACE0;
#define PACE_WAIT(d) do { while ((int)((unsigned)__builtin_amdgcn_s_memrealtime() - due) < 0) __builtin_amdgcn_s_sleep(1); due += (d); } while (0)
#define PACE_WAIT2(d)
  due -= (PRO + 2) * (PACE / 2);
  K1(0); KTILE(K2_POL, rq, 0, Q_OFF);
  if (PRO >= 3) PACE_WAIT(PACE / 2);
  K1(1);
  if (PRO >= 2) PACE_WAIT(PACE / 2);
  V1(0);
  wait_vm<12>(); PACE_WAIT(PACE / 2); K1(2);
  wait_vm<12>(); LBARRIER(); PACE_WAIT(PACE / 2); V1(1);
  wait_vm<12>(); LBARRIER(); PACE_WAIT(PACE / 2); K1(3);
#pragma unroll 1
  for (int s = 2; s < NA - 2; ++s) {
    wait_vm<12>(); PACE_WAIT(PACE / 2); V1(s);
    wait_vm<12>(); PACE_WAIT(PACE / 2); K1(s + 2); LBARRIER();
  }
  wait_vm<12>(); PACE_WAIT(PACE / 2); V1(NA - 2); wait_vm<12>(); PACE_WAIT(PACE / 4); K2(NA); LBARRIER();
  wait_vm<12>(); PACE_WAIT2(PACE / 2); V1(NA - 1); wait_vm<12>(); PACE_WAIT2(PACE / 4); K2(NA + 1); LBARRIER();
  wait_vm<12>(); V2(NA); wait_vm<10>(); K2(NA + 2); LBARRIER();
  wait_vm<10>(); V2(NA + 1); wait_vm<8>(); K2(NA + 3); LBARRIER();
#pragma unroll 1
  for (int s = NA + 2; s < NI - 2; ++s) {
    wait_vm<10>(); V2(s);
    wait_vm<8>(); K2(s + 2); LBARRIER();
  }
  V2(NI - 2);
  wait_vm<8>(); LBARRIER();
  V2(NI - 1);
  wait_vm<4>(); LBARRIER();
  wait_vm<2>(); LBARRIER();
  wait_vm<0>(); LBARRIER();
  DIAG_OUT(0);
}

}

extern "C" void kernel_launch(void* const* d_in, const int* in_sizes, int n_in, void* d_out, int out_size, void* d_ws, size_t ws_size,
                              hipStream_t stream) {
  gqa_attn<<<2 * NPAIR, NTHREADS, 0, stream>>>((const __hip_bfloat16*)d_in[0], (const __hip_bfloat16*)d_in[1], (const __hip_bfloat16*)d_in[2],
                                                (__hip_bfloat16*)d_out, (unsigned char*)d_ws);
}
```

```cpp
#define PROBE 77
#include <hip/hip_runtime.h>
#include <hip/hip_bf16.h>
#include <stdint.h>

#define NA 12
#define K2_POL "nt"
#define OUT_AUX 18
#define KV_BYTES (SK * 256)
#define PACE 108
#define PACE0 360
#define NOCOMP 0
#define NOX 0
#define FLAG_LAG 2
#define NLOAD 4
#define PROBE_X -1
#define PREREAD 0
#define PRE_POL ""
#define TRANS 3
#define P2ORD 0
#define PRO 3
#define PROBAR 1
#define VTOUCH 1
#define PRIO 0
#define PACEK 250
#define PACEM 2000
#define MINSP 0
#define SLIP 0
#define STOUCH 0
#define ORD 0
#define MAP 4
#define ROT 0
#define PBASE 0
#define L1 1
#define L2 3
#define L3 1
#define ST_POL "sc1"
#define LD_POL "sc1"
#define BARFIRST 2
#define PACE_LAST 0

namespace {

constexpr int B_ = 16, HKV = 8, SQ = 16, SK = 2048;
constexpr int TK = 64, NT = SK / TK;
constexpr int NB = NT - 2 * NA, NI = NA + NB;
constexpr int NPAIR = B_ * HKV;
constexpr int KT_BYTES = TK * 256, VH_BYTES = TK * 128;
constexpr int SLOT_BYTES = KT_BYTES + 2 * VH_BYTES;
constexpr int RING_BYTES = 4 * SLOT_BYTES;
constexpr int Q_OFF = RING_BYTES;
constexpr int XCH_BYTES = 4096;
constexpr int LDS_BYTES = RING_BYTES + 4 * XCH_BYTES;
constexpr int NTHREADS = 576;
constexpr float C2 = 0.08838834764831845f * 1.4426950408889634f;
constexpr float SLACK = 7.0f;
constexpr unsigned GE2 = 0x40004000u;
constexpr int FLAG_OFF = 8192, EPOCH_OFF = FLAG_OFF + 1024;
constexpr int BOX_BYTES = EPOCH_OFF + 256;
constexpr unsigned TAG_STEP = 0x9E3779B1u;
static_assert(NB >= 6 && NA >= 4, "the swap needs a few tiles to hide under");

typedef __attribute__((ext_vector_type(8))) __bf16 bf16x8;
typedef __attribute__((ext_vector_type(4))) __bf16 bf16x4;
typedef __attribute__((ext_vector_type(2))) __bf16 bf16x2;
typedef __attribute__((ext_vector_type(4))) float f32x4;
typedef __attribute__((ext_vector_type(2))) float f32x2;
typedef __attribute__((ext_vector_type(4))) unsigned u32x4;
typedef __attribute__((ext_vector_type(2))) unsigned u32x2;
#define LAS __attribute__((address_space(3)))

#define BARRIER() asm volatile("s_waitcnt lgkmcnt(0)\n\ts_barrier" ::: "memory")
#define LBARRIER() asm volatile("s_barrier" ::: "memory")
#define DIAG_OUT(code)
#define RSTAMP(i)
#define SB() __builtin_amdgcn_sched_barrier(0)

template <int N> __device__ __forceinline__ void wait_vm() { asm volatile("s_waitcnt vmcnt(%0)" ::"n"(N) : "memory"); }

__device__ __forceinline__ float xmax4(float x) {
  auto s = __builtin_amdgcn_permlane16_swap(__float_as_uint(x), __float_as_uint(x), false, false);
  x = fmaxf(__uint_as_float(s[0]), __uint_as_float(s[1]));
  auto t = __builtin_amdgcn_permlane32_swap(__float_as_uint(x), __float_as_uint(x), false, false);
  return fmaxf(__uint_as_float(t[0]), __uint_as_float(t[1]));
}
__device__ __forceinline__ unsigned pk(float a, float b) { f32x2 v = {a, b}; return __builtin_bit_cast(unsigned, __builtin_convertvector(v, bf16x2)); }
__device__ __forceinline__ f32x4 mfma(u32x4 a, u32x4 b, f32x4 c) {
  return __builtin_amdgcn_mfma_f32_16x16x32_bf16(__builtin_bit_cast(bf16x8, a), __builtin_bit_cast(bf16x8, b), c, 0, 0, 0);
}
__device__ __forceinline__ u32x2 trd(LAS char* p) { return __builtin_bit_cast(u32x2, __builtin_amdgcn_ds_read_tr16_b64_v4bf16((LAS bf16x4*)p)); }

__global__ void __launch_bounds__(NTHREADS, 3)
gqa_attn(const __hip_bfloat16* __restrict__ Q, const __hip_bfloat16* __restrict__ K, const __hip_bfloat16* __restrict__ V,
         __hip_bfloat16* __restrict__ O, unsigned char* __restrict__ ws) {
  __shared__ __attribute__((aligned(1024))) char smem[LDS_BYTES];
  LAS char* lds = (LAS char*)smem;
  const unsigned tstart = (unsigned)__builtin_amdgcn_s_memrealtime();
  const int lane = threadIdx.x & 63;
  const int wave = __builtin_amdgcn_readfirstlane(threadIdx.x >> 6);
  const int pair = blockIdx.x & (NPAIR - 1), half = blockIdx.x >> 7;
  const int g = lane >> 4, c = lane & 15;
  unsigned ptx = tstart; int pn = 0;
#undef LBARRIER
#define LBARRIER() do { asm volatile("s_barrier" ::: "memory"); if (pn == PROBE) ptx = (unsigned)__builtin_amdgcn_s_memrealtime(); ++pn; } while (0)

  if (__builtin_expect(wave >= NLOAD, 0)) {
    if (wave == 8) {
      if (NOX || NOCOMP) return;
      const unsigned char* wp = ws + (size_t)pair * (2 * BOX_BYTES);
      const int bo = half * BOX_BYTES, bi = (half ^ 1) * BOX_BYTES;
      unsigned ep_out, ep_in;
      asm volatile("s_load_dword %0, %2, %3\n\ts_load_dword %1, %2, %4\n\ts_waitcnt lgkmcnt(0)" : "=&s"(ep_out), "=&s"(ep_in) : "s"(wp), "s"(bo + EPOCH_OFF), "s"(bi + EPOCH_OFF) : "memory");
      const __amdgpu_buffer_rsrc_t rp = __builtin_amdgcn_make_buffer_rsrc((void*)wp, 0, 2 * BOX_BYTES, 0x00020000);
      const unsigned xb = (unsigned)(uintptr_t)lds + Q_OFF;
      {
#pragma unroll 1
        for (int j = 0; j < 6; ++j) asm volatile("s_barrier" ::: "memory");
        unsigned t0, t1;
        asm volatile("buffer_load_dword %0, off, %2, %3 offset:128\n\t"
                     "s_barrier\n\ts_barrier\n\ts_barrier\n\ts_waitcnt vmcnt(0)"
                     : "=&v"(t0), "=&v"(t1) : "s"(rp), "s"(bo + EPOCH_OFF), "v"(0), "s"((char*)O + (size_t)pair * (4 * SQ * 256)) : "memory");
#pragma unroll 1
        for (int j = 9; j < NA + 3; ++j) asm volatile("s_barrier" ::: "memory");
      }
      int spins = 1 << 20;
      unsigned long long tmp64;
#define BAR "s_barrier\n\t"
#define TS(n)
#define TS_CLOB
#define BAR0
#define BAR1 BAR
#define BAR2 BAR BAR
#define BAR3 BAR BAR BAR
#define BAR4 BAR BAR BAR BAR
#define BAR5 BAR BAR BAR BAR BAR
#define BAR6 BAR BAR BAR BAR BAR BAR
#define BARS_(n) BAR##n
#define BARS(n) BARS_(n)
      static_assert(L1 + L2 + L3 + 3 == NB, "barrier count");
#define FLAG_LOAD "buffer_load_dwordx4 v[48:51], %[po], %[rp], %[fi] offen sc1\n\t"
#define FLAG_TEST "v_cmp_ne_u32 vcc, %[tin], v49\n\tv_cmp_ne_u32 %[t], %[tin], v51\n\ts_nop 3\n\ts_or_b64 vcc, vcc, %[t]\n\t"
#define PART_IO(op, s0, s1, pol) \
      op " v[16:19], %[po], %[rp], " s0 " offen " pol "\n\t" op " v[20:23], %[po], %[rp], " s0 " offen offset:1024 " pol "\n\t" \
      op " v[24:27], %[po], %[rp], " s0 " offen offset:2048 " pol "\n\t" op " v[28:31], %[po], %[rp], " s0 " offen offset:3072 " pol "\n\t" \
      op " v[32:35], %[po], %[rp], " s1 " offen " pol "\n\t" op " v[36:39], %[po], %[rp], " s1 " offen offset:1024 " pol "\n\t" \
      op " v[40:43], %[po], %[rp], " s1 " offen offset:2048 " pol "\n\t" op " v[44:47], %[po], %[rp], " s1 " offen offset:3072 " pol "\n\t"
      asm volatile(
          TS(60)
          "ds_read_b128 v[16:19], %[xi]\n\tds_read_b128 v[20:23], %[xi] offset:1024\n\t"
          "ds_read_b128 v[24:27], %[xi] offset:4096\n\tds_read_b128 v[28:31], %[xi] offset:5120\n\t"
          "ds_read_b128 v[32:35], %[xi] offset:8192\n\tds_read_b128 v[36:39], %[xi] offset:9216\n\t"
          "ds_read_b128 v[40:43], %[xi] offset:12288\n\tds_read_b128 v[44:47], %[xi] offset:13312\n\t"
          "ds_read_b128 v[48:51], %[xs]\n\t"
          "s_waitcnt lgkmcnt(0)\n\t"
          PART_IO("buffer_store_dwordx4", "%[bo]", "%[bo2]", ST_POL)
          "v_mov_b32 v49, %[tout]\n\tv_mov_b32 v51, %[tout]\n\t" TS(62)
          BARS(L1) TS(64)
          "s_waitcnt vmcnt(0)\n\t" TS(66)
          "buffer_store_dwordx4 v[48:51], %[po], %[rp], %[fo] offen sc1\n\t"
          BARS(L2)
          FLAG_LOAD
          BARS(L3) TS(68)
          "s_waitcnt vmcnt(0)\n\t" TS(70)
          FLAG_TEST
          "s_cbranch_vccnz 1f\n\t"
          PART_IO("buffer_load_dwordx4", "%[bi]", "%[bi2]", LD_POL)
          BAR
          "3:\n\t"
          "ds_write_b128 %[xs], v[48:51]\n\t" TS(72)
          "s_waitcnt vmcnt(0)\n\t" TS(74)
          "ds_write_b128 %[xi], v[16:19]\n\tds_write_b128 %[xi], v[20:23] offset:1024\n\t"
          "ds_write_b128 %[xi], v[24:27] offset:4096\n\tds_write_b128 %[xi], v[28:31] offset:5120\n\t"
          "ds_write_b128 %[xi], v[32:35] offset:8192\n\tds_write_b128 %[xi], v[36:39] offset:9216\n\t"
          "ds_write_b128 %[xi], v[40:43] offset:12288\n\tds_write_b128 %[xi], v[44:47] offset:13312\n\t"
          "s_waitcnt lgkmcnt(0)\n\t" BAR
          "buffer_store_dword v49, %[eo], %[rp], %[bi] offen sc1\n\t"
          "s_branch 4f\n"
          "1:\n\t"
          FLAG_LOAD
          BAR
          "2:\n\t"
          "s_waitcnt vmcnt(0)\n\t"
          FLAG_TEST
          "s_cbranch_vccz 5f\n\t"
          FLAG_LOAD
          "s_sub_u32 %[spins], %[spins], 1\n\t"
          "s_cbranch_scc0 2b\n\t"
          "s_waitcnt vmcnt(0)\n\t"
          "v_mov_b32 v50, 0x7fc00000\n"
          "5:\n\t"
          "s_sub_u32 %[spins], %[spins], 1\n\t"
          PART_IO("buffer_load_dwordx4", "%[bi]", "%[bi2]", LD_POL)
          "s_branch 3b\n"
          "4:"
          : [spins] "+s"(spins), [t] "=&s"(tmp64)
          : [tin] "s"(ep_in + TAG_STEP), [tout] "s"(ep_out + TAG_STEP), [bi] "s"(bi), [bi2] "s"(bi + 4096), [fi] "s"(bi + FLAG_OFF),
            [bo] "s"(bo), [bo2] "s"(bo + 4096), [fo] "s"(bo + FLAG_OFF), [po] "v"(lane * 16), [eo] "v"(lane == 0 ? EPOCH_OFF : 2 * BOX_BYTES),
            [xi] "v"(xb + lane * 16), [xs] "v"(xb + g * XCH_BYTES + 2048 + c * 16), [rp] "s"(rp)
          : "memory", "vcc", "scc", "v16", "v17", "v18", "v19", "v20", "v21", "v22", "v23", "v24", "v25", "v26", "v27", "v28", "v29", "v30", "v31",
            "v32", "v33", "v34", "v35", "v36", "v37", "v38", "v39", "v40", "v41", "v42", "v43", "v44", "v45", "v46", "v47", "v48", "v49", "v50", "v51" TS_CLOB);
      DIAG_OUT(spins == (1 << 20) ? 2 : spins == (1 << 20) - 1 ? 3 : 5);
      return;
    }
    const int head = wave - NLOAD;
    if (PRIO) __builtin_amdgcn_s_setprio(PRIO);
    const __amdgpu_buffer_rsrc_t ro = __builtin_amdgcn_make_buffer_rsrc((char*)O + ((size_t)pair * 4 + head) * (SQ * 256), 0, SQ * 256, 0x00020000);
    const unsigned ka0 = c * 256 + ((g ^ ((c & 7) << 1)) << 4);
    const int vr = 4 * g + (c >> 2);
    const unsigned va0 = KT_BYTES + vr * 128 + (((vr >> 1) & 3) << 5) + (c & 3) * 8;
    unsigned xi = Q_OFF + head * XCH_BYTES + lane * 16;
    u32x4 ones = {0x3F803F80u, 0x3F803F80u, 0x3F803F80u, 0x3F803F80u};
    asm volatile("" : "+v"(ones), "+v"(xi));
    f32x4 o[8], lsum = {0.f, 0.f, 0.f, 0.f};
#pragma unroll
    for (int db = 0; db < 8; ++db) { o[db] = f32x4{0.f, 0.f, 0.f, 0.f}; asm volatile("" : "+v"(o[db])); }
    float nm = 3.0e38f;
    unsigned sb = SLOT_BYTES;
    unsigned kx[4];
#pragma unroll
    for (int st = 0; st < 4; ++st) kx[st] = (ka0 ^ (st << 6)) + sb;
    BARRIER();
    u32x4 q[4], kf[4][4];
#pragma unroll
    for (int st = 0; st < 4; ++st) q[st] = *(const LAS u32x4*)(lds + (Q_OFF + head * 4096) + (ka0 ^ (st << 6)));
#pragma unroll
    for (int sub = 0; sub < 4; ++sub)
#pragma unroll
      for (int st = 0; st < 4; ++st) kf[sub][st] = *(const LAS u32x4*)(lds + (ka0 ^ (st << 6)) + sub * 4096);
    asm volatile("" : "+v"(kf[0][0]), "+v"(kf[0][1]), "+v"(kf[0][2]), "+v"(kf[0][3]), "+v"(kf[1][0]), "+v"(kf[1][1]), "+v"(kf[1][2]), "+v"(kf[1][3]),
                 "+v"(kf[2][0]), "+v"(kf[2][1]), "+v"(kf[2][2]), "+v"(kf[2][3]), "+v"(kf[3][0]), "+v"(kf[3][1]), "+v"(kf[3][2]), "+v"(kf[3][3]),
                 "+v"(q[0]), "+v"(q[1]), "+v"(q[2]), "+v"(q[3]));
    BARRIER();
    int left = NI;
    asm volatile("" : "+s"(left));
#pragma unroll 1
    do {
      f32x4 s[4], x[4];
      u32x4 p[2];
      u32x2 vf[8][2];
      const f32x4 zero = {0.f, 0.f, 0.f, 0.f};
      sb = (sb + SLOT_BYTES) & (RING_BYTES - 1);
      unsigned vx[4];
#define QK1(sub, st) do { s[sub] = mfma(kf[sub][st], q[st], (st) ? s[sub] : zero); kf[sub][st] = *(const LAS u32x4*)(lds + kx[st] + (sub) * 4096); } while (0)
#define FM(sub) x[sub] = s[sub] * C2 + nm
#define EX(sub, r) x[sub][r] = __builtin_amdgcn_exp2f(x[sub][r])
#define CV(sub, h) p[(sub) >> 1][2 * ((sub) & 1) + (h)] = pk(x[sub][2 * (h)], x[sub][2 * (h) + 1])
#define VX(i) do { vx[i] = (i) ? vx[0] ^ ((i) << 5) : va0 + sb; asm volatile("" : "+v"(vx[i])); } while (0)
#define VRD(i, off) do { vf[i][0] = trd(lds + vx[(i) & 3] + ((i) >> 2) * 4096 + (off)); vf[i][1] = trd(lds + vx[(i) & 3] + ((i) >> 2) * 4096 + 2048 + (off)); } while (0)
#define PV1(i, ob) o[(ob) + ((i) & 3)] = mfma(u32x4{vf[i][0][0], vf[i][0][1], vf[i][1][0], vf[i][1][1]}, p[(i) >> 2], o[(ob) + ((i) & 3)])
      QK1(0, 0); SB(); QK1(1, 0); VX(0); SB(); QK1(0, 1); VX(1); SB(); QK1(1, 1); VX(2); SB();
      QK1(0, 2); VX(3); SB(); QK1(1, 2); SB(); QK1(0, 3); SB(); QK1(1, 3); SB();
      QK1(2, 0); SB(); QK1(3, 0); FM(0); SB();
      QK1(2, 1); FM(1); EX(0, 0); SB(); QK1(3, 1); EX(0, 1); EX(0, 2); SB();
      QK1(2, 2); EX(0, 3); EX(1, 0); SB(); QK1(3, 2); EX(1, 1); EX(1, 2); SB();
      QK1(2, 3); EX(1, 3); CV(0, 0); SB(); QK1(3, 3); CV(0, 1); CV(1, 0); CV(1, 1); SB();
      unsigned any = (p[0][0] | p[0][1] | p[0][2]) | p[0][3];
      asm volatile("" : "+v"(any) :: "memory");
      SB();
      FM(2); FM(3); SB();
      EX(2, 0); EX(2, 1); EX(2, 2); EX(2, 3); EX(3, 0); EX(3, 1); EX(3, 2); EX(3, 3); SB();
      CV(2, 0); CV(2, 1); CV(3, 0); CV(3, 1);
      any = (any | p[1][0] | p[1][1]) | p[1][2] | p[1][3];
      if (__builtin_expect(__builtin_amdgcn_ballot_w64((any & GE2) != 0) != 0, 0)) {
        float t = s[0][0];
#pragma unroll
        for (int sub = 0; sub < 4; ++sub)
#pragma unroll
          for (int r = 0; r < 4; ++r) t = fmaxf(t, s[sub][r]);
        const float nn = fminf(nm, -fmaf(xmax4(t), C2, SLACK));
        const float alpha = __builtin_amdgcn_exp2f(nn - nm);
        nm = nn;
#pragma unroll
        for (int db = 0; db < 8; ++db) o[db] *= alpha;
        lsum *= alpha;
#pragma unroll
        for (int sub = 0; sub < 4; ++sub) { FM(sub); EX(sub, 0); EX(sub, 1); EX(sub, 2); EX(sub, 3); CV(sub, 0); CV(sub, 1); }
      }
      BARRIER();
      VRD(0, 0); SB(); VRD(1, 0); SB(); VRD(2, 0); SB(); VRD(3, 0); SB(); VRD(4, 0); SB(); VRD(5, 0); SB();
      lsum = mfma(ones, p[0], lsum);
      lsum = mfma(ones, p[1], lsum);
      SB();
#define KX(i) do { kx[i] = (i) ? kx[0] ^ ((i) << 6) : ka0 + sb; asm volatile("" : "+v"(kx[i])); } while (0)
      PV1(0, 0); SB(); VRD(6, 0); SB(); PV1(1, 0); SB(); VRD(7, 0); SB(); PV1(2, 0); SB(); PV1(3, 0); KX(0); SB();
      PV1(4, 0); KX(1); SB(); PV1(5, 0); KX(2); SB(); PV1(6, 0); KX(3); SB(); PV1(7, 0); SB();
      if (__builtin_expect(left > NB, 0)) {
        VRD(0, VH_BYTES); SB(); VRD(1, VH_BYTES); SB(); VRD(2, VH_BYTES); SB(); VRD(3, VH_BYTES); SB(); VRD(4, VH_BYTES); SB();
        PV1(0, 4); SB(); VRD(5, VH_BYTES); SB(); PV1(1, 4); SB(); VRD(6, VH_BYTES); SB(); PV1(2, 4); SB(); VRD(7, VH_BYTES); SB(); PV1(3, 4); PV1(4, 4); PV1(5, 4); PV1(6, 4); PV1(7, 4); SB();
        if (left == NB + 1) {
          *(LAS f32x4*)(lds + xi + 2048) = f32x4{nm, 0.f, lsum[0], 0.f};
#pragma unroll
          for (int i = 0; i < 2; ++i) {
            const f32x4 a = o[4 + 2 * i], b = o[5 + 2 * i];
            *(LAS u32x4*)(lds + xi + i * 1024) = u32x4{pk(a[0], a[1]), pk(a[2], a[3]), pk(b[0], b[1]), pk(b[2], b[3])};
          }
        }
      }
    } while (--left != 0);
    RSTAMP(52);
    {
      asm volatile("" : "+v"(xi));
      const unsigned ln = (xi >> 4) & 63, xb = Q_OFF + head * XCH_BYTES;
      const unsigned xw = xb + (ln & 15) * 144 + (ln >> 4) * 8, xr = xb + (ln >> 3) * 144 + (ln & 7) * 16;
      const int oo = (ln >> 3) * 256 + half * 128 + (ln & 7) * 16;
      const f32x4 st = *(LAS f32x4*)(lds + (xi & ~0x300u) + 2048);
      const u32x4 yy[2] = {*(LAS u32x4*)(lds + xi), *(LAS u32x4*)(lds + xi + 1024)};
      SB();
      f32x4 y[4];
#pragma unroll
      for (int i = 0; i < 4; ++i) {
        const unsigned y0 = yy[i >> 1][2 * (i & 1)], y1 = yy[i >> 1][2 * (i & 1) + 1];
        y[i] = f32x4{__uint_as_float(y0 << 16), __uint_as_float(y0 & 0xFFFF0000u), __uint_as_float(y1 << 16), __uint_as_float(y1 & 0xFFFF0000u)};
      }
      const float nn = fminf(nm, st[0]);
      const float b1 = __builtin_amdgcn_exp2f(nn - nm), b2 = __builtin_amdgcn_exp2f(nn - st[0]);
      const float inv = __builtin_amdgcn_rcpf(fmaf(b1, lsum[0], b2 * st[2]));
      const float w1 = b1 * inv, w2 = b2 * inv;
#pragma unroll
      for (int i = 0; i < 4; ++i) {
        const f32x4 r = o[i] * w1 + y[i] * w2;
        *(LAS u32x2*)(lds + xw + i * 32) = u32x2{pk(r[0], r[1]), pk(r[2], r[3])};
      }
      asm volatile("" ::: "memory");
      const u32x4 r0 = *(LAS u32x4*)(lds + xr), r1 = *(LAS u32x4*)(lds + xr + 8 * 144);
      __builtin_amdgcn_raw_buffer_store_b128(r0, ro, oo, 0, OUT_AUX);
      __builtin_amdgcn_raw_buffer_store_b128(r1, ro, oo + 2048, 0, OUT_AUX);
    }
    DIAG_OUT(4);
    return;
  }

  const int ko0 = g * 256 + ((c ^ (g << 1)) << 4), ko1 = ko0 ^ 128;
  const int vo0 = (lane >> 3) * 256 + half * 128 + (((lane & 7) ^ ((lane >> 3) & 6)) << 4);
  const int vo1 = vo0 + 1024, vo2 = vo0 ^ 128, vo3 = vo2 + 1024;
  const __amdgpu_buffer_rsrc_t rk = __builtin_amdgcn_make_buffer_rsrc((char*)K + (size_t)pair * (SK * 256), 0, KV_BYTES, 0x00020000);
  const __amdgpu_buffer_rsrc_t rq = __builtin_amdgcn_make_buffer_rsrc((char*)Q + (size_t)pair * KT_BYTES, 0, KT_BYTES, 0x00020000);
  const __amdgpu_buffer_rsrc_t rv = __builtin_amdgcn_make_buffer_rsrc((char*)V + (size_t)pair * (SK * 256), 0, KV_BYTES, 0x00020000);
  const unsigned lbase = (unsigned)(uintptr_t)lds;
#define KTILE(POL, r, so, dst) \
    asm volatile("s_mov_b32 m0, %0\n\ts_nop 0\n\t" \
                 "buffer_load_dwordx4 %1, %3, %4 offen " POL " lds\n\t" \
                 "buffer_load_dwordx4 %2, %3, %4 offen offset:1024 " POL " lds\n\t" \
                 "buffer_load_dwordx4 %1, %3, %4 offen offset:2048 " POL " lds\n\t" \
                 "buffer_load_dwordx4 %2, %3, %4 offen offset:3072 " POL " lds" \
                 :: "s"(lbase + (dst) + wave * 4096), "v"(ko0), "v"(ko1), "s"(r), "s"((so) + wave * 4096) : "memory")
#define VHALF(a, b, so, dst) \
    asm volatile("s_mov_b32 m0, %0\n\ts_nop 0\n\t" \
                 "buffer_load_dwordx4 %1, %3, %4 offen nt lds\n\t" \
                 "buffer_load_dwordx4 %2, %3, %4 offen offset:1024 nt lds" \
                 :: "s"(lbase + (dst) + wave * 2048), "v"(a), "v"(b), "s"(rv), "s"((so) + wave * 4096) : "memory")
#define SLOT(s) (((s) & 3) * SLOT_BYTES)
  const int rot = 0;
#define OFF1(t) (((2 * (t) + half + rot) & (NT - 1)) * KT_BYTES)
#define OFF2(t) (((NA + (t) + rot) & (NT - 1)) * KT_BYTES)
#define K1(t) KTILE("nt", rk, OFF1(t), SLOT(t))
#define K2(t) KTILE(K2_POL, rk, OFF2(t), SLOT(t))
#define V1(t) do { VHALF(vo0, vo1, OFF1(t), SLOT((t) + 2) + KT_BYTES); VHALF(vo2, vo3, OFF1(t), SLOT((t) + 2) + KT_BYTES + VH_BYTES); } while (0)
#define V2(t) VHALF(vo0, vo1, OFF2(t), SLOT((t) + 2) + KT_BYTES)
  unsigned due = tstart + PACE0;
#define PACE_WAIT(d) do { while ((int)((unsigned)__builtin_amdgcn_s_memrealtime() - due) < 0) __builtin_amdgcn_s_sleep(1); due += (d); } while (0)
#define PACE_WAIT2(d)
  due -= (PRO + 2) * (PACE / 2);
  K1(0); KTILE(K2_POL, rq, 0, Q_OFF);
  if (PRO >= 3) PACE_WAIT(PACE / 2);
  K1(1);
  if (PRO >= 2) PACE_WAIT(PACE / 2);
  V1(0);
  wait_vm<12>(); PACE_WAIT(PACE / 2); K1(2);
  wait_vm<12>(); LBARRIER(); PACE_WAIT(PACE / 2); V1(1);
  wait_vm<12>(); LBARRIER(); PACE_WAIT(PACE / 2); K1(3);
#pragma unroll 1
  for (int s = 2; s < NA - 2; ++s) {
    wait_vm<12>(); PACE_WAIT(PACE / 2); V1(s);
    wait_vm<12>(); LBARRIER(); PACE_WAIT(PACE / 2); K1(s + 2);
  }
  wait_vm<12>(); PACE_WAIT(PACE / 2); V1(NA - 2); wait_vm<12>(); LBARRIER(); PACE_WAIT(PACE / 4); K2(NA);
  wait_vm<8>(); LBARRIER(); PACE_WAIT(PACE / 2); V1(NA - 1); PACE_WAIT2(PACE / 4); K2(NA + 1);
  wait_vm<12>(); V2(NA); wait_vm<10>(); K2(NA + 2); LBARRIER();
#pragma unroll 1
  for (int s = NA + 1; s < NI - 2; ++s) {
    wait_vm<10>(); V2(s);
    wait_vm<8>(); K2(s + 2); LBARRIER();
  }
  V2(NI - 2);
  wait_vm<8>(); LBARRIER();
  V2(NI - 1);
  wait_vm<4>(); LBARRIER();
  wait_vm<2>(); LBARRIER();
  wait_vm<0>(); LBARRIER();
  if (PROBE_X < 0 || (int)(blockIdx.x & 7) == PROBE_X) { const unsigned due2 = ptx + 4000; while ((int)((unsigned)__builtin_amdgcn_s_memrealtime() - due2) < 0) __builtin_amdgcn_s_sleep(1); }
  DIAG_OUT(0);
}

}

extern "C" void kernel_launch(void* const* d_in, const int* in_sizes, int n_in, void* d_out, int out_size, void* d_ws, size_t ws_size,
                              hipStream_t stream) {
  gqa_attn<<<2 * NPAIR, NTHREADS, 0, stream>>>((const __hip_bfloat16*)d_in[0], (const __hip_bfloat16*)d_in[1], (const __hip_bfloat16*)d_in[2],
                                                (__hip_bfloat16*)d_out, (unsigned char*)d_ws);
}
```

```cpp
#define PACE0 390
#define ROT 5
#define BMAP 4
#include <hip/hip_runtime.h>
#include <hip/hip_bf16.h>
#include <stdint.h>

#define NA 12
#define K2_POL "nt"
#define OUT_AUX 18
#define KV_BYTES (SK * 256)
#define PACE 108
#define NOCOMP 0
#define NOX 0
#define FLAG_LAG 2
#define NLOAD 4
#define PGAIN 4
#define PROBE_X -1
#define PREREAD 0
#define PRE_POL ""
#define TRANS 3
#define P2ORD 0
#define PRO 3
#define PROBAR 1
#define VTOUCH 1
#define PRIO 0
#define PACEK 250
#define PACEM 2000
#define MINSP 0
#define SLIP 0
#define STOUCH 0
#define ORD 0
#define MAP 4
#define PBASE 0
#define L1 1
#define L2 3
#define L3 1
#define ST_POL "sc1"
#define LD_POL "sc1"
#define BARFIRST 2
#define PACE_LAST 0

namespace {

constexpr int B_ = 16, HKV = 8, SQ = 16, SK = 2048;
constexpr int TK = 64, NT = SK / TK;
constexpr int NB = NT - 2 * NA, NI = NA + NB;
constexpr int NPAIR = B_ * HKV;
constexpr int KT_BYTES = TK * 256, VH_BYTES = TK * 128;
constexpr int SLOT_BYTES = KT_BYTES + 2 * VH_BYTES;
constexpr int RING_BYTES = 4 * SLOT_BYTES;
constexpr int Q_OFF = RING_BYTES;
constexpr int XCH_BYTES = 4096;
constexpr int LDS_BYTES = RING_BYTES + 4 * XCH_BYTES;
constexpr int NTHREADS = 576;
constexpr float C2 = 0.08838834764831845f * 1.4426950408889634f;
constexpr float SLACK = 7.0f;
constexpr unsigned GE2 = 0x40004000u;
constexpr int FLAG_OFF = 8192, EPOCH_OFF = FLAG_OFF + 1024;
constexpr int BOX_BYTES = EPOCH_OFF + 256;
constexpr unsigned TAG_STEP = 0x9E3779B1u;
static_assert(NB >= 6 && NA >= 4, "the swap needs a few tiles to hide under");

typedef __attribute__((ext_vector_type(8))) __bf16 bf16x8;
typedef __attribute__((ext_vector_type(4))) __bf16 bf16x4;
typedef __attribute__((ext_vector_type(2))) __bf16 bf16x2;
typedef __attribute__((ext_vector_type(4))) float f32x4;
typedef __attribute__((ext_vector_type(2))) float f32x2;
typedef __attribute__((ext_vector_type(4))) unsigned u32x4;
typedef __attribute__((ext_vector_type(2))) unsigned u32x2;
#define LAS __attribute__((address_space(3)))

#define BARRIER() asm volatile("s_waitcnt lgkmcnt(0)\n\ts_barrier" ::: "memory")
#define LBARRIER() asm volatile("s_barrier" ::: "memory")
#define DIAG_OUT(code)
#define RSTAMP(i)
#define SB() __builtin_amdgcn_sched_barrier(0)

template <int N> __device__ __forceinline__ void wait_vm() { asm volatile("s_waitcnt vmcnt(%0)" ::"n"(N) : "memory"); }

__device__ __forceinline__ float xmax4(float x) {
  auto s = __builtin_amdgcn_permlane16_swap(__float_as_uint(x), __float_as_uint(x), false, false);
  x = fmaxf(__uint_as_float(s[0]), __uint_as_float(s[1]));
  auto t = __builtin_amdgcn_permlane32_swap(__float_as_uint(x), __float_as_uint(x), false, false);
  return fmaxf(__uint_as_float(t[0]), __uint_as_float(t[1]));
}
__device__ __forceinline__ unsigned pk(float a, float b) { f32x2 v = {a, b}; return __builtin_bit_cast(unsigned, __builtin_convertvector(v, bf16x2)); }
__device__ __forceinline__ f32x4 mfma(u32x4 a, u32x4 b, f32x4 c) {
  return __builtin_amdgcn_mfma_f32_16x16x32_bf16(__builtin_bit_cast(bf16x8, a), __builtin_bit_cast(bf16x8, b), c, 0, 0, 0);
}
__device__ __forceinline__ u32x2 trd(LAS char* p) { return __builtin_bit_cast(u32x2, __builtin_amdgcn_ds_read_tr16_b64_v4bf16((LAS bf16x4*)p)); }

__global__ void __launch_bounds__(NTHREADS, 3)
gqa_attn(const __hip_bfloat16* __restrict__ Q, const __hip_bfloat16* __restrict__ K, const __hip_bfloat16* __restrict__ V,
         __hip_bfloat16* __restrict__ O, unsigned char* __restrict__ ws) {
  __shared__ __attribute__((aligned(1024))) char smem[LDS_BYTES];
  LAS char* lds = (LAS char*)smem;
  const unsigned tstart = (unsigned)__builtin_amdgcn_s_memrealtime();
  const int lane = threadIdx.x & 63;
  const int wave = __builtin_amdgcn_readfirstlane(threadIdx.x >> 6);
  const int pair = blockIdx.x & (NPAIR - 1), half = blockIdx.x >> 7;
  const int g = lane >> 4, c = lane & 15;

  if (__builtin_expect(wave >= NLOAD, 0)) {
    if (wave == 8) {
      if (NOX || NOCOMP) return;
      const unsigned char* wp = ws + (size_t)pair * (2 * BOX_BYTES);
      const int bo = half * BOX_BYTES, bi = (half ^ 1) * BOX_BYTES;
      unsigned ep_out, ep_in;
      asm volatile("s_load_dword %0, %2, %3\n\ts_load_dword %1, %2, %4\n\ts_waitcnt lgkmcnt(0)" : "=&s"(ep_out), "=&s"(ep_in) : "s"(wp), "s"(bo + EPOCH_OFF), "s"(bi + EPOCH_OFF) : "memory");
      const __amdgpu_buffer_rsrc_t rp = __builtin_amdgcn_make_buffer_rsrc((void*)wp, 0, 2 * BOX_BYTES, 0x00020000);
      const unsigned xb = (unsigned)(uintptr_t)lds + Q_OFF;
      {
#pragma unroll 1
        for (int j = 0; j < 6; ++j) asm volatile("s_barrier" ::: "memory");
        unsigned t0, t1;
        asm volatile("buffer_load_dword %0, off, %2, %3 offset:128\n\t"
                     "s_barrier\n\ts_barrier\n\ts_barrier\n\ts_waitcnt vmcnt(0)"
                     : "=&v"(t0), "=&v"(t1) : "s"(rp), "s"(bo + EPOCH_OFF), "v"(0), "s"((char*)O + (size_t)pair * (4 * SQ * 256)) : "memory");
#pragma unroll 1
        for (int j = 9; j < NA + 3; ++j) asm volatile("s_barrier" ::: "memory");
      }
      int spins = 1 << 20;
      unsigned long long tmp64;
#define BAR "s_barrier\n\t"
#define TS(n)
#define TS_CLOB
#define BAR0
#define BAR1 BAR
#define BAR2 BAR BAR
#define BAR3 BAR BAR BAR
#define BAR4 BAR BAR BAR BAR
#define BAR5 BAR BAR BAR BAR BAR
#define BAR6 BAR BAR BAR BAR BAR BAR
#define BARS_(n) BAR##n
#define BARS(n) BARS_(n)
      static_assert(L1 + L2 + L3 + 3 == NB, "barrier count");
#define FLAG_LOAD "buffer_load_dwordx4 v[48:51], %[po], %[rp], %[fi] offen sc1\n\t"
#define FLAG_TEST "v_cmp_ne_u32 vcc, %[tin], v49\n\tv_cmp_ne_u32 %[t], %[tin], v51\n\ts_nop 3\n\ts_or_b64 vcc, vcc, %[t]\n\t"
#define PART_IO(op, s0, s1, pol) \
      op " v[16:19], %[po], %[rp], " s0 " offen " pol "\n\t" op " v[20:23], %[po], %[rp], " s0 " offen offset:1024 " pol "\n\t" \
      op " v[24:27], %[po], %[rp], " s0 " offen offset:2048 " pol "\n\t" op " v[28:31], %[po], %[rp], " s0 " offen offset:3072 " pol "\n\t" \
      op " v[32:35], %[po], %[rp], " s1 " offen " pol "\n\t" op " v[36:39], %[po], %[rp], " s1 " offen offset:1024 " pol "\n\t" \
      op " v[40:43], %[po], %[rp], " s1 " offen offset:2048 " pol "\n\t" op " v[44:47], %[po], %[rp], " s1 " offen offset:3072 " pol "\n\t"
      asm volatile(
          TS(60)
          "ds_read_b128 v[16:19], %[xi]\n\tds_read_b128 v[20:23], %[xi] offset:1024\n\t"
          "ds_read_b128 v[24:27], %[xi] offset:4096\n\tds_read_b128 v[28:31], %[xi] offset:5120\n\t"
          "ds_read_b128 v[32:35], %[xi] offset:8192\n\tds_read_b128 v[36:39], %[xi] offset:9216\n\t"
          "ds_read_b128 v[40:43], %[xi] offset:12288\n\tds_read_b128 v[44:47], %[xi] offset:13312\n\t"
          "ds_read_b128 v[48:51], %[xs]\n\t"
          "s_waitcnt lgkmcnt(0)\n\t"
          PART_IO("buffer_store_dwordx4", "%[bo]", "%[bo2]", ST_POL)
          "v_mov_b32 v49, %[tout]\n\tv_mov_b32 v51, %[tout]\n\t" TS(62)
          BARS(L1) TS(64)
          "s_waitcnt vmcnt(0)\n\t" TS(66)
          "buffer_store_dwordx4 v[48:51], %[po], %[rp], %[fo] offen sc1\n\t"
          BARS(L2)
          FLAG_LOAD
          BARS(L3) TS(68)
          "s_waitcnt vmcnt(0)\n\t" TS(70)
          FLAG_TEST
          "s_cbranch_vccnz 1f\n\t"
          PART_IO("buffer_load_dwordx4", "%[bi]", "%[bi2]", LD_POL)
          BAR
          "3:\n\t"
          "ds_write_b128 %[xs], v[48:51]\n\t" TS(72)
          "s_waitcnt vmcnt(0)\n\t" TS(74)
          "ds_write_b128 %[xi], v[16:19]\n\tds_write_b128 %[xi], v[20:23] offset:1024\n\t"
          "ds_write_b128 %[xi], v[24:27] offset:4096\n\tds_write_b128 %[xi], v[28:31] offset:5120\n\t"
          "ds_write_b128 %[xi], v[32:35] offset:8192\n\tds_write_b128 %[xi], v[36:39] offset:9216\n\t"
          "ds_write_b128 %[xi], v[40:43] offset:12288\n\tds_write_b128 %[xi], v[44:47] offset:13312\n\t"
          "s_waitcnt lgkmcnt(0)\n\t" BAR
          "buffer_store_dword v49, %[eo], %[rp], %[bi] offen sc1\n\t"
          "s_branch 4f\n"
          "1:\n\t"
          FLAG_LOAD
          BAR
          "2:\n\t"
          "s_waitcnt vmcnt(0)\n\t"
          FLAG_TEST
          "s_cbranch_vccz 5f\n\t"
          FLAG_LOAD
          "s_sub_u32 %[spins], %[spins], 1\n\t"
          "s_cbranch_scc0 2b\n\t"
          "s_waitcnt vmcnt(0)\n\t"
          "v_mov_b32 v50, 0x7fc00000\n"
          "5:\n\t"
          "s_sub_u32 %[spins], %[spins], 1\n\t"
          PART_IO("buffer_load_dwordx4", "%[bi]", "%[bi2]", LD_POL)
          "s_branch 3b\n"
          "4:"
          : [spins] "+s"(spins), [t] "=&s"(tmp64)
          : [tin] "s"(ep_in + TAG_STEP), [tout] "s"(ep_out + TAG_STEP), [bi] "s"(bi), [bi2] "s"(bi + 4096), [fi] "s"(bi + FLAG_OFF),
            [bo] "s"(bo), [bo2] "s"(bo + 4096), [fo] "s"(bo + FLAG_OFF), [po] "v"(lane * 16), [eo] "v"(lane == 0 ? EPOCH_OFF : 2 * BOX_BYTES),
            [xi] "v"(xb + lane * 16), [xs] "v"(xb + g * XCH_BYTES + 2048 + c * 16), [rp] "s"(rp)
          : "memory", "vcc", "scc", "v16", "v17", "v18", "v19", "v20", "v21", "v22", "v23", "v24", "v25", "v26", "v27", "v28", "v29", "v30", "v31",
            "v32", "v33", "v34", "v35", "v36", "v37", "v38", "v39", "v40", "v41", "v42", "v43", "v44", "v45", "v46", "v47", "v48", "v49", "v50", "v51" TS_CLOB);
      DIAG_OUT(spins == (1 << 20) ? 2 : spins == (1 << 20) - 1 ? 3 : 5);
      return;
    }
    const int head = wave - NLOAD;
    if (PRIO) __builtin_amdgcn_s_setprio(PRIO);
    const __amdgpu_buffer_rsrc_t ro = __builtin_amdgcn_make_buffer_rsrc((char*)O + ((size_t)pair * 4 + head) * (SQ * 256), 0, SQ * 256, 0x00020000);
    const unsigned ka0 = c * 256 + ((g ^ ((c & 7) << 1)) << 4);
    const int vr = 4 * g + (c >> 2);
    const unsigned va0 = KT_BYTES + vr * 128 + (((vr >> 1) & 3) << 5) + (c & 3) * 8;
    unsigned xi = Q_OFF + head * XCH_BYTES + lane * 16;
    u32x4 ones = {0x3F803F80u, 0x3F803F80u, 0x3F803F80u, 0x3F803F80u};
    asm volatile("" : "+v"(ones), "+v"(xi));
    f32x4 o[8], lsum = {0.f, 0.f, 0.f, 0.f};
#pragma unroll
    for (int db = 0; db < 8; ++db) { o[db] = f32x4{0.f, 0.f, 0.f, 0.f}; asm volatile("" : "+v"(o[db])); }
    float nm = 3.0e38f;
    unsigned sb = SLOT_BYTES;
    unsigned kx[4];
#pragma unroll
    for (int st = 0; st < 4; ++st) kx[st] = (ka0 ^ (st << 6)) + sb;
    BARRIER();
    u32x4 q[4], kf[4][4];
#pragma unroll
    for (int st = 0; st < 4; ++st) q[st] = *(const LAS u32x4*)(lds + (Q_OFF + head * 4096) + (ka0 ^ (st << 6)));
#pragma unroll
    for (int sub = 0; sub < 4; ++sub)
#pragma unroll
      for (int st = 0; st < 4; ++st) kf[sub][st] = *(const LAS u32x4*)(lds + (ka0 ^ (st << 6)) + sub * 4096);
    asm volatile("" : "+v"(kf[0][0]), "+v"(kf[0][1]), "+v"(kf[0][2]), "+v"(kf[0][3]), "+v"(kf[1][0]), "+v"(kf[1][1]), "+v"(kf[1][2]), "+v"(kf[1][3]),
                 "+v"(kf[2][0]), "+v"(kf[2][1]), "+v"(kf[2][2]), "+v"(kf[2][3]), "+v"(kf[3][0]), "+v"(kf[3][1]), "+v"(kf[3][2]), "+v"(kf[3][3]),
                 "+v"(q[0]), "+v"(q[1]), "+v"(q[2]), "+v"(q[3]));
    BARRIER();
    int left = NI;
    asm volatile("" : "+s"(left));
#pragma unroll 1
    do {
      f32x4 s[4], x[4];
      u32x4 p[2];
      u32x2 vf[8][2];
      const f32x4 zero = {0.f, 0.f, 0.f, 0.f};
      sb = (sb + SLOT_BYTES) & (RING_BYTES - 1);
      unsigned vx[4];
#define QK1(sub, st) do { s[sub] = mfma(kf[sub][st], q[st], (st) ? s[sub] : zero); kf[sub][st] = *(const LAS u32x4*)(lds + kx[st] + (sub) * 4096); } while (0)
#define FM(sub) x[sub] = s[sub] * C2 + nm
#define EX(sub, r) x[sub][r] = __builtin_amdgcn_exp2f(x[sub][r])
#define CV(sub, h) p[(sub) >> 1][2 * ((sub) & 1) + (h)] = pk(x[sub][2 * (h)], x[sub][2 * (h) + 1])
#define VX(i) do { vx[i] = (i) ? vx[0] ^ ((i) << 5) : va0 + sb; asm volatile("" : "+v"(vx[i])); } while (0)
#define VRD(i, off) do { vf[i][0] = trd(lds + vx[(i) & 3] + ((i) >> 2) * 4096 + (off)); vf[i][1] = trd(lds + vx[(i) & 3] + ((i) >> 2) * 4096 + 2048 + (off)); } while (0)
#define PV1(i, ob) o[(ob) + ((i) & 3)] = mfma(u32x4{vf[i][0][0], vf[i][0][1], vf[i][1][0], vf[i][1][1]}, p[(i) >> 2], o[(ob) + ((i) & 3)])
      QK1(0, 0); SB(); QK1(1, 0); VX(0); SB(); QK1(0, 1); VX(1); SB(); QK1(1, 1); VX(2); SB();
      QK1(0, 2); VX(3); SB(); QK1(1, 2); SB(); QK1(0, 3); SB(); QK1(1, 3); SB();
      QK1(2, 0); SB(); QK1(3, 0); FM(0); SB();
      QK1(2, 1); FM(1); EX(0, 0); SB(); QK1(3, 1); EX(0, 1); EX(0, 2); SB();
      QK1(2, 2); EX(0, 3); EX(1, 0); SB(); QK1(3, 2); EX(1, 1); EX(1, 2); SB();
      QK1(2, 3); EX(1, 3); CV(0, 0); SB(); QK1(3, 3); CV(0, 1); CV(1, 0); CV(1, 1); SB();
      unsigned any = (p[0][0] | p[0][1] | p[0][2]) | p[0][3];
      asm volatile("" : "+v"(any) :: "memory");
      SB();
      FM(2); FM(3); SB();
      EX(2, 0); EX(2, 1); EX(2, 2); EX(2, 3); EX(3, 0); EX(3, 1); EX(3, 2); EX(3, 3); SB();
      CV(2, 0); CV(2, 1); CV(3, 0); CV(3, 1);
      any = (any | p[1][0] | p[1][1]) | p[1][2] | p[1][3];
      if (__builtin_expect(__builtin_amdgcn_ballot_w64((any & GE2) != 0) != 0, 0)) {
        float t = s[0][0];
#pragma unroll
        for (int sub = 0; sub < 4; ++sub)
#pragma unroll
          for (int r = 0; r < 4; ++r) t = fmaxf(t, s[sub][r]);
        const float nn = fminf(nm, -fmaf(xmax4(t), C2, SLACK));
        const float alpha = __builtin_amdgcn_exp2f(nn - nm);
        nm = nn;
#pragma unroll
        for (int db = 0; db < 8; ++db) o[db] *= alpha;
        lsum *= alpha;
#pragma unroll
        for (int sub = 0; sub < 4; ++sub) { FM(sub); EX(sub, 0); EX(sub, 1); EX(sub, 2); EX(sub, 3); CV(sub, 0); CV(sub, 1); }
      }
      BARRIER();
      VRD(0, 0); SB(); VRD(1, 0); SB(); VRD(2, 0); SB(); VRD(3, 0); SB(); VRD(4, 0); SB(); VRD(5, 0); SB();
      lsum = mfma(ones, p[0], lsum);
      lsum = mfma(ones, p[1], lsum);
      SB();
#define KX(i) do { kx[i] = (i) ? kx[0] ^ ((i) << 6) : ka0 + sb; asm volatile("" : "+v"(kx[i])); } while (0)
      PV1(0, 0); SB(); VRD(6, 0); SB(); PV1(1, 0); SB(); VRD(7, 0); SB(); PV1(2, 0); SB(); PV1(3, 0); KX(0); SB();
      PV1(4, 0); KX(1); SB(); PV1(5, 0); KX(2); SB(); PV1(6, 0); KX(3); SB(); PV1(7, 0); SB();
      if (__builtin_expect(left > NB, 0)) {
        VRD(0, VH_BYTES); SB(); VRD(1, VH_BYTES); SB(); VRD(2, VH_BYTES); SB(); VRD(3, VH_BYTES); SB(); VRD(4, VH_BYTES); SB();
        PV1(0, 4); SB(); VRD(5, VH_BYTES); SB(); PV1(1, 4); SB(); VRD(6, VH_BYTES); SB(); PV1(2, 4); SB(); VRD(7, VH_BYTES); SB(); PV1(3, 4); PV1(4, 4); PV1(5, 4); PV1(6, 4); PV1(7, 4); SB();
        if (left == NB + 1) {
          *(LAS f32x4*)(lds + xi + 2048) = f32x4{nm, 0.f, lsum[0], 0.f};
#pragma unroll
          for (int i = 0; i < 2; ++i) {
            const f32x4 a = o[4 + 2 * i], b = o[5 + 2 * i];
            *(LAS u32x4*)(lds + xi + i * 1024) = u32x4{pk(a[0], a[1]), pk(a[2], a[3]), pk(b[0], b[1]), pk(b[2], b[3])};
          }
        }
      }
    } while (--left != 0);
    RSTAMP(52);
    {
      asm volatile("" : "+v"(xi));
      const unsigned ln = (xi >> 4) & 63, xb = Q_OFF + head * XCH_BYTES;
      const unsigned xw = xb + (ln & 15) * 144 + (ln >> 4) * 8, xr = xb + (ln >> 3) * 144 + (ln & 7) * 16;
      const int oo = (ln >> 3) * 256 + half * 128 + (ln & 7) * 16;
      const f32x4 st = *(LAS f32x4*)(lds + (xi & ~0x300u) + 2048);
      const u32x4 yy[2] = {*(LAS u32x4*)(lds + xi), *(LAS u32x4*)(lds + xi + 1024)};
      SB();
      f32x4 y[4];
#pragma unroll
      for (int i = 0; i < 4; ++i) {
        const unsigned y0 = yy[i >> 1][2 * (i & 1)], y1 = yy[i >> 1][2 * (i & 1) + 1];
        y[i] = f32x4{__uint_as_float(y0 << 16), __uint_as_float(y0 & 0xFFFF0000u), __uint_as_float(y1 << 16), __uint_as_float(y1 & 0xFFFF0000u)};
      }
      const float nn = fminf(nm, st[0]);
      const float b1 = __builtin_amdgcn_exp2f(nn - nm), b2 = __builtin_amdgcn_exp2f(nn - st[0]);
      const float inv = __builtin_amdgcn_rcpf(fmaf(b1, lsum[0], b2 * st[2]));
      const float w1 = b1 * inv, w2 = b2 * inv;
#pragma unroll
      for (int i = 0; i < 4; ++i) {
        const f32x4 r = o[i] * w1 + y[i] * w2;
        *(LAS u32x2*)(lds + xw + i * 32) = u32x2{pk(r[0], r[1]), pk(r[2], r[3])};
      }
      asm volatile("" ::: "memory");
      const u32x4 r0 = *(LAS u32x4*)(lds + xr), r1 = *(LAS u32x4*)(lds + xr + 8 * 144);
      __builtin_amdgcn_raw_buffer_store_b128(r0, ro, oo, 0, OUT_AUX);
      __builtin_amdgcn_raw_buffer_store_b128(r1, ro, oo + 2048, 0, OUT_AUX);
    }
    DIAG_OUT(4);
    return;
  }

  const int ko0 = g * 256 + ((c ^ (g << 1)) << 4), ko1 = ko0 ^ 128;
  const int vo0 = (lane >> 3) * 256 + half * 128 + (((lane & 7) ^ ((lane >> 3) & 6)) << 4);
  const int vo1 = vo0 + 1024, vo2 = vo0 ^ 128, vo3 = vo2 + 1024;
  const __amdgpu_buffer_rsrc_t rk = __builtin_amdgcn_make_buffer_rsrc((char*)K + (size_t)pair * (SK * 256), 0, KV_BYTES, 0x00020000);
  const __amdgpu_buffer_rsrc_t rq = __builtin_amdgcn_make_buffer_rsrc((char*)Q + (size_t)pair * KT_BYTES, 0, KT_BYTES, 0x00020000);
  const __amdgpu_buffer_rsrc_t rv = __builtin_amdgcn_make_buffer_rsrc((char*)V + (size_t)pair * (SK * 256), 0, KV_BYTES, 0x00020000);
  const unsigned lbase = (unsigned)(uintptr_t)lds;
#define KTILE(POL, r, so, dst) \
    asm volatile("s_mov_b32 m0, %0\n\ts_nop 0\n\t" \
                 "buffer_load_dwordx4 %1, %3, %4 offen " POL " lds\n\t" \
                 "buffer_load_dwordx4 %2, %3, %4 offen offset:1024 " POL " lds\n\t" \
                 "buffer_load_dwordx4 %1, %3, %4 offen offset:2048 " POL " lds\n\t" \
                 "buffer_load_dwordx4 %2, %3, %4 offen offset:3072 " POL " lds" \
                 :: "s"(lbase + (dst) + wave * 4096), "v"(ko0), "v"(ko1), "s"(r), "s"((so) + wave * 4096) : "memory")
#define VHALF(a, b, so, dst) \
    asm volatile("s_mov_b32 m0, %0\n\ts_nop 0\n\t" \
                 "buffer_load_dwordx4 %1, %3, %4 offen nt lds\n\t" \
                 "buffer_load_dwordx4 %2, %3, %4 offen offset:1024 nt lds" \
                 :: "s"(lbase + (dst) + wave * 2048), "v"(a), "v"(b), "s"(rv), "s"((so) + wave * 4096) : "memory")
#define SLOT(s) (((s) & 3) * SLOT_BYTES)
  const int rot = (pair & 3) * 8;
#define BLK(u) ((wave | ((((u) >> 1) & 3) << 2) | (((u) & 1) << 4) | (((u) >> 3) << 5)) ^ (rot * 4))
#define OFF1(t) ((BLK(2 * (t) + half) - wave) * 4096)
#define OFF2(t) ((BLK(NA + (t)) - wave) * 4096)
#define K1(t) KTILE("nt", rk, OFF1(t), SLOT(t))
#define K2(t) KTILE(K2_POL, rk, OFF2(t), SLOT(t))
#define V1(t) do { VHALF(vo0, vo1, OFF1(t), SLOT((t) + 2) + KT_BYTES); VHALF(vo2, vo3, OFF1(t), SLOT((t) + 2) + KT_BYTES + VH_BYTES); } while (0)
#define V2(t) VHALF(vo0, vo1, OFF2(t), SLOT((t) + 2) + KT_BYTES)
  unsigned due = tstart + PACE0;
#define PACE_WAIT(d) do { while ((int)((unsigned)__builtin_amdgcn_s_memrealtime() - due) < 0) __builtin_amdgcn_s_sleep(1); due += (d); } while (0)
#define PACE_WAIT2(d)
  due -= (PRO + 2) * (PACE / 2);
  K1(0); KTILE(K2_POL, rq, 0, Q_OFF);
  if (PRO >= 3) PACE_WAIT(PACE / 2);
  K1(1);
  if (PRO >= 2) PACE_WAIT(PACE / 2);
  V1(0);
  wait_vm<12>(); PACE_WAIT(PACE / 2); K1(2);
  wait_vm<12>(); LBARRIER(); PACE_WAIT(PACE / 2); V1(1);
  wait_vm<12>(); LBARRIER(); PACE_WAIT(PACE / 2); K1(3);
#pragma unroll 1
  for (int s = 2; s < NA - 2; ++s) {
    wait_vm<12>(); PACE_WAIT(PACE / 2); V1(s);
    wait_vm<12>(); LBARRIER(); PACE_WAIT(PACE / 2); K1(s + 2);
  }
  wait_vm<12>(); PACE_WAIT(PACE / 2); V1(NA - 2); wait_vm<12>(); LBARRIER(); PACE_WAIT(PACE / 4); K2(NA);
  wait_vm<8>(); LBARRIER(); PACE_WAIT(PACE / 2); V1(NA - 1); PACE_WAIT2(PACE / 4); K2(NA + 1);
  wait_vm<12>(); V2(NA); wait_vm<10>(); K2(NA + 2); LBARRIER();
#pragma unroll 1
  for (int s = NA + 1; s < NI - 2; ++s) {
    wait_vm<10>(); V2(s);
    wait_vm<8>(); K2(s + 2); LBARRIER();
  }
  V2(NI - 2);
  wait_vm<8>(); LBARRIER();
  V2(NI - 1);
  wait_vm<4>(); LBARRIER();
  wait_vm<2>(); LBARRIER();
  wait_vm<0>(); LBARRIER();
  DIAG_OUT(0);
}

}

extern "C" void kernel_launch(void* const* d_in, const int* in_sizes, int n_in, void* d_out, int out_size, void* d_ws, size_t ws_size,
                              hipStream_t stream) {
  gqa_attn<<<2 * NPAIR, NTHREADS, 0, stream>>>((const __hip_bfloat16*)d_in[0], (const __hip_bfloat16*)d_in[1], (const __hip_bfloat16*)d_in[2],
                                                (__hip_bfloat16*)d_out, (unsigned char*)d_ws);
}
```

```cpp
#define PACE0 390
#define PACE 112
#include <hip/hip_runtime.h>
#include <hip/hip_bf16.h>
#include <stdint.h>

namespace {

constexpr int B_ = 16, HKV = 8, SQ = 16, SK = 2048;
constexpr int TK = 64, NT = SK / TK;
constexpr int NA = 12, NB = NT - 2 * NA, NI = NA + NB;
constexpr int NPAIR = B_ * HKV;
constexpr int KT_BYTES = TK * 256, VH_BYTES = TK * 128;
constexpr int SLOT_BYTES = KT_BYTES + 2 * VH_BYTES;
constexpr int RING_BYTES = 4 * SLOT_BYTES;
constexpr int Q_OFF = RING_BYTES;
constexpr int XCH_BYTES = 4096;
constexpr int LDS_BYTES = RING_BYTES + 4 * XCH_BYTES;
constexpr int NTHREADS = 576;
constexpr float C2 = 0.08838834764831845f * 1.4426950408889634f;
constexpr float SLACK = 7.0f;
constexpr unsigned GE2 = 0x40004000u;
constexpr int FLAG_OFF = 8192, EPOCH_OFF = FLAG_OFF + 1024, BOX_BYTES = EPOCH_OFF + 256;
static_assert(NB == 8, "the mail wave's barrier count");

typedef __attribute__((ext_vector_type(8))) __bf16 bf16x8;
typedef __attribute__((ext_vector_type(4))) __bf16 bf16x4;
typedef __attribute__((ext_vector_type(2))) __bf16 bf16x2;
typedef __attribute__((ext_vector_type(4))) float f32x4;
typedef __attribute__((ext_vector_type(2))) float f32x2;
typedef __attribute__((ext_vector_type(4))) unsigned u32x4;
typedef __attribute__((ext_vector_type(2))) unsigned u32x2;
#define LAS __attribute__((address_space(3)))

#define BARRIER() asm volatile("s_waitcnt lgkmcnt(0)\n\ts_barrier" ::: "memory")
#define LBARRIER() asm volatile("s_barrier" ::: "memory")
#define SB() __builtin_amdgcn_sched_barrier(0)

template <int N> __device__ __forceinline__ void wait_vm() { asm volatile("s_waitcnt vmcnt(%0)" ::"n"(N) : "memory"); }

__device__ __forceinline__ float xmax4(float x) {
  auto s = __builtin_amdgcn_permlane16_swap(__float_as_uint(x), __float_as_uint(x), false, false);
  x = fmaxf(__uint_as_float(s[0]), __uint_as_float(s[1]));
  auto t = __builtin_amdgcn_permlane32_swap(__float_as_uint(x), __float_as_uint(x), false, false);
  return fmaxf(__uint_as_float(t[0]), __uint_as_float(t[1]));
}
__device__ __forceinline__ unsigned pk(float a, float b) { f32x2 v = {a, b}; return __builtin_bit_cast(unsigned, __builtin_convertvector(v, bf16x2)); }
__device__ __forceinline__ f32x4 mfma(u32x4 a, u32x4 b, f32x4 c) {
  return __builtin_amdgcn_mfma_f32_16x16x32_bf16(__builtin_bit_cast(bf16x8, a), __builtin_bit_cast(bf16x8, b), c, 0, 0, 0);
}
__device__ __forceinline__ u32x2 trd(LAS char* p) { return __builtin_bit_cast(u32x2, __builtin_amdgcn_ds_read_tr16_b64_v4bf16((LAS bf16x4*)p)); }

__global__ void __launch_bounds__(NTHREADS, 3)
gqa_attn(const __hip_bfloat16* __restrict__ Q, const __hip_bfloat16* __restrict__ K, const __hip_bfloat16* __restrict__ V,
         __hip_bfloat16* __restrict__ O, unsigned char* __restrict__ ws) {
  __shared__ __attribute__((aligned(1024))) char smem[LDS_BYTES];
  LAS char* lds = (LAS char*)smem;
  const unsigned long long t64 = __builtin_amdgcn_s_memrealtime();
  const int lane = threadIdx.x & 63;
  const int wave = __builtin_amdgcn_readfirstlane(threadIdx.x >> 6);
  const int pair = ((blockIdx.x >> 3) & 7) | ((blockIdx.x & 7) << 3) | (blockIdx.x & 64), half = blockIdx.x >> 7;
  const int g = lane >> 4, c = lane & 15;
  const int ko0 = g * 256 + ((c ^ (g << 1)) << 4), ko1 = ko0 ^ 128;
  const int kso = ((half << 1) | ((pair & 3) << 3)) * KT_BYTES + wave * 4096;
  asm volatile("" :: "s"(K), "s"(Q), "s"(t64), "s"(kso), "s"(pair << 19), "v"(ko0), "v"(ko1));

  if (__builtin_expect(wave >= 4, 0)) {
    if (wave == 8) {
      const __amdgpu_buffer_rsrc_t rp = __builtin_amdgcn_make_buffer_rsrc(ws + (size_t)pair * (2 * BOX_BYTES), 0, 2 * BOX_BYTES, 0x00020000);
      const int bo = half * BOX_BYTES, bi = (half ^ 1) * BOX_BYTES;
      const unsigned xb = (unsigned)(uintptr_t)lds + Q_OFF;
      int spins = 1 << 20;
      unsigned long long tmp64;
#define BAR "s_barrier\n\t"
#define FLAG_LOAD "buffer_load_dwordx4 v[48:51], %[po], %[rp], %[fi] offen sc1\n\t"
#define FLAG_TEST "v_cmp_ne_u32 vcc, v53, v49\n\tv_cmp_ne_u32 %[t], v53, v51\n\ts_nop 3\n\ts_or_b64 vcc, vcc, %[t]\n\t"
#define PART_IO(op, s0, s1) \
      op " v[16:19], %[po], %[rp], " s0 " offen sc1\n\t" op " v[20:23], %[po], %[rp], " s0 " offen offset:1024 sc1\n\t" \
      op " v[24:27], %[po], %[rp], " s0 " offen offset:2048 sc1\n\t" op " v[28:31], %[po], %[rp], " s0 " offen offset:3072 sc1\n\t" \
      op " v[32:35], %[po], %[rp], " s1 " offen sc1\n\t" op " v[36:39], %[po], %[rp], " s1 " offen offset:1024 sc1\n\t" \
      op " v[40:43], %[po], %[rp], " s1 " offen offset:2048 sc1\n\t" op " v[44:47], %[po], %[rp], " s1 " offen offset:3072 sc1\n\t"
      asm volatile(
          BAR BAR BAR BAR BAR BAR
          "buffer_load_dword v52, off, %[rp], %[fo] offset:1024 sc1\n\t"
          "buffer_load_dword v53, off, %[rp], %[fi] offset:1024 sc1\n\t"
          BAR BAR BAR BAR BAR BAR BAR BAR
          "s_waitcnt vmcnt(0)\n\t"
          "v_add_u32 v52, 0x9e3779b1, v52\n\tv_add_u32 v53, 0x9e3779b1, v53\n\t"
          BAR
          "ds_read_b128 v[16:19], %[xi]\n\tds_read_b128 v[20:23], %[xi] offset:1024\n\t"
          "ds_read_b128 v[24:27], %[xi] offset:4096\n\tds_read_b128 v[28:31], %[xi] offset:5120\n\t"
          "ds_read_b128 v[32:35], %[xi] offset:8192\n\tds_read_b128 v[36:39], %[xi] offset:9216\n\t"
          "ds_read_b128 v[40:43], %[xi] offset:12288\n\tds_read_b128 v[44:47], %[xi] offset:13312\n\t"
          "ds_read_b128 v[48:51], %[xs]\n\t"
          "s_waitcnt lgkmcnt(0)\n\t"
          PART_IO("buffer_store_dwordx4", "%[bo]", "%[bo2]")
          "v_mov_b32 v49, v52\n\tv_mov_b32 v51, v52\n\t"
          BAR
          "s_waitcnt vmcnt(0)\n\t"
          "buffer_store_dwordx4 v[48:51], %[po], %[rp], %[fo] offen sc1\n\t"
          BAR BAR BAR
          FLAG_LOAD
          BAR
          "s_waitcnt vmcnt(0)\n\t"
          FLAG_TEST
          "s_cbranch_vccnz 1f\n\t"
          PART_IO("buffer_load_dwordx4", "%[bi]", "%[bi2]")
          BAR
          "3:\n\t"
          "ds_write_b128 %[xs], v[48:51]\n\t"
          "s_waitcnt vmcnt(0)\n\t"
          "ds_write_b128 %[xi], v[16:19]\n\tds_write_b128 %[xi], v[20:23] offset:1024\n\t"
          "ds_write_b128 %[xi], v[24:27] offset:4096\n\tds_write_b128 %[xi], v[28:31] offset:5120\n\t"
          "ds_write_b128 %[xi], v[32:35] offset:8192\n\tds_write_b128 %[xi], v[36:39] offset:9216\n\t"
          "ds_write_b128 %[xi], v[40:43] offset:12288\n\tds_write_b128 %[xi], v[44:47] offset:13312\n\t"
          "s_waitcnt lgkmcnt(0)\n\t"
          BAR
          "buffer_store_dword v53, %[eo], %[rp], %[bi] offen sc1\n\t"
          "s_branch 4f\n"
          "1:\n\t"
          FLAG_LOAD
          BAR
          "2:\n\t"
          "s_waitcnt vmcnt(0)\n\t"
          FLAG_TEST
          "s_cbranch_vccz 5f\n\t"
          FLAG_LOAD
          "s_sub_u32 %[spins], %[spins], 1\n\t"
          "s_cbranch_scc0 2b\n\t"
          "s_waitcnt vmcnt(0)\n\t"
          "v_mov_b32 v50, 0x7fc00000\n"
          "5:\n\t"
          PART_IO("buffer_load_dwordx4", "%[bi]", "%[bi2]")
          "s_branch 3b\n"
          "4:"
          : [spins] "+s"(spins), [t] "=&s"(tmp64)
          : [bi] "s"(bi), [bi2] "s"(bi + 4096), [fi] "s"(bi + FLAG_OFF), [bo] "s"(bo), [bo2] "s"(bo + 4096), [fo] "s"(bo + FLAG_OFF),
            [po] "v"(lane * 16), [eo] "v"(lane == 0 ? EPOCH_OFF : 2 * BOX_BYTES),
            [xi] "v"(xb + lane * 16), [xs] "v"(xb + g * XCH_BYTES + 2048 + c * 16), [rp] "s"(rp)
          : "memory", "vcc", "scc", "v16", "v17", "v18", "v19", "v20", "v21", "v22", "v23", "v24", "v25", "v26", "v27", "v28", "v29", "v30", "v31",
            "v32", "v33", "v34", "v35", "v36", "v37", "v38", "v39", "v40", "v41", "v42", "v43", "v44", "v45", "v46", "v47", "v48", "v49", "v50", "v51",
            "v52", "v53");
      return;
    }

    const int head = wave - 4;
    const unsigned ka0 = c * 256 + ((g ^ ((c & 7) << 1)) << 4);
    const int vr = 4 * g + (c >> 2);
    const unsigned va0 = KT_BYTES + vr * 128 + (((vr >> 1) & 3) << 5) + (c & 3) * 8;
    unsigned xi = Q_OFF + head * XCH_BYTES + lane * 16;
    u32x4 ones = {0x3F803F80u, 0x3F803F80u, 0x3F803F80u, 0x3F803F80u};
    asm volatile("" : "+v"(ones), "+v"(xi));
    f32x4 o[8], lsum = {0.f, 0.f, 0.f, 0.f};
#pragma unroll
    for (int db = 0; db < 8; ++db) { o[db] = f32x4{0.f, 0.f, 0.f, 0.f}; asm volatile("" : "+v"(o[db])); }
    float nm = 3.0e38f;
    unsigned sb = SLOT_BYTES;
    unsigned kx[4];
#pragma unroll
    for (int st = 0; st < 4; ++st) kx[st] = (ka0 ^ (st << 6)) + sb;
    BARRIER();
    u32x4 q[4], kf[4][4];
#pragma unroll
    for (int st = 0; st < 4; ++st) q[st] = *(const LAS u32x4*)(lds + (Q_OFF + head * 4096) + (ka0 ^ (st << 6)));
#pragma unroll
    for (int sub = 0; sub < 4; ++sub)
#pragma unroll
      for (int st = 0; st < 4; ++st) kf[sub][st] = *(const LAS u32x4*)(lds + (ka0 ^ (st << 6)) + sub * 4096);
    asm volatile("" : "+v"(kf[0][0]), "+v"(kf[0][1]), "+v"(kf[0][2]), "+v"(kf[0][3]), "+v"(kf[1][0]), "+v"(kf[1][1]), "+v"(kf[1][2]), "+v"(kf[1][3]),
                 "+v"(kf[2][0]), "+v"(kf[2][1]), "+v"(kf[2][2]), "+v"(kf[2][3]), "+v"(kf[3][0]), "+v"(kf[3][1]), "+v"(kf[3][2]), "+v"(kf[3][3]),
                 "+v"(q[0]), "+v"(q[1]), "+v"(q[2]), "+v"(q[3]));
    BARRIER();
    int left = NI;
    asm volatile("" : "+s"(left));
#pragma unroll 1
    do {
      f32x4 s[4], x[4];
      u32x4 p[2];
      u32x2 vf[8][2];
      const f32x4 zero = {0.f, 0.f, 0.f, 0.f};
      sb = (sb + SLOT_BYTES) & (RING_BYTES - 1);
      unsigned vx[4];
#define QK1(sub, st) do { s[sub] = mfma(kf[sub][st], q[st], (st) ? s[sub] : zero); kf[sub][st] = *(const LAS u32x4*)(lds + kx[st] + (sub) * 4096); } while (0)
#define FM(sub) x[sub] = s[sub] * C2 + nm
#define EX(sub, r) x[sub][r] = __builtin_amdgcn_exp2f(x[sub][r])
#define CV(sub, h) p[(sub) >> 1][2 * ((sub) & 1) + (h)] = pk(x[sub][2 * (h)], x[sub][2 * (h) + 1])
#define VX(i) do { vx[i] = (i) ? vx[0] ^ ((i) << 5) : va0 + sb; asm volatile("" : "+v"(vx[i])); } while (0)
#define KX(i) do { kx[i] = (i) ? kx[0] ^ ((i) << 6) : ka0 + sb; asm volatile("" : "+v"(kx[i])); } while (0)
#define VRD(i, off) do { vf[i][0] = trd(lds + vx[(i) & 3] + ((i) >> 2) * 4096 + (off)); vf[i][1] = trd(lds + vx[(i) & 3] + ((i) >> 2) * 4096 + 2048 + (off)); } while (0)
#define PV1(i, ob) o[(ob) + ((i) & 3)] = mfma(u32x4{vf[i][0][0], vf[i][0][1], vf[i][1][0], vf[i][1][1]}, p[(i) >> 2], o[(ob) + ((i) & 3)])
      QK1(0, 0); SB(); QK1(1, 0); VX(0); SB(); QK1(0, 1); VX(1); SB(); QK1(1, 1); VX(2); SB();
      QK1(0, 2); VX(3); SB(); QK1(1, 2); SB(); QK1(0, 3); SB(); QK1(1, 3); SB();
      QK1(2, 0); SB(); QK1(3, 0); FM(0); SB();
      QK1(2, 1); FM(1); EX(0, 0); SB(); QK1(3, 1); EX(0, 1); EX(0, 2); SB();
      QK1(2, 2); EX(0, 3); EX(1, 0); SB(); QK1(3, 2); EX(1, 1); EX(1, 2); SB();
      QK1(2, 3); EX(1, 3); CV(0, 0); SB(); QK1(3, 3); CV(0, 1); CV(1, 0); CV(1, 1); SB();
      unsigned any = (p[0][0] | p[0][1] | p[0][2]) | p[0][3];
      asm volatile("" : "+v"(any) :: "memory");
      SB();
      FM(2); FM(3); SB();
      EX(2, 0); EX(2, 1); EX(2, 2); EX(2, 3); EX(3, 0); EX(3, 1); EX(3, 2); EX(3, 3); SB();
      CV(2, 0); CV(2, 1); CV(3, 0); CV(3, 1);
      any = (any | p[1][0] | p[1][1]) | p[1][2] | p[1][3];
      if (__builtin_expect(__builtin_amdgcn_ballot_w64((any & GE2) != 0) != 0, 0)) {
        float t = s[0][0];
#pragma unroll
        for (int sub = 0; sub < 4; ++sub)
#pragma unroll
          for (int r = 0; r < 4; ++r) t = fmaxf(t, s[sub][r]);
        const float nn = fminf(nm, -fmaf(xmax4(t), C2, SLACK));
        const float alpha = __builtin_amdgcn_exp2f(nn - nm);
        nm = nn;
#pragma unroll
        for (int db = 0; db < 8; ++db) o[db] *= alpha;
        lsum *= alpha;
#pragma unroll
        for (int sub = 0; sub < 4; ++sub) { FM(sub); EX(sub, 0); EX(sub, 1); EX(sub, 2); EX(sub, 3); CV(sub, 0); CV(sub, 1); }
      }
      BARRIER();
      VRD(0, 0); SB(); VRD(1, 0); SB(); VRD(2, 0); SB(); VRD(3, 0); SB(); VRD(4, 0); SB(); VRD(5, 0); SB();
      lsum = mfma(ones, p[0], lsum);
      lsum = mfma(ones, p[1], lsum);
      SB();
      PV1(0, 0); SB(); VRD(6, 0); SB(); PV1(1, 0); SB(); VRD(7, 0); SB(); PV1(2, 0); SB(); PV1(3, 0); KX(0); SB();
      PV1(4, 0); KX(1); SB(); PV1(5, 0); KX(2); SB(); PV1(6, 0); KX(3); SB(); PV1(7, 0); SB();
      if (__builtin_expect(left > NB, 0)) {
        VRD(0, VH_BYTES); SB(); VRD(1, VH_BYTES); SB(); VRD(2, VH_BYTES); SB(); VRD(3, VH_BYTES); SB(); VRD(4, VH_BYTES); SB();
        PV1(0, 4); SB(); VRD(5, VH_BYTES); SB(); PV1(1, 4); SB(); VRD(6, VH_BYTES); SB(); PV1(2, 4); SB(); VRD(7, VH_BYTES); SB();
        PV1(3, 4); PV1(4, 4); PV1(5, 4); PV1(6, 4); PV1(7, 4); SB();
        if (left == NB + 1) {
          *(LAS f32x4*)(lds + xi + 2048) = f32x4{nm, 0.f, lsum[0], 0.f};
#pragma unroll
          for (int i = 0; i < 2; ++i) {
            const f32x4 a = o[4 + 2 * i], b = o[5 + 2 * i];
            *(LAS u32x4*)(lds + xi + i * 1024) = u32x4{pk(a[0], a[1]), pk(a[2], a[3]), pk(b[0], b[1]), pk(b[2], b[3])};
          }
        }
      }
    } while (--left != 0);
    {
      asm volatile("" : "+v"(xi));
      const unsigned ln = (xi >> 4) & 63, xb = Q_OFF + head * XCH_BYTES;
      const unsigned xw = xb + (ln & 15) * 144 + (ln >> 4) * 8, xr = xb + (ln >> 3) * 144 + (ln & 7) * 16;
      const int oo = (ln >> 3) * 256 + half * 128 + (ln & 7) * 16;
      const f32x4 st = *(LAS f32x4*)(lds + (xi & ~0x300u) + 2048);
      const u32x4 yy[2] = {*(LAS u32x4*)(lds + xi), *(LAS u32x4*)(lds + xi + 1024)};
      SB();
      const float nn = fminf(nm, st[0]);
      const float b1 = __builtin_amdgcn_exp2f(nn - nm), b2 = __builtin_amdgcn_exp2f(nn - st[0]);
      const float inv = __builtin_amdgcn_rcpf(fmaf(b1, lsum[0], b2 * st[2]));
      const float w1 = b1 * inv, w2 = b2 * inv;
#pragma unroll
      for (int i = 0; i < 4; ++i) {
        const unsigned y0 = yy[i >> 1][2 * (i & 1)], y1 = yy[i >> 1][2 * (i & 1) + 1];
        const f32x4 y = {__uint_as_float(y0 << 16), __uint_as_float(y0 & 0xFFFF0000u), __uint_as_float(y1 << 16), __uint_as_float(y1 & 0xFFFF0000u)};
        const f32x4 r = o[i] * w1 + y * w2;
        *(LAS u32x2*)(lds + xw + i * 32) = u32x2{pk(r[0], r[1]), pk(r[2], r[3])};
      }
      asm volatile("" ::: "memory");
      const u32x4 r0 = *(LAS u32x4*)(lds + xr), r1 = *(LAS u32x4*)(lds + xr + 8 * 144);
      const __amdgpu_buffer_rsrc_t ro = __builtin_amdgcn_make_buffer_rsrc((char*)O + ((size_t)pair * 4 + head) * (SQ * 256), 0, SQ * 256, 0x00020000);
      __builtin_amdgcn_raw_buffer_store_b128(r0, ro, oo, 0, 18);
      __builtin_amdgcn_raw_buffer_store_b128(r1, ro, oo + 2048, 0, 18);
    }
    return;
  }

  const int vo0 = (lane >> 3) * 256 + half * 128 + (((lane & 7) ^ ((lane >> 3) & 6)) << 4);
  const int vo1 = vo0 + 1024, vo2 = vo0 ^ 128, vo3 = vo2 + 1024;
  const __amdgpu_buffer_rsrc_t rk = __builtin_amdgcn_make_buffer_rsrc((char*)K + (size_t)pair * (SK * 256), 0, SK * 256, 0x00020000);
  const __amdgpu_buffer_rsrc_t rq = __builtin_amdgcn_make_buffer_rsrc((char*)Q + (size_t)pair * KT_BYTES, 0, KT_BYTES, 0x00020000);
  const __amdgpu_buffer_rsrc_t rv = __builtin_amdgcn_make_buffer_rsrc((char*)V + (size_t)pair * (SK * 256), 0, SK * 256, 0x00020000);
  const unsigned lbase = (unsigned)(uintptr_t)lds;
#define KTILE(r, so, dst) \
    asm volatile("s_mov_b32 m0, %0\n\ts_nop 0\n\t" \
                 "buffer_load_dwordx4 %1, %3, %4 offen nt lds\n\t" \
                 "buffer_load_dwordx4 %2, %3, %4 offen offset:1024 nt lds\n\t" \
                 "buffer_load_dwordx4 %1, %3, %4 offen offset:2048 nt lds\n\t" \
                 "buffer_load_dwordx4 %2, %3, %4 offen offset:3072 nt lds" \
                 :: "s"(lbase + (dst) + wave * 4096), "v"(ko0), "v"(ko1), "s"(r), "s"((so) + wave * 4096) : "memory")
#define VHALF(a, b, so, dst) \
    asm volatile("s_mov_b32 m0, %0\n\ts_nop 0\n\t" \
                 "buffer_load_dwordx4 %1, %3, %4 offen nt lds\n\t" \
                 "buffer_load_dwordx4 %2, %3, %4 offen offset:1024 nt lds" \
                 :: "s"(lbase + (dst) + wave * 2048), "v"(a), "v"(b), "s"(rv), "s"((so) + wave * 4096) : "memory")
#define SLOT(s) (((s) & 3) * SLOT_BYTES)
#define TOFF(u) ((((((u) >> 2) & 1) | (((u) & 3) << 1) | (((u) >> 3) << 3)) ^ ((pair & 3) << 3)) * KT_BYTES)
#define OFF1(t) TOFF(2 * (t) + half)
#define OFF2(t) TOFF(NA + (t))
#define K1(t) KTILE(rk, OFF1(t), SLOT(t))
#define K2(t) KTILE(rk, OFF2(t), SLOT(t))
#define V1(t) do { VHALF(vo0, vo1, OFF1(t), SLOT((t) + 2) + KT_BYTES); VHALF(vo2, vo3, OFF1(t), SLOT((t) + 2) + KT_BYTES + VH_BYTES); } while (0)
#define V2(t) VHALF(vo0, vo1, OFF2(t), SLOT((t) + 2) + KT_BYTES)
  unsigned due = (unsigned)t64 + (PACE0 - 5 * (PACE / 2)) - (__builtin_amdgcn_s_getreg(20 | (3 << 11)) & 1) * (PACE / 4);
#define PACE_WAIT(d) do { while ((int)((unsigned)__builtin_amdgcn_s_memrealtime() - due) < 0) __builtin_amdgcn_s_sleep(1); due += (d); } while (0)
  K1(0); KTILE(rq, 0, Q_OFF);
  PACE_WAIT(PACE / 2); K1(1);
  PACE_WAIT(PACE / 2); V1(0);
  wait_vm<12>(); PACE_WAIT(PACE / 2); K1(2);
  wait_vm<12>(); LBARRIER(); PACE_WAIT(PACE / 2); V1(1);
  wait_vm<12>(); LBARRIER(); PACE_WAIT(PACE / 2); K1(3);
#pragma unroll 1
  for (int s = 2; s < NA - 2; ++s) {
    wait_vm<12>(); PACE_WAIT(PACE / 2); V1(s);
    wait_vm<12>(); LBARRIER(); PACE_WAIT(PACE / 2); K1(s + 2);
  }
  wait_vm<12>(); PACE_WAIT(PACE / 2); V1(NA - 2); wait_vm<12>(); LBARRIER(); PACE_WAIT(PACE / 4); K2(NA);
  wait_vm<8>(); LBARRIER(); PACE_WAIT(0); V1(NA - 1); K2(NA + 1);
  wait_vm<12>(); V2(NA); wait_vm<10>(); K2(NA + 2); LBARRIER();
#pragma unroll 1
  for (int s = NA + 1; s < NI - 2; ++s) {
    wait_vm<10>(); V2(s);
    wait_vm<8>(); K2(s + 2); LBARRIER();
  }
  V2(NI - 2);
  wait_vm<8>(); LBARRIER();
  V2(NI - 1);
  wait_vm<4>(); LBARRIER();
  wait_vm<2>(); LBARRIER();
  wait_vm<0>(); LBARRIER();
}

}

extern "C" void kernel_launch(void* const* d_in, const int* in_sizes, int n_in, void* d_out, int out_size, void* d_ws, size_t ws_size,
                              hipStream_t stream) {
  gqa_attn<<<2 * NPAIR, NTHREADS, 0, stream>>>((const __hip_bfloat16*)d_in[0], (const __hip_bfloat16*)d_in[1], (const __hip_bfloat16*)d_in[2],
                                                (__hip_bfloat16*)d_out, (unsigned char*)d_ws);
}
```

```cpp
#define PACE 112
#include <hip/hip_runtime.h>
#include <hip/hip_bf16.h>
#include <stdint.h>

#define NA 12
#define K2_POL "nt"
#define OUT_AUX 18
#define KV_BYTES (SK * 256)
#define PACE0 390
#define NOCOMP 0
#define NOX 0
#define FLAG_LAG 2
#define NLOAD 4
#define TOUCH0 0
#define TOUCHV 0
#define ADAPT 0
#define STAG 0
#define STW_ 54
#define EVD 0
#define ODD 27
#define WN 8
#define BMAP 5
#define PGAIN 4
#define PROBE_X -1
#define PREREAD 0
#define PRE_POL ""
#define TRANS 3
#define P2ORD 0
#define PRO 3
#define PROBAR 1
#define VTOUCH 1
#define PRIO 0
#define PACEK 250
#define PACEM 2000
#define MINSP 0
#define SLIP 0
#define STOUCH 0
#define ORD 0
#define MAP 4
#define ROT 5
#define PBASE 0
#define L1 1
#define L2 3
#define L3 1
#define ST_POL "sc1"
#define LD_POL "sc1"
#define BARFIRST 2
#define PACE_LAST 0

namespace {

constexpr int B_ = 16, HKV = 8, SQ = 16, SK = 2048;
constexpr int TK = 64, NT = SK / TK;
constexpr int NB = NT - 2 * NA, NI = NA + NB;
constexpr int NPAIR = B_ * HKV;
constexpr int KT_BYTES = TK * 256, VH_BYTES = TK * 128;
constexpr int SLOT_BYTES = KT_BYTES + 2 * VH_BYTES;
constexpr int RING_BYTES = 4 * SLOT_BYTES;
constexpr int Q_OFF = RING_BYTES;
constexpr int XCH_BYTES = 4096;
constexpr int LDS_BYTES = RING_BYTES + 4 * XCH_BYTES;
constexpr int NTHREADS = 576;
constexpr float C2 = 0.08838834764831845f * 1.4426950408889634f;
constexpr float SLACK = 7.0f;
constexpr unsigned GE2 = 0x40004000u;
constexpr int FLAG_OFF = 8192, EPOCH_OFF = FLAG_OFF + 1024;
constexpr int BOX_BYTES = EPOCH_OFF + 256;
constexpr unsigned TAG_STEP = 0x9E3779B1u;
static_assert(NB >= 6 && NA >= 4, "the swap needs a few tiles to hide under");

typedef __attribute__((ext_vector_type(8))) __bf16 bf16x8;
typedef __attribute__((ext_vector_type(4))) __bf16 bf16x4;
typedef __attribute__((ext_vector_type(2))) __bf16 bf16x2;
typedef __attribute__((ext_vector_type(4))) float f32x4;
typedef __attribute__((ext_vector_type(2))) float f32x2;
typedef __attribute__((ext_vector_type(4))) unsigned u32x4;
typedef __attribute__((ext_vector_type(2))) unsigned u32x2;
#define LAS __attribute__((address_space(3)))

#define BARRIER() asm volatile("s_waitcnt lgkmcnt(0)\n\ts_barrier" ::: "memory")
#define LBARRIER() asm volatile("s_barrier" ::: "memory")
#define DIAG_OUT(code)
#define RSTAMP(i)
#define SB() __builtin_amdgcn_sched_barrier(0)

template <int N> __device__ __forceinline__ void wait_vm() { asm volatile("s_waitcnt vmcnt(%0)" ::"n"(N) : "memory"); }

__device__ __forceinline__ float xmax4(float x) {
  auto s = __builtin_amdgcn_permlane16_swap(__float_as_uint(x), __float_as_uint(x), false, false);
  x = fmaxf(__uint_as_float(s[0]), __uint_as_float(s[1]));
  auto t = __builtin_amdgcn_permlane32_swap(__float_as_uint(x), __float_as_uint(x), false, false);
  return fmaxf(__uint_as_float(t[0]), __uint_as_float(t[1]));
}
__device__ __forceinline__ unsigned pk(float a, float b) { f32x2 v = {a, b}; return __builtin_bit_cast(unsigned, __builtin_convertvector(v, bf16x2)); }
__device__ __forceinline__ f32x4 mfma(u32x4 a, u32x4 b, f32x4 c) {
  return __builtin_amdgcn_mfma_f32_16x16x32_bf16(__builtin_bit_cast(bf16x8, a), __builtin_bit_cast(bf16x8, b), c, 0, 0, 0);
}
__device__ __forceinline__ u32x2 trd(LAS char* p) { return __builtin_bit_cast(u32x2, __builtin_amdgcn_ds_read_tr16_b64_v4bf16((LAS bf16x4*)p)); }

__global__ void __launch_bounds__(NTHREADS, 3)
gqa_attn(const __hip_bfloat16* __restrict__ Q, const __hip_bfloat16* __restrict__ K, const __hip_bfloat16* __restrict__ V,
         __hip_bfloat16* __restrict__ O, unsigned char* __restrict__ ws) {
  __shared__ __attribute__((aligned(1024))) char smem[LDS_BYTES];
  LAS char* lds = (LAS char*)smem;
  const unsigned tstart = (unsigned)__builtin_amdgcn_s_memrealtime();
  const int lane = threadIdx.x & 63;
  const int wave = __builtin_amdgcn_readfirstlane(threadIdx.x >> 6);
  const int pair = blockIdx.x & (NPAIR - 1), half = blockIdx.x >> 7;
  const int g = lane >> 4, c = lane & 15;

  if (__builtin_expect(wave >= NLOAD, 0)) {
    if (wave == 8) {
      if (NOX || NOCOMP) return;
      const unsigned char* wp = ws + (size_t)pair * (2 * BOX_BYTES);
      const int bo = half * BOX_BYTES, bi = (half ^ 1) * BOX_BYTES;
      unsigned ep_out, ep_in;
      asm volatile("s_load_dword %0, %2, %3\n\ts_load_dword %1, %2, %4\n\ts_waitcnt lgkmcnt(0)" : "=&s"(ep_out), "=&s"(ep_in) : "s"(wp), "s"(bo + EPOCH_OFF), "s"(bi + EPOCH_OFF) : "memory");
      const __amdgpu_buffer_rsrc_t rp = __builtin_amdgcn_make_buffer_rsrc((void*)wp, 0, 2 * BOX_BYTES, 0x00020000);
      const unsigned xb = (unsigned)(uintptr_t)lds + Q_OFF;
      {
#pragma unroll 1
        for (int j = 0; j < 6; ++j) asm volatile("s_barrier" ::: "memory");
        unsigned t0, t1;
        asm volatile("buffer_load_dword %0, off, %2, %3 offset:128\n\t"
                     "s_barrier\n\ts_barrier\n\ts_barrier\n\ts_waitcnt vmcnt(0)"
                     : "=&v"(t0), "=&v"(t1) : "s"(rp), "s"(bo + EPOCH_OFF), "v"(0), "s"((char*)O + (size_t)pair * (4 * SQ * 256)) : "memory");
#pragma unroll 1
        for (int j = 9; j < NA + 3; ++j) asm volatile("s_barrier" ::: "memory");
      }
      int spins = 1 << 20;
      unsigned long long tmp64;
#define BAR "s_barrier\n\t"
#define TS(n)
#define TS_CLOB
#define BAR0
#define BAR1 BAR
#define BAR2 BAR BAR
#define BAR3 BAR BAR BAR
#define BAR4 BAR BAR BAR BAR
#define BAR5 BAR BAR BAR BAR BAR
#define BAR6 BAR BAR BAR BAR BAR BAR
#define BARS_(n) BAR##n
#define BARS(n) BARS_(n)
      static_assert(L1 + L2 + L3 + 3 == NB, "barrier count");
#define FLAG_LOAD "buffer_load_dwordx4 v[48:51], %[po], %[rp], %[fi] offen sc1\n\t"
#define FLAG_TEST "v_cmp_ne_u32 vcc, %[tin], v49\n\tv_cmp_ne_u32 %[t], %[tin], v51\n\ts_nop 3\n\ts_or_b64 vcc, vcc, %[t]\n\t"
#define PART_IO(op, s0, s1, pol) \
      op " v[16:19], %[po], %[rp], " s0 " offen " pol "\n\t" op " v[20:23], %[po], %[rp], " s0 " offen offset:1024 " pol "\n\t" \
      op " v[24:27], %[po], %[rp], " s0 " offen offset:2048 " pol "\n\t" op " v[28:31], %[po], %[rp], " s0 " offen offset:3072 " pol "\n\t" \
      op " v[32:35], %[po], %[rp], " s1 " offen " pol "\n\t" op " v[36:39], %[po], %[rp], " s1 " offen offset:1024 " pol "\n\t" \
      op " v[40:43], %[po], %[rp], " s1 " offen offset:2048 " pol "\n\t" op " v[44:47], %[po], %[rp], " s1 " offen offset:3072 " pol "\n\t"
      asm volatile(
          TS(60)
          "ds_read_b128 v[16:19], %[xi]\n\tds_read_b128 v[20:23], %[xi] offset:1024\n\t"
          "ds_read_b128 v[24:27], %[xi] offset:4096\n\tds_read_b128 v[28:31], %[xi] offset:5120\n\t"
          "ds_read_b128 v[32:35], %[xi] offset:8192\n\tds_read_b128 v[36:39], %[xi] offset:9216\n\t"
          "ds_read_b128 v[40:43], %[xi] offset:12288\n\tds_read_b128 v[44:47], %[xi] offset:13312\n\t"
          "ds_read_b128 v[48:51], %[xs]\n\t"
          "s_waitcnt lgkmcnt(0)\n\t"
          PART_IO("buffer_store_dwordx4", "%[bo]", "%[bo2]", ST_POL)
          "v_mov_b32 v49, %[tout]\n\tv_mov_b32 v51, %[tout]\n\t" TS(62)
          BARS(L1) TS(64)
          "s_waitcnt vmcnt(0)\n\t" TS(66)
          "buffer_store_dwordx4 v[48:51], %[po], %[rp], %[fo] offen sc1\n\t"
          BARS(L2)
          FLAG_LOAD
          BARS(L3) TS(68)
          "s_waitcnt vmcnt(0)\n\t" TS(70)
          FLAG_TEST
          "s_cbranch_vccnz 1f\n\t"
          PART_IO("buffer_load_dwordx4", "%[bi]", "%[bi2]", LD_POL)
          BAR
          "3:\n\t"
          "ds_write_b128 %[xs], v[48:51]\n\t" TS(72)
          "s_waitcnt vmcnt(0)\n\t" TS(74)
          "ds_write_b128 %[xi], v[16:19]\n\tds_write_b128 %[xi], v[20:23] offset:1024\n\t"
          "ds_write_b128 %[xi], v[24:27] offset:4096\n\tds_write_b128 %[xi], v[28:31] offset:5120\n\t"
          "ds_write_b128 %[xi], v[32:35] offset:8192\n\tds_write_b128 %[xi], v[36:39] offset:9216\n\t"
          "ds_write_b128 %[xi], v[40:43] offset:12288\n\tds_write_b128 %[xi], v[44:47] offset:13312\n\t"
          "s_waitcnt lgkmcnt(0)\n\t" BAR
          "buffer_store_dword v49, %[eo], %[rp], %[bi] offen sc1\n\t"
          "s_branch 4f\n"
          "1:\n\t"
          FLAG_LOAD
          BAR
          "2:\n\t"
          "s_waitcnt vmcnt(0)\n\t"
          FLAG_TEST
          "s_cbranch_vccz 5f\n\t"
          FLAG_LOAD
          "s_sub_u32 %[spins], %[spins], 1\n\t"
          "s_cbranch_scc0 2b\n\t"
          "s_waitcnt vmcnt(0)\n\t"
          "v_mov_b32 v50, 0x7fc00000\n"
          "5:\n\t"
          "s_sub_u32 %[spins], %[spins], 1\n\t"
          PART_IO("buffer_load_dwordx4", "%[bi]", "%[bi2]", LD_POL)
          "s_branch 3b\n"
          "4:"
          : [spins] "+s"(spins), [t] "=&s"(tmp64)
          : [tin] "s"(ep_in + TAG_STEP), [tout] "s"(ep_out + TAG_STEP), [bi] "s"(bi), [bi2] "s"(bi + 4096), [fi] "s"(bi + FLAG_OFF),
            [bo] "s"(bo), [bo2] "s"(bo + 4096), [fo] "s"(bo + FLAG_OFF), [po] "v"(lane * 16), [eo] "v"(lane == 0 ? EPOCH_OFF : 2 * BOX_BYTES),
            [xi] "v"(xb + lane * 16), [xs] "v"(xb + g * XCH_BYTES + 2048 + c * 16), [rp] "s"(rp)
          : "memory", "vcc", "scc", "v16", "v17", "v18", "v19", "v20", "v21", "v22", "v23", "v24", "v25", "v26", "v27", "v28", "v29", "v30", "v31",
            "v32", "v33", "v34", "v35", "v36", "v37", "v38", "v39", "v40", "v41", "v42", "v43", "v44", "v45", "v46", "v47", "v48", "v49", "v50", "v51" TS_CLOB);
      DIAG_OUT(spins == (1 << 20) ? 2 : spins == (1 << 20) - 1 ? 3 : 5);
      return;
    }
    const int head = wave - NLOAD;
    if (PRIO) __builtin_amdgcn_s_setprio(PRIO);
    const __amdgpu_buffer_rsrc_t ro = __builtin_amdgcn_make_buffer_rsrc((char*)O + ((size_t)pair * 4 + head) * (SQ * 256), 0, SQ * 256, 0x00020000);
    const unsigned ka0 = c * 256 + ((g ^ ((c & 7) << 1)) << 4);
    const int vr = 4 * g + (c >> 2);
    const unsigned va0 = KT_BYTES + vr * 128 + (((vr >> 1) & 3) << 5) + (c & 3) * 8;
    unsigned xi = Q_OFF + head * XCH_BYTES + lane * 16;
    u32x4 ones = {0x3F803F80u, 0x3F803F80u, 0x3F803F80u, 0x3F803F80u};
    asm volatile("" : "+v"(ones), "+v"(xi));
    f32x4 o[8], lsum = {0.f, 0.f, 0.f, 0.f};
#pragma unroll
    for (int db = 0; db < 8; ++db) { o[db] = f32x4{0.f, 0.f, 0.f, 0.f}; asm volatile("" : "+v"(o[db])); }
    float nm = 3.0e38f;
    unsigned sb = SLOT_BYTES;
    unsigned kx[4];
#pragma unroll
    for (int st = 0; st < 4; ++st) kx[st] = (ka0 ^ (st << 6)) + sb;
    BARRIER();
    u32x4 q[4], kf[4][4];
#pragma unroll
    for (int st = 0; st < 4; ++st) q[st] = *(const LAS u32x4*)(lds + (Q_OFF + head * 4096) + (ka0 ^ (st << 6)));
#pragma unroll
    for (int sub = 0; sub < 4; ++sub)
#pragma unroll
      for (int st = 0; st < 4; ++st) kf[sub][st] = *(const LAS u32x4*)(lds + (ka0 ^ (st << 6)) + sub * 4096);
    asm volatile("" : "+v"(kf[0][0]), "+v"(kf[0][1]), "+v"(kf[0][2]), "+v"(kf[0][3]), "+v"(kf[1][0]), "+v"(kf[1][1]), "+v"(kf[1][2]), "+v"(kf[1][3]),
                 "+v"(kf[2][0]), "+v"(kf[2][1]), "+v"(kf[2][2]), "+v"(kf[2][3]), "+v"(kf[3][0]), "+v"(kf[3][1]), "+v"(kf[3][2]), "+v"(kf[3][3]),
                 "+v"(q[0]), "+v"(q[1]), "+v"(q[2]), "+v"(q[3]));
    BARRIER();
    int left = NI;
    asm volatile("" : "+s"(left));
#pragma unroll 1
    do {
      f32x4 s[4], x[4];
      u32x4 p[2];
      u32x2 vf[8][2];
      const f32x4 zero = {0.f, 0.f, 0.f, 0.f};
      sb = (sb + SLOT_BYTES) & (RING_BYTES - 1);
      unsigned vx[4];
#define QK1(sub, st) do { s[sub] = mfma(kf[sub][st], q[st], (st) ? s[sub] : zero); kf[sub][st] = *(const LAS u32x4*)(lds + kx[st] + (sub) * 4096); } while (0)
#define FM(sub) x[sub] = s[sub] * C2 + nm
#define EX(sub, r) x[sub][r] = __builtin_amdgcn_exp2f(x[sub][r])
#define CV(sub, h) p[(sub) >> 1][2 * ((sub) & 1) + (h)] = pk(x[sub][2 * (h)], x[sub][2 * (h) + 1])
#define VX(i) do { vx[i] = (i) ? vx[0] ^ ((i) << 5) : va0 + sb; asm volatile("" : "+v"(vx[i])); } while (0)
#define VRD(i, off) do { vf[i][0] = trd(lds + vx[(i) & 3] + ((i) >> 2) * 4096 + (off)); vf[i][1] = trd(lds + vx[(i) & 3] + ((i) >> 2) * 4096 + 2048 + (off)); } while (0)
#define PV1(i, ob) o[(ob) + ((i) & 3)] = mfma(u32x4{vf[i][0][0], vf[i][0][1], vf[i][1][0], vf[i][1][1]}, p[(i) >> 2], o[(ob) + ((i) & 3)])
      QK1(0, 0); SB(); QK1(1, 0); VX(0); SB(); QK1(0, 1); VX(1); SB(); QK1(1, 1); VX(2); SB();
      QK1(0, 2); VX(3); SB(); QK1(1, 2); SB(); QK1(0, 3); SB(); QK1(1, 3); SB();
      QK1(2, 0); SB(); QK1(3, 0); FM(0); SB();
      QK1(2, 1); FM(1); EX(0, 0); SB(); QK1(3, 1); EX(0, 1); EX(0, 2); SB();
      QK1(2, 2); EX(0, 3); EX(1, 0); SB(); QK1(3, 2); EX(1, 1); EX(1, 2); SB();
      QK1(2, 3); EX(1, 3); CV(0, 0); SB(); QK1(3, 3); CV(0, 1); CV(1, 0); CV(1, 1); SB();
      unsigned any = (p[0][0] | p[0][1] | p[0][2]) | p[0][3];
      asm volatile("" : "+v"(any) :: "memory");
      SB();
      FM(2); FM(3); SB();
      EX(2, 0); EX(2, 1); EX(2, 2); EX(2, 3); EX(3, 0); EX(3, 1); EX(3, 2); EX(3, 3); SB();
      CV(2, 0); CV(2, 1); CV(3, 0); CV(3, 1);
      any = (any | p[1][0] | p[1][1]) | p[1][2] | p[1][3];
      if (__builtin_expect(__builtin_amdgcn_ballot_w64((any & GE2) != 0) != 0, 0)) {
        float t = s[0][0];
#pragma unroll
        for (int sub = 0; sub < 4; ++sub)
#pragma unroll
          for (int r = 0; r < 4; ++r) t = fmaxf(t, s[sub][r]);
        const float nn = fminf(nm, -fmaf(xmax4(t), C2, SLACK));
        const float alpha = __builtin_amdgcn_exp2f(nn - nm);
        nm = nn;
#pragma unroll
        for (int db = 0; db < 8; ++db) o[db] *= alpha;
        lsum *= alpha;
#pragma unroll
        for (int sub = 0; sub < 4; ++sub) { FM(sub); EX(sub, 0); EX(sub, 1); EX(sub, 2); EX(sub, 3); CV(sub, 0); CV(sub, 1); }
      }
      BARRIER();
      VRD(0, 0); SB(); VRD(1, 0); SB(); VRD(2, 0); SB(); VRD(3, 0); SB(); VRD(4, 0); SB(); VRD(5, 0); SB();
      lsum = mfma(ones, p[0], lsum);
      lsum = mfma(ones, p[1], lsum);
      SB();
#define KX(i) do { kx[i] = (i) ? kx[0] ^ ((i) << 6) : ka0 + sb; asm volatile("" : "+v"(kx[i])); } while (0)
      PV1(0, 0); SB(); VRD(6, 0); SB(); PV1(1, 0); SB(); VRD(7, 0); SB(); PV1(2, 0); SB(); PV1(3, 0); KX(0); SB();
      PV1(4, 0); KX(1); SB(); PV1(5, 0); KX(2); SB(); PV1(6, 0); KX(3); SB(); PV1(7, 0); SB();
      if (__builtin_expect(left > NB, 0)) {
        VRD(0, VH_BYTES); SB(); VRD(1, VH_BYTES); SB(); VRD(2, VH_BYTES); SB(); VRD(3, VH_BYTES); SB(); VRD(4, VH_BYTES); SB();
        PV1(0, 4); SB(); VRD(5, VH_BYTES); SB(); PV1(1, 4); SB(); VRD(6, VH_BYTES); SB(); PV1(2, 4); SB(); VRD(7, VH_BYTES); SB(); PV1(3, 4); PV1(4, 4); PV1(5, 4); PV1(6, 4); PV1(7, 4); SB();
        if (left == NB + 1) {
          *(LAS f32x4*)(lds + xi + 2048) = f32x4{nm, 0.f, lsum[0], 0.f};
#pragma unroll
          for (int i = 0; i < 2; ++i) {
            const f32x4 a = o[4 + 2 * i], b = o[5 + 2 * i];
            *(LAS u32x4*)(lds + xi + i * 1024) = u32x4{pk(a[0], a[1]), pk(a[2], a[3]), pk(b[0], b[1]), pk(b[2], b[3])};
          }
        }
      }
    } while (--left != 0);
    RSTAMP(52);
    {
      asm volatile("" : "+v"(xi));
      const unsigned ln = (xi >> 4) & 63, xb = Q_OFF + head * XCH_BYTES;
      const unsigned xw = xb + (ln & 15) * 144 + (ln >> 4) * 8, xr = xb + (ln >> 3) * 144 + (ln & 7) * 16;
      const int oo = (ln >> 3) * 256 + half * 128 + (ln & 7) * 16;
      const f32x4 st = *(LAS f32x4*)(lds + (xi & ~0x300u) + 2048);
      const u32x4 yy[2] = {*(LAS u32x4*)(lds + xi), *(LAS u32x4*)(lds + xi + 1024)};
      SB();
      f32x4 y[4];
#pragma unroll
      for (int i = 0; i < 4; ++i) {
        const unsigned y0 = yy[i >> 1][2 * (i & 1)], y1 = yy[i >> 1][2 * (i & 1) + 1];
        y[i] = f32x4{__uint_as_float(y0 << 16), __uint_as_float(y0 & 0xFFFF0000u), __uint_as_float(y1 << 16), __uint_as_float(y1 & 0xFFFF0000u)};
      }
      const float nn = fminf(nm, st[0]);
      const float b1 = __builtin_amdgcn_exp2f(nn - nm), b2 = __builtin_amdgcn_exp2f(nn - st[0]);
      const float inv = __builtin_amdgcn_rcpf(fmaf(b1, lsum[0], b2 * st[2]));
      const float w1 = b1 * inv, w2 = b2 * inv;
#pragma unroll
      for (int i = 0; i < 4; ++i) {
        const f32x4 r = o[i] * w1 + y[i] * w2;
        *(LAS u32x2*)(lds + xw + i * 32) = u32x2{pk(r[0], r[1]), pk(r[2], r[3])};
      }
      asm volatile("" ::: "memory");
      const u32x4 r0 = *(LAS u32x4*)(lds + xr), r1 = *(LAS u32x4*)(lds + xr + 8 * 144);
      __builtin_amdgcn_raw_buffer_store_b128(r0, ro, oo, 0, OUT_AUX);
      __builtin_amdgcn_raw_buffer_store_b128(r1, ro, oo + 2048, 0, OUT_AUX);
    }
    DIAG_OUT(4);
    return;
  }

  const int ko0 = g * 256 + ((c ^ (g << 1)) << 4), ko1 = ko0 ^ 128;
  const int vo0 = (lane >> 3) * 256 + half * 128 + (((lane & 7) ^ ((lane >> 3) & 6)) << 4);
  const int vo1 = vo0 + 1024, vo2 = vo0 ^ 128, vo3 = vo2 + 1024;
  const __amdgpu_buffer_rsrc_t rk = __builtin_amdgcn_make_buffer_rsrc((char*)K + (size_t)pair * (SK * 256), 0, KV_BYTES, 0x00020000);
  const __amdgpu_buffer_rsrc_t rq = __builtin_amdgcn_make_buffer_rsrc((char*)Q + (size_t)pair * KT_BYTES, 0, KT_BYTES, 0x00020000);
  const __amdgpu_buffer_rsrc_t rv = __builtin_amdgcn_make_buffer_rsrc((char*)V + (size_t)pair * (SK * 256), 0, KV_BYTES, 0x00020000);
  const unsigned lbase = (unsigned)(uintptr_t)lds;
  const bool polite = !(__builtin_amdgcn_s_getreg(20 | (3 << 11)) & 1);
#define KTILE(POL, r, so, dst) \
    asm volatile("s_mov_b32 m0, %0\n\ts_nop 0\n\t" \
                 "buffer_load_dwordx4 %1, %3, %4 offen " POL " lds\n\t" \
                 "buffer_load_dwordx4 %2, %3, %4 offen offset:1024 " POL " lds\n\t" \
                 "buffer_load_dwordx4 %1, %3, %4 offen offset:2048 " POL " lds\n\t" \
                 "buffer_load_dwordx4 %2, %3, %4 offen offset:3072 " POL " lds" \
                 :: "s"(lbase + (dst) + wave * 4096), "v"(ko0), "v"(ko1), "s"(r), "s"((so) + wave * 4096) : "memory")
#define VHALF(a, b, so, dst) \
    asm volatile("s_mov_b32 m0, %0\n\ts_nop 0\n\t" \
                 "buffer_load_dwordx4 %1, %3, %4 offen nt lds\n\t" \
                 "buffer_load_dwordx4 %2, %3, %4 offen offset:1024 nt lds" \
                 :: "s"(lbase + (dst) + wave * 2048), "v"(a), "v"(b), "s"(rv), "s"((so) + wave * 4096) : "memory")
#define SLOT(s) (((s) & 3) * SLOT_BYTES)
  const int rot = (pair & 3) * 8;
#define BLK(u) ((wave | ((((u) >> 2) & 1) << 2) | (((u) & 3) << 3) | (((u) >> 3) << 5)) ^ (rot * 4))
#define OFF1(t) ((BLK(2 * (t) + half) - wave) * 4096)
#define OFF2(t) ((BLK(NA + (t)) - wave) * 4096)
#define K1(t) KTILE("nt", rk, OFF1(t), SLOT(t))
#define K2(t) KTILE(K2_POL, rk, OFF2(t), SLOT(t))
#define V1(t) do { VHALF(vo0, vo1, OFF1(t), SLOT((t) + 2) + KT_BYTES); VHALF(vo2, vo3, OFF1(t), SLOT((t) + 2) + KT_BYTES + VH_BYTES); } while (0)
#define V2(t) VHALF(vo0, vo1, OFF2(t), SLOT((t) + 2) + KT_BYTES)
  unsigned due = tstart + PACE0 + (polite ? EVD : -(ODD)) - (STAG == 1 ? ((blockIdx.x >> 3) & 15) * STW_ / 16 : STAG == 2 ? ((blockIdx.x >> 5) & 3) * STW_ / 4 : STAG == 3 ? ((blockIdx.x >> 1) & 3) * STW_ / 4 : STAG == 4 ? wave * STW_ / 4 : 0);
#define PACE_WAIT(d) do { while ((int)((unsigned)__builtin_amdgcn_s_memrealtime() - due) < 0) __builtin_amdgcn_s_sleep(1); due += (d); } while (0)
#define PACE_WAIT2(d)
  due -= (PRO + 2) * (PACE / 2);
  K1(0); KTILE(K2_POL, rq, 0, Q_OFF);
  if (PRO >= 3) PACE_WAIT(PACE / 2);
  K1(1);
  if (PRO >= 2) PACE_WAIT(PACE / 2);
  V1(0);
  wait_vm<12>(); PACE_WAIT(PACE / 2); K1(2);
  wait_vm<12>(); LBARRIER(); PACE_WAIT(PACE / 2); V1(1);
  wait_vm<12>(); LBARRIER(); PACE_WAIT(PACE / 2); K1(3);
#pragma unroll 1
  for (int s = 2; s < NA - 2; ++s) {
    wait_vm<12>(); PACE_WAIT(PACE / 2); V1(s);
    wait_vm<12>(); LBARRIER(); PACE_WAIT(PACE / 2); K1(s + 2);
  }
  wait_vm<12>(); PACE_WAIT(PACE / 2); V1(NA - 2); wait_vm<12>(); LBARRIER(); PACE_WAIT(PACE / 4); K2(NA);
  wait_vm<8>(); LBARRIER(); PACE_WAIT(PACE / 2); V1(NA - 1); PACE_WAIT2(PACE / 4); K2(NA + 1);
  wait_vm<12>(); V2(NA); wait_vm<10>(); K2(NA + 2); LBARRIER();
#pragma unroll 1
  for (int s = NA + 1; s < NI - 2; ++s) {
    wait_vm<10>(); V2(s);
    wait_vm<8>(); K2(s + 2); LBARRIER();
  }
  V2(NI - 2);
  wait_vm<8>(); LBARRIER();
  V2(NI - 1);
  wait_vm<4>(); LBARRIER();
  wait_vm<2>(); LBARRIER();
  wait_vm<0>(); LBARRIER();
  DIAG_OUT(0);
}

}

extern "C" void kernel_launch(void* const* d_in, const int* in_sizes, int n_in, void* d_out, int out_size, void* d_ws, size_t ws_size,
                              hipStream_t stream) {
  gqa_attn<<<2 * NPAIR, NTHREADS, 0, stream>>>((const __hip_bfloat16*)d_in[0], (const __hip_bfloat16*)d_in[1], (const __hip_bfloat16*)d_in[2],
                                                (__hip_bfloat16*)d_out, (unsigned char*)d_ws);
}
```

```cpp
#define PROBE 77
#define PROBE_X 0
#include <hip/hip_runtime.h>
#include <hip/hip_bf16.h>
#include <stdint.h>

#define NA 12
#define K2_POL "nt"
#define OUT_AUX 18
#define KV_BYTES (SK * 256)
#define PACE 108
#define PACE0 390
#define NOCOMP 0
#define NOX 0
#define FLAG_LAG 2
#define NLOAD 4
#define TOUCH0 0
#define TOUCHV 0
#define ADAPT 0
#define STAG 0
#define STW_ 54
#define EVD 0
#define ODD 27
#define WN 8
#define BMAP 5
#define PGAIN 4
#define PREREAD 0
#define PRE_POL ""
#define TRANS 3
#define P2ORD 0
#define PRO 3
#define PROBAR 1
#define VTOUCH 1
#define PRIO 0
#define PACEK 250
#define PACEM 2000
#define MINSP 0
#define SLIP 0
#define STOUCH 0
#define ORD 0
#define MAP 4
#define ROT 5
#define PBASE 0
#define L1 1
#define L2 3
#define L3 1
#define ST_POL "sc1"
#define LD_POL "sc1"
#define BARFIRST 2
#define PACE_LAST 0

namespace {

constexpr int B_ = 16, HKV = 8, SQ = 16, SK = 2048;
constexpr int TK = 64, NT = SK / TK;
constexpr int NB = NT - 2 * NA, NI = NA + NB;
constexpr int NPAIR = B_ * HKV;
constexpr int KT_BYTES = TK * 256, VH_BYTES = TK * 128;
constexpr int SLOT_BYTES = KT_BYTES + 2 * VH_BYTES;
constexpr int RING_BYTES = 4 * SLOT_BYTES;
constexpr int Q_OFF = RING_BYTES;
constexpr int XCH_BYTES = 4096;
constexpr int LDS_BYTES = RING_BYTES + 4 * XCH_BYTES;
constexpr int NTHREADS = 576;
constexpr float C2 = 0.08838834764831845f * 1.4426950408889634f;
constexpr float SLACK = 7.0f;
constexpr unsigned GE2 = 0x40004000u;
constexpr int FLAG_OFF = 8192, EPOCH_OFF = FLAG_OFF + 1024;
constexpr int BOX_BYTES = EPOCH_OFF + 256;
constexpr unsigned TAG_STEP = 0x9E3779B1u;
static_assert(NB >= 6 && NA >= 4, "the swap needs a few tiles to hide under");

typedef __attribute__((ext_vector_type(8))) __bf16 bf16x8;
typedef __attribute__((ext_vector_type(4))) __bf16 bf16x4;
typedef __attribute__((ext_vector_type(2))) __bf16 bf16x2;
typedef __attribute__((ext_vector_type(4))) float f32x4;
typedef __attribute__((ext_vector_type(2))) float f32x2;
typedef __attribute__((ext_vector_type(4))) unsigned u32x4;
typedef __attribute__((ext_vector_type(2))) unsigned u32x2;
#define LAS __attribute__((address_space(3)))

#define BARRIER() asm volatile("s_waitcnt lgkmcnt(0)\n\ts_barrier" ::: "memory")
#define LBARRIER() asm volatile("s_barrier" ::: "memory")
#define DIAG_OUT(code)
#define RSTAMP(i)
#define SB() __builtin_amdgcn_sched_barrier(0)

template <int N> __device__ __forceinline__ void wait_vm() { asm volatile("s_waitcnt vmcnt(%0)" ::"n"(N) : "memory"); }

__device__ __forceinline__ float xmax4(float x) {
  auto s = __builtin_amdgcn_permlane16_swap(__float_as_uint(x), __float_as_uint(x), false, false);
  x = fmaxf(__uint_as_float(s[0]), __uint_as_float(s[1]));
  auto t = __builtin_amdgcn_permlane32_swap(__float_as_uint(x), __float_as_uint(x), false, false);
  return fmaxf(__uint_as_float(t[0]), __uint_as_float(t[1]));
}
__device__ __forceinline__ unsigned pk(float a, float b) { f32x2 v = {a, b}; return __builtin_bit_cast(unsigned, __builtin_convertvector(v, bf16x2)); }
__device__ __forceinline__ f32x4 mfma(u32x4 a, u32x4 b, f32x4 c) {
  return __builtin_amdgcn_mfma_f32_16x16x32_bf16(__builtin_bit_cast(bf16x8, a), __builtin_bit_cast(bf16x8, b), c, 0, 0, 0);
}
__device__ __forceinline__ u32x2 trd(LAS char* p) { return __builtin_bit_cast(u32x2, __builtin_amdgcn_ds_read_tr16_b64_v4bf16((LAS bf16x4*)p)); }

__global__ void __launch_bounds__(NTHREADS, 3)
gqa_attn(const __hip_bfloat16* __restrict__ Q, const __hip_bfloat16* __restrict__ K, const __hip_bfloat16* __restrict__ V,
         __hip_bfloat16* __restrict__ O, unsigned char* __restrict__ ws) {
  __shared__ __attribute__((aligned(1024))) char smem[LDS_BYTES];
  LAS char* lds = (LAS char*)smem;
  const unsigned tstart = (unsigned)__builtin_amdgcn_s_memrealtime();
  const int lane = threadIdx.x & 63;
  const int wave = __builtin_amdgcn_readfirstlane(threadIdx.x >> 6);
  const int pair = blockIdx.x & (NPAIR - 1), half = blockIdx.x >> 7;
  const int g = lane >> 4, c = lane & 15;
  unsigned ptx = tstart; int pn = 0;
#undef LBARRIER
#define LBARRIER() do { asm volatile("s_barrier" ::: "memory"); if (pn == PROBE) ptx = (unsigned)__builtin_amdgcn_s_memrealtime(); ++pn; } while (0)

  if (__builtin_expect(wave >= NLOAD, 0)) {
    if (wave == 8) {
      if (NOX || NOCOMP) return;
      const unsigned char* wp = ws + (size_t)pair * (2 * BOX_BYTES);
      const int bo = half * BOX_BYTES, bi = (half ^ 1) * BOX_BYTES;
      unsigned ep_out, ep_in;
      asm volatile("s_load_dword %0, %2, %3\n\ts_load_dword %1, %2, %4\n\ts_waitcnt lgkmcnt(0)" : "=&s"(ep_out), "=&s"(ep_in) : "s"(wp), "s"(bo + EPOCH_OFF), "s"(bi + EPOCH_OFF) : "memory");
      const __amdgpu_buffer_rsrc_t rp = __builtin_amdgcn_make_buffer_rsrc((void*)wp, 0, 2 * BOX_BYTES, 0x00020000);
      const unsigned xb = (unsigned)(uintptr_t)lds + Q_OFF;
      {
#pragma unroll 1
        for (int j = 0; j < 6; ++j) asm volatile("s_barrier" ::: "memory");
        unsigned t0, t1;
        asm volatile("buffer_load_dword %0, off, %2, %3 offset:128\n\t"
                     "s_barrier\n\ts_barrier\n\ts_barrier\n\ts_waitcnt vmcnt(0)"
                     : "=&v"(t0), "=&v"(t1) : "s"(rp), "s"(bo + EPOCH_OFF), "v"(0), "s"((char*)O + (size_t)pair * (4 * SQ * 256)) : "memory");
#pragma unroll 1
        for (int j = 9; j < NA + 3; ++j) asm volatile("s_barrier" ::: "memory");
      }
      int spins = 1 << 20;
      unsigned long long tmp64;
#define BAR "s_barrier\n\t"
#define TS(n)
#define TS_CLOB
#define BAR0
#define BAR1 BAR
#define BAR2 BAR BAR
#define BAR3 BAR BAR BAR
#define BAR4 BAR BAR BAR BAR
#define BAR5 BAR BAR BAR BAR BAR
#define BAR6 BAR BAR BAR BAR BAR BAR
#define BARS_(n) BAR##n
#define BARS(n) BARS_(n)
      static_assert(L1 + L2 + L3 + 3 == NB, "barrier count");
#define FLAG_LOAD "buffer_load_dwordx4 v[48:51], %[po], %[rp], %[fi] offen sc1\n\t"
#define FLAG_TEST "v_cmp_ne_u32 vcc, %[tin], v49\n\tv_cmp_ne_u32 %[t], %[tin], v51\n\ts_nop 3\n\ts_or_b64 vcc, vcc, %[t]\n\t"
#define PART_IO(op, s0, s1, pol) \
      op " v[16:19], %[po], %[rp], " s0 " offen " pol "\n\t" op " v[20:23], %[po], %[rp], " s0 " offen offset:1024 " pol "\n\t" \
      op " v[24:27], %[po], %[rp], " s0 " offen offset:2048 " pol "\n\t" op " v[28:31], %[po], %[rp], " s0 " offen offset:3072 " pol "\n\t" \
      op " v[32:35], %[po], %[rp], " s1 " offen " pol "\n\t" op " v[36:39], %[po], %[rp], " s1 " offen offset:1024 " pol "\n\t" \
      op " v[40:43], %[po], %[rp], " s1 " offen offset:2048 " pol "\n\t" op " v[44:47], %[po], %[rp], " s1 " offen offset:3072 " pol "\n\t"
      asm volatile(
          TS(60)
          "ds_read_b128 v[16:19], %[xi]\n\tds_read_b128 v[20:23], %[xi] offset:1024\n\t"
          "ds_read_b128 v[24:27], %[xi] offset:4096\n\tds_read_b128 v[28:31], %[xi] offset:5120\n\t"
          "ds_read_b128 v[32:35], %[xi] offset:8192\n\tds_read_b128 v[36:39], %[xi] offset:9216\n\t"
          "ds_read_b128 v[40:43], %[xi] offset:12288\n\tds_read_b128 v[44:47], %[xi] offset:13312\n\t"
          "ds_read_b128 v[48:51], %[xs]\n\t"
          "s_waitcnt lgkmcnt(0)\n\t"
          PART_IO("buffer_store_dwordx4", "%[bo]", "%[bo2]", ST_POL)
          "v_mov_b32 v49, %[tout]\n\tv_mov_b32 v51, %[tout]\n\t" TS(62)
          BARS(L1) TS(64)
          "s_waitcnt vmcnt(0)\n\t" TS(66)
          "buffer_store_dwordx4 v[48:51], %[po], %[rp], %[fo] offen sc1\n\t"
          BARS(L2)
          FLAG_LOAD
          BARS(L3) TS(68)
          "s_waitcnt vmcnt(0)\n\t" TS(70)
          FLAG_TEST
          "s_cbranch_vccnz 1f\n\t"
          PART_IO("buffer_load_dwordx4", "%[bi]", "%[bi2]", LD_POL)
          BAR
          "3:\n\t"
          "ds_write_b128 %[xs], v[48:51]\n\t" TS(72)
          "s_waitcnt vmcnt(0)\n\t" TS(74)
          "ds_write_b128 %[xi], v[16:19]\n\tds_write_b128 %[xi], v[20:23] offset:1024\n\t"
          "ds_write_b128 %[xi], v[24:27] offset:4096\n\tds_write_b128 %[xi], v[28:31] offset:5120\n\t"
          "ds_write_b128 %[xi], v[32:35] offset:8192\n\tds_write_b128 %[xi], v[36:39] offset:9216\n\t"
          "ds_write_b128 %[xi], v[40:43] offset:12288\n\tds_write_b128 %[xi], v[44:47] offset:13312\n\t"
          "s_waitcnt lgkmcnt(0)\n\t" BAR
          "buffer_store_dword v49, %[eo], %[rp], %[bi] offen sc1\n\t"
          "s_branch 4f\n"
          "1:\n\t"
          FLAG_LOAD
          BAR
          "2:\n\t"
          "s_waitcnt vmcnt(0)\n\t"
          FLAG_TEST
          "s_cbranch_vccz 5f\n\t"
          FLAG_LOAD
          "s_sub_u32 %[spins], %[spins], 1\n\t"
          "s_cbranch_scc0 2b\n\t"
          "s_waitcnt vmcnt(0)\n\t"
          "v_mov_b32 v50, 0x7fc00000\n"
          "5:\n\t"
          "s_sub_u32 %[spins], %[spins], 1\n\t"
          PART_IO("buffer_load_dwordx4", "%[bi]", "%[bi2]", LD_POL)
          "s_branch 3b\n"
          "4:"
          : [spins] "+s"(spins), [t] "=&s"(tmp64)
          : [tin] "s"(ep_in + TAG_STEP), [tout] "s"(ep_out + TAG_STEP), [bi] "s"(bi), [bi2] "s"(bi + 4096), [fi] "s"(bi + FLAG_OFF),
            [bo] "s"(bo), [bo2] "s"(bo + 4096), [fo] "s"(bo + FLAG_OFF), [po] "v"(lane * 16), [eo] "v"(lane == 0 ? EPOCH_OFF : 2 * BOX_BYTES),
            [xi] "v"(xb + lane * 16), [xs] "v"(xb + g * XCH_BYTES + 2048 + c * 16), [rp] "s"(rp)
          : "memory", "vcc", "scc", "v16", "v17", "v18", "v19", "v20", "v21", "v22", "v23", "v24", "v25", "v26", "v27", "v28", "v29", "v30", "v31",
            "v32", "v33", "v34", "v35", "v36", "v37", "v38", "v39", "v40", "v41", "v42", "v43", "v44", "v45", "v46", "v47", "v48", "v49", "v50", "v51" TS_CLOB);
      DIAG_OUT(spins == (1 << 20) ? 2 : spins == (1 << 20) - 1 ? 3 : 5);
      return;
    }
    const int head = wave - NLOAD;
    if (PRIO) __builtin_amdgcn_s_setprio(PRIO);
    const __amdgpu_buffer_rsrc_t ro = __builtin_amdgcn_make_buffer_rsrc((char*)O + ((size_t)pair * 4 + head) * (SQ * 256), 0, SQ * 256, 0x00020000);
    const unsigned ka0 = c * 256 + ((g ^ ((c & 7) << 1)) << 4);
    const int vr = 4 * g + (c >> 2);
    const unsigned va0 = KT_BYTES + vr * 128 + (((vr >> 1) & 3) << 5) + (c & 3) * 8;
    unsigned xi = Q_OFF + head * XCH_BYTES + lane * 16;
    u32x4 ones = {0x3F803F80u, 0x3F803F80u, 0x3F803F80u, 0x3F803F80u};
    asm volatile("" : "+v"(ones), "+v"(xi));
    f32x4 o[8], lsum = {0.f, 0.f, 0.f, 0.f};
#pragma unroll
    for (int db = 0; db < 8; ++db) { o[db] = f32x4{0.f, 0.f, 0.f, 0.f}; asm volatile("" : "+v"(o[db])); }
    float nm = 3.0e38f;
    unsigned sb = SLOT_BYTES;
    unsigned kx[4];
#pragma unroll
    for (int st = 0; st < 4; ++st) kx[st] = (ka0 ^ (st << 6)) + sb;
    BARRIER();
    u32x4 q[4], kf[4][4];
#pragma unroll
    for (int st = 0; st < 4; ++st) q[st] = *(const LAS u32x4*)(lds + (Q_OFF + head * 4096) + (ka0 ^ (st << 6)));
#pragma unroll
    for (int sub = 0; sub < 4; ++sub)
#pragma unroll
      for (int st = 0; st < 4; ++st) kf[sub][st] = *(const LAS u32x4*)(lds + (ka0 ^ (st << 6)) + sub * 4096);
    asm volatile("" : "+v"(kf[0][0]), "+v"(kf[0][1]), "+v"(kf[0][2]), "+v"(kf[0][3]), "+v"(kf[1][0]), "+v"(kf[1][1]), "+v"(kf[1][2]), "+v"(kf[1][3]),
                 "+v"(kf[2][0]), "+v"(kf[2][1]), "+v"(kf[2][2]), "+v"(kf[2][3]), "+v"(kf[3][0]), "+v"(kf[3][1]), "+v"(kf[3][2]), "+v"(kf[3][3]),
                 "+v"(q[0]), "+v"(q[1]), "+v"(q[2]), "+v"(q[3]));
    BARRIER();
    int left = NI;
    asm volatile("" : "+s"(left));
#pragma unroll 1
    do {
      f32x4 s[4], x[4];
      u32x4 p[2];
      u32x2 vf[8][2];
      const f32x4 zero = {0.f, 0.f, 0.f, 0.f};
      sb = (sb + SLOT_BYTES) & (RING_BYTES - 1);
      unsigned vx[4];
#define QK1(sub, st) do { s[sub] = mfma(kf[sub][st], q[st], (st) ? s[sub] : zero); kf[sub][st] = *(const LAS u32x4*)(lds + kx[st] + (sub) * 4096); } while (0)
#define FM(sub) x[sub] = s[sub] * C2 + nm
#define EX(sub, r) x[sub][r] = __builtin_amdgcn_exp2f(x[sub][r])
#define CV(sub, h) p[(sub) >> 1][2 * ((sub) & 1) + (h)] = pk(x[sub][2 * (h)], x[sub][2 * (h) + 1])
#define VX(i) do { vx[i] = (i) ? vx[0] ^ ((i) << 5) : va0 + sb; asm volatile("" : "+v"(vx[i])); } while (0)
#define VRD(i, off) do { vf[i][0] = trd(lds + vx[(i) & 3] + ((i) >> 2) * 4096 + (off)); vf[i][1] = trd(lds + vx[(i) & 3] + ((i) >> 2) * 4096 + 2048 + (off)); } while (0)
#define PV1(i, ob) o[(ob) + ((i) & 3)] = mfma(u32x4{vf[i][0][0], vf[i][0][1], vf[i][1][0], vf[i][1][1]}, p[(i) >> 2], o[(ob) + ((i) & 3)])
      QK1(0, 0); SB(); QK1(1, 0); VX(0); SB(); QK1(0, 1); VX(1); SB(); QK1(1, 1); VX(2); SB();
      QK1(0, 2); VX(3); SB(); QK1(1, 2); SB(); QK1(0, 3); SB(); QK1(1, 3); SB();
      QK1(2, 0); SB(); QK1(3, 0); FM(0); SB();
      QK1(2, 1); FM(1); EX(0, 0); SB(); QK1(3, 1); EX(0, 1); EX(0, 2); SB();
      QK1(2, 2); EX(0, 3); EX(1, 0); SB(); QK1(3, 2); EX(1, 1); EX(1, 2); SB();
      QK1(2, 3); EX(1, 3); CV(0, 0); SB(); QK1(3, 3); CV(0, 1); CV(1, 0); CV(1, 1); SB();
      unsigned any = (p[0][0] | p[0][1] | p[0][2]) | p[0][3];
      asm volatile("" : "+v"(any) :: "memory");
      SB();
      FM(2); FM(3); SB();
      EX(2, 0); EX(2, 1); EX(2, 2); EX(2, 3); EX(3, 0); EX(3, 1); EX(3, 2); EX(3, 3); SB();
      CV(2, 0); CV(2, 1); CV(3, 0); CV(3, 1);
      any = (any | p[1][0] | p[1][1]) | p[1][2] | p[1][3];
      if (__builtin_expect(__builtin_amdgcn_ballot_w64((any & GE2) != 0) != 0, 0)) {
        float t = s[0][0];
#pragma unroll
        for (int sub = 0; sub < 4; ++sub)
#pragma unroll
          for (int r = 0; r < 4; ++r) t = fmaxf(t, s[sub][r]);
        const float nn = fminf(nm, -fmaf(xmax4(t), C2, SLACK));
        const float alpha = __builtin_amdgcn_exp2f(nn - nm);
        nm = nn;
#pragma unroll
        for (int db = 0; db < 8; ++db) o[db] *= alpha;
        lsum *= alpha;
#pragma unroll
        for (int sub = 0; sub < 4; ++sub) { FM(sub); EX(sub, 0); EX(sub, 1); EX(sub, 2); EX(sub, 3); CV(sub, 0); CV(sub, 1); }
      }
      BARRIER();
      VRD(0, 0); SB(); VRD(1, 0); SB(); VRD(2, 0); SB(); VRD(3, 0); SB(); VRD(4, 0); SB(); VRD(5, 0); SB();
      lsum = mfma(ones, p[0], lsum);
      lsum = mfma(ones, p[1], lsum);
      SB();
#define KX(i) do { kx[i] = (i) ? kx[0] ^ ((i) << 6) : ka0 + sb; asm volatile("" : "+v"(kx[i])); } while (0)
      PV1(0, 0); SB(); VRD(6, 0); SB(); PV1(1, 0); SB(); VRD(7, 0); SB(); PV1(2, 0); SB(); PV1(3, 0); KX(0); SB();
      PV1(4, 0); KX(1); SB(); PV1(5, 0); KX(2); SB(); PV1(6, 0); KX(3); SB(); PV1(7, 0); SB();
      if (__builtin_expect(left > NB, 0)) {
        VRD(0, VH_BYTES); SB(); VRD(1, VH_BYTES); SB(); VRD(2, VH_BYTES); SB(); VRD(3, VH_BYTES); SB(); VRD(4, VH_BYTES); SB();
        PV1(0, 4); SB(); VRD(5, VH_BYTES); SB(); PV1(1, 4); SB(); VRD(6, VH_BYTES); SB(); PV1(2, 4); SB(); VRD(7, VH_BYTES); SB(); PV1(3, 4); PV1(4, 4); PV1(5, 4); PV1(6, 4); PV1(7, 4); SB();
        if (left == NB + 1) {
          *(LAS f32x4*)(lds + xi + 2048) = f32x4{nm, 0.f, lsum[0], 0.f};
#pragma unroll
          for (int i = 0; i < 2; ++i) {
            const f32x4 a = o[4 + 2 * i], b = o[5 + 2 * i];
            *(LAS u32x4*)(lds + xi + i * 1024) = u32x4{pk(a[0], a[1]), pk(a[2], a[3]), pk(b[0], b[1]), pk(b[2], b[3])};
          }
        }
      }
    } while (--left != 0);
    RSTAMP(52);
    {
      asm volatile("" : "+v"(xi));
      const unsigned ln = (xi >> 4) & 63, xb = Q_OFF + head * XCH_BYTES;
      const unsigned xw = xb + (ln & 15) * 144 + (ln >> 4) * 8, xr = xb + (ln >> 3) * 144 + (ln & 7) * 16;
      const int oo = (ln >> 3) * 256 + half * 128 + (ln & 7) * 16;
      const f32x4 st = *(LAS f32x4*)(lds + (xi & ~0x300u) + 2048);
      const u32x4 yy[2] = {*(LAS u32x4*)(lds + xi), *(LAS u32x4*)(lds + xi + 1024)};
      SB();
      f32x4 y[4];
#pragma unroll
      for (int i = 0; i < 4; ++i) {
        const unsigned y0 = yy[i >> 1][2 * (i & 1)], y1 = yy[i >> 1][2 * (i & 1) + 1];
        y[i] = f32x4{__uint_as_float(y0 << 16), __uint_as_float(y0 & 0xFFFF0000u), __uint_as_float(y1 << 16), __uint_as_float(y1 & 0xFFFF0000u)};
      }
      const float nn = fminf(nm, st[0]);
      const float b1 = __builtin_amdgcn_exp2f(nn - nm), b2 = __builtin_amdgcn_exp2f(nn - st[0]);
      const float inv = __builtin_amdgcn_rcpf(fmaf(b1, lsum[0], b2 * st[2]));
      const float w1 = b1 * inv, w2 = b2 * inv;
#pragma unroll
      for (int i = 0; i < 4; ++i) {
        const f32x4 r = o[i] * w1 + y[i] * w2;
        *(LAS u32x2*)(lds + xw + i * 32) = u32x2{pk(r[0], r[1]), pk(r[2], r[3])};
      }
      asm volatile("" ::: "memory");
      const u32x4 r0 = *(LAS u32x4*)(lds + xr), r1 = *(LAS u32x4*)(lds + xr + 8 * 144);
      __builtin_amdgcn_raw_buffer_store_b128(r0, ro, oo, 0, OUT_AUX);
      __builtin_amdgcn_raw_buffer_store_b128(r1, ro, oo + 2048, 0, OUT_AUX);
    }
    DIAG_OUT(4);
    return;
  }

  const int ko0 = g * 256 + ((c ^ (g << 1)) << 4), ko1 = ko0 ^ 128;
  const int vo0 = (lane >> 3) * 256 + half * 128 + (((lane & 7) ^ ((lane >> 3) & 6)) << 4);
  const int vo1 = vo0 + 1024, vo2 = vo0 ^ 128, vo3 = vo2 + 1024;
  const __amdgpu_buffer_rsrc_t rk = __builtin_amdgcn_make_buffer_rsrc((char*)K + (size_t)pair * (SK * 256), 0, KV_BYTES, 0x00020000);
  const __amdgpu_buffer_rsrc_t rq = __builtin_amdgcn_make_buffer_rsrc((char*)Q + (size_t)pair * KT_BYTES, 0, KT_BYTES, 0x00020000);
  const __amdgpu_buffer_rsrc_t rv = __builtin_amdgcn_make_buffer_rsrc((char*)V + (size_t)pair * (SK * 256), 0, KV_BYTES, 0x00020000);
  const unsigned lbase = (unsigned)(uintptr_t)lds;
  const bool polite = !(__builtin_amdgcn_s_getreg(20 | (3 << 11)) & 1);
#define KTILE(POL, r, so, dst) \
    asm volatile("s_mov_b32 m0, %0\n\ts_nop 0\n\t" \
                 "buffer_load_dwordx4 %1, %3, %4 offen " POL " lds\n\t" \
                 "buffer_load_dwordx4 %2, %3, %4 offen offset:1024 " POL " lds\n\t" \
                 "buffer_load_dwordx4 %1, %3, %4 offen offset:2048 " POL " lds\n\t" \
                 "buffer_load_dwordx4 %2, %3, %4 offen offset:3072 " POL " lds" \
                 :: "s"(lbase + (dst) + wave * 4096), "v"(ko0), "v"(ko1), "s"(r), "s"((so) + wave * 4096) : "memory")
#define VHALF(a, b, so, dst) \
    asm volatile("s_mov_b32 m0, %0\n\ts_nop 0\n\t" \
                 "buffer_load_dwordx4 %1, %3, %4 offen nt lds\n\t" \
                 "buffer_load_dwordx4 %2, %3, %4 offen offset:1024 nt lds" \
                 :: "s"(lbase + (dst) + wave * 2048), "v"(a), "v"(b), "s"(rv), "s"((so) + wave * 4096) : "memory")
#define SLOT(s) (((s) & 3) * SLOT_BYTES)
  const int rot = (pair & 3) * 8;
#define BLK(u) ((wave | ((((u) >> 2) & 1) << 2) | (((u) & 3) << 3) | (((u) >> 3) << 5)) ^ (rot * 4))
#define OFF1(t) ((BLK(2 * (t) + half) - wave) * 4096)
#define OFF2(t) ((BLK(NA + (t)) - wave) * 4096)
#define K1(t) KTILE("nt", rk, OFF1(t), SLOT(t))
#define K2(t) KTILE(K2_POL, rk, OFF2(t), SLOT(t))
#define V1(t) do { VHALF(vo0, vo1, OFF1(t), SLOT((t) + 2) + KT_BYTES); VHALF(vo2, vo3, OFF1(t), SLOT((t) + 2) + KT_BYTES + VH_BYTES); } while (0)
#define V2(t) VHALF(vo0, vo1, OFF2(t), SLOT((t) + 2) + KT_BYTES)
  unsigned due = tstart + PACE0 + (polite ? EVD : -(ODD)) - (STAG == 1 ? ((blockIdx.x >> 3) & 15) * STW_ / 16 : STAG == 2 ? ((blockIdx.x >> 5) & 3) * STW_ / 4 : STAG == 3 ? ((blockIdx.x >> 1) & 3) * STW_ / 4 : STAG == 4 ? wave * STW_ / 4 : 0);
#define PACE_WAIT(d) do { while ((int)((unsigned)__builtin_amdgcn_s_memrealtime() - due) < 0) __builtin_amdgcn_s_sleep(1); due += (d); } while (0)
#define PACE_WAIT2(d)
  due -= (PRO + 2) * (PACE / 2);
  K1(0); KTILE(K2_POL, rq, 0, Q_OFF);
  if (PRO >= 3) PACE_WAIT(PACE / 2);
  K1(1);
  if (PRO >= 2) PACE_WAIT(PACE / 2);
  V1(0);
  wait_vm<12>(); PACE_WAIT(PACE / 2); K1(2);
  wait_vm<12>(); LBARRIER(); PACE_WAIT(PACE / 2); V1(1);
  wait_vm<12>(); LBARRIER(); PACE_WAIT(PACE / 2); K1(3);
#pragma unroll 1
  for (int s = 2; s < NA - 2; ++s) {
    wait_vm<12>(); PACE_WAIT(PACE / 2); V1(s);
    wait_vm<12>(); LBARRIER(); PACE_WAIT(PACE / 2); K1(s + 2);
  }
  wait_vm<12>(); PACE_WAIT(PACE / 2); V1(NA - 2); wait_vm<12>(); LBARRIER(); PACE_WAIT(PACE / 4); K2(NA);
  wait_vm<8>(); LBARRIER(); PACE_WAIT(PACE / 2); V1(NA - 1); PACE_WAIT2(PACE / 4); K2(NA + 1);
  wait_vm<12>(); V2(NA); wait_vm<10>(); K2(NA + 2); LBARRIER();
#pragma unroll 1
  for (int s = NA + 1; s < NI - 2; ++s) {
    wait_vm<10>(); V2(s);
    wait_vm<8>(); K2(s + 2); LBARRIER();
  }
  V2(NI - 2);
  wait_vm<8>(); LBARRIER();
  V2(NI - 1);
  wait_vm<4>(); LBARRIER();
  wait_vm<2>(); LBARRIER();
  wait_vm<0>(); LBARRIER();
  if (PROBE_X < 0 || (int)(blockIdx.x & 7) == PROBE_X) { const unsigned due2 = ptx + 4000; while ((int)((unsigned)__builtin_amdgcn_s_memrealtime() - due2) < 0) __builtin_amdgcn_s_sleep(1); }
  DIAG_OUT(0);
}

}

extern "C" void kernel_launch(void* const* d_in, const int* in_sizes, int n_in, void* d_out, int out_size, void* d_ws, size_t ws_size,
                              hipStream_t stream) {
  gqa_attn<<<2 * NPAIR, NTHREADS, 0, stream>>>((const __hip_bfloat16*)d_in[0], (const __hip_bfloat16*)d_in[1], (const __hip_bfloat16*)d_in[2],
                                                (__hip_bfloat16*)d_out, (unsigned char*)d_ws);
}
```

```cpp
#define PACE 106
#define PACE0 420
#include <hip/hip_runtime.h>
#include <hip/hip_bf16.h>
#include <stdint.h>

#define ODD 40

namespace {

constexpr int B_ = 16, HKV = 8, SQ = 16, SK = 2048;
constexpr int TK = 64, NT = SK / TK;
constexpr int NA = 12, NB = NT - 2 * NA, NI = NA + NB;
constexpr int NPAIR = B_ * HKV;
constexpr int KT_BYTES = TK * 256, VH_BYTES = TK * 128;
constexpr int SLOT_BYTES = KT_BYTES + 2 * VH_BYTES;
constexpr int RING_BYTES = 4 * SLOT_BYTES;
constexpr int Q_OFF = RING_BYTES;
constexpr int XCH_BYTES = 4096;
constexpr int LDS_BYTES = RING_BYTES + 4 * XCH_BYTES;
constexpr int NTHREADS = 576;
constexpr float C2 = 0.08838834764831845f * 1.4426950408889634f;
constexpr float SLACK = 7.0f;
constexpr unsigned GE2 = 0x40004000u;
constexpr int FLAG_OFF = 8192, EPOCH_OFF = FLAG_OFF + 1024, BOX_BYTES = EPOCH_OFF + 256;
constexpr int CODE_LINES = 44;
static_assert(NB == 8, "the mail wave's barrier count");

typedef __attribute__((ext_vector_type(8))) __bf16 bf16x8;
typedef __attribute__((ext_vector_type(4))) __bf16 bf16x4;
typedef __attribute__((ext_vector_type(2))) __bf16 bf16x2;
typedef __attribute__((ext_vector_type(4))) float f32x4;
typedef __attribute__((ext_vector_type(2))) float f32x2;
typedef __attribute__((ext_vector_type(4))) unsigned u32x4;
typedef __attribute__((ext_vector_type(2))) unsigned u32x2;
#define LAS __attribute__((address_space(3)))

#define BARRIER() asm volatile("s_waitcnt lgkmcnt(0)\n\ts_barrier" ::: "memory")
#define LBARRIER() asm volatile("s_barrier" ::: "memory")
#define SB() __builtin_amdgcn_sched_barrier(0)

template <int N> __device__ __forceinline__ void wait_vm() { asm volatile("s_waitcnt vmcnt(%0)" ::"n"(N) : "memory"); }

__device__ __forceinline__ float xmax4(float x) {
  auto s = __builtin_amdgcn_permlane16_swap(__float_as_uint(x), __float_as_uint(x), false, false);
  x = fmaxf(__uint_as_float(s[0]), __uint_as_float(s[1]));
  auto t = __builtin_amdgcn_permlane32_swap(__float_as_uint(x), __float_as_uint(x), false, false);
  return fmaxf(__uint_as_float(t[0]), __uint_as_float(t[1]));
}
__device__ __forceinline__ unsigned pk(float a, float b) { f32x2 v = {a, b}; return __builtin_bit_cast(unsigned, __builtin_convertvector(v, bf16x2)); }
__device__ __forceinline__ f32x4 mfma(u32x4 a, u32x4 b, f32x4 c) {
  return __builtin_amdgcn_mfma_f32_16x16x32_bf16(__builtin_bit_cast(bf16x8, a), __builtin_bit_cast(bf16x8, b), c, 0, 0, 0);
}
__device__ __forceinline__ u32x2 trd(LAS char* p) { return __builtin_bit_cast(u32x2, __builtin_amdgcn_ds_read_tr16_b64_v4bf16((LAS bf16x4*)p)); }

__global__ void __launch_bounds__(NTHREADS, 3)
gqa_attn(const __hip_bfloat16* __restrict__ Q, const __hip_bfloat16* __restrict__ K, const __hip_bfloat16* __restrict__ V,
         __hip_bfloat16* __restrict__ O, unsigned char* __restrict__ ws) {
  __shared__ __attribute__((aligned(1024))) char smem[LDS_BYTES];
  LAS char* lds = (LAS char*)smem;
  const unsigned long long t64 = __builtin_amdgcn_s_memrealtime();
  SB();
  const int lane = threadIdx.x & 63;
  const int wave = __builtin_amdgcn_readfirstlane(threadIdx.x >> 6);
  const int pair = ((blockIdx.x >> 3) & 7) | ((blockIdx.x & 7) << 3) | (blockIdx.x & 64), half = (blockIdx.x >> 7) & 1;
  const int g = lane >> 4, c = lane & 15;
  const int ko0 = g * 256 + ((c ^ (g << 1)) << 4), ko1 = ko0 ^ 128;
  const int kso = ((half << 1) | ((pair & 3) << 3)) * KT_BYTES + wave * 4096;
  asm volatile("s_cmp_lg_u32 %0, 8\n\ts_cbranch_scc1 1f\n\t"
               "s_load_dwordx2 s[42:43], %2, 0x10\n\t"
               "s_getpc_b64 s[40:41]\n\tglobal_load_dword v54, %1, s[40:41]\n\t"
               "v_mov_b32 v55, %3\n\ts_waitcnt lgkmcnt(0)\n\tglobal_load_dword v55, v55, s[42:43] nt\n1:"
               :: "s"(wave), "v"(min(lane, CODE_LINES) * 128), "s"(__builtin_amdgcn_kernarg_segment_ptr()), "s"(pair << 19)
               : "memory", "scc", "s40", "s41", "s42", "s43", "v54", "v55");
  asm volatile("" :: "s"(K), "s"(Q), "s"(V), "s"(t64), "s"(kso), "s"(pair << 19), "v"(ko0), "v"(ko1));

  if (__builtin_expect(wave >= 4, 0)) {
    if (wave == 8) {
      const __amdgpu_buffer_rsrc_t rp = __builtin_amdgcn_make_buffer_rsrc(ws + (size_t)pair * (2 * BOX_BYTES), 0, 2 * BOX_BYTES, 0x00020000);
      const int bo = half * BOX_BYTES, bi = (half ^ 1) * BOX_BYTES;
      const unsigned xb = (unsigned)(uintptr_t)lds + Q_OFF;
      int spins = 1 << 20;
      unsigned long long tmp64;
#define BAR "s_barrier\n\t"
#define FLAG_LOAD "buffer_load_dwordx4 v[48:51], %[po], %[rp], %[fi] offen sc1\n\t"
#define FLAG_TEST "v_cmp_ne_u32 vcc, v53, v49\n\tv_cmp_ne_u32 %[t], v53, v51\n\ts_nop 3\n\ts_or_b64 vcc, vcc, %[t]\n\t"
#define PART_IO(op, s0, s1) \
      op " v[16:19], %[po], %[rp], " s0 " offen sc1\n\t" op " v[20:23], %[po], %[rp], " s0 " offen offset:1024 sc1\n\t" \
      op " v[24:27], %[po], %[rp], " s0 " offen offset:2048 sc1\n\t" op " v[28:31], %[po], %[rp], " s0 " offen offset:3072 sc1\n\t" \
      op " v[32:35], %[po], %[rp], " s1 " offen sc1\n\t" op " v[36:39], %[po], %[rp], " s1 " offen offset:1024 sc1\n\t" \
      op " v[40:43], %[po], %[rp], " s1 " offen offset:2048 sc1\n\t" op " v[44:47], %[po], %[rp], " s1 " offen offset:3072 sc1\n\t"
      asm volatile(
          BAR BAR BAR BAR BAR BAR
          "buffer_load_dword v52, off, %[rp], %[fo] offset:1024 sc1\n\t"
          "buffer_load_dword v53, off, %[rp], %[fi] offset:1024 sc1\n\t"
          BAR BAR BAR BAR BAR BAR BAR BAR
          "s_waitcnt vmcnt(0)\n\t"
          "v_add_u32 v52, 0x9e3779b1, v52\n\tv_add_u32 v53, 0x9e3779b1, v53\n\t"
          BAR
          "ds_read_b128 v[16:19], %[xi]\n\tds_read_b128 v[20:23], %[xi] offset:1024\n\t"
          "ds_read_b128 v[24:27], %[xi] offset:4096\n\tds_read_b128 v[28:31], %[xi] offset:5120\n\t"
          "ds_read_b128 v[32:35], %[xi] offset:8192\n\tds_read_b128 v[36:39], %[xi] offset:9216\n\t"
          "ds_read_b128 v[40:43], %[xi] offset:12288\n\tds_read_b128 v[44:47], %[xi] offset:13312\n\t"
          "ds_read_b128 v[48:51], %[xs]\n\t"
          "s_waitcnt lgkmcnt(0)\n\t"
          PART_IO("buffer_store_dwordx4", "%[bo]", "%[bo2]")
          "v_mov_b32 v49, v52\n\tv_mov_b32 v51, v52\n\t"
          BAR
          "s_waitcnt vmcnt(0)\n\t"
          "buffer_store_dwordx4 v[48:51], %[po], %[rp], %[fo] offen sc1\n\t"
          BAR BAR BAR
          FLAG_LOAD
          BAR
          "s_waitcnt vmcnt(0)\n\t"
          FLAG_TEST
          "s_cbranch_vccnz 1f\n\t"
          PART_IO("buffer_load_dwordx4", "%[bi]", "%[bi2]")
          BAR
          "3:\n\t"
          "ds_write_b128 %[xs], v[48:51]\n\t"
          "s_waitcnt vmcnt(0)\n\t"
          "ds_write_b128 %[xi], v[16:19]\n\tds_write_b128 %[xi], v[20:23] offset:1024\n\t"
          "ds_write_b128 %[xi], v[24:27] offset:4096\n\tds_write_b128 %[xi], v[28:31] offset:5120\n\t"
          "ds_write_b128 %[xi], v[32:35] offset:8192\n\tds_write_b128 %[xi], v[36:39] offset:9216\n\t"
          "ds_write_b128 %[xi], v[40:43] offset:12288\n\tds_write_b128 %[xi], v[44:47] offset:13312\n\t"
          "s_waitcnt lgkmcnt(0)\n\t"
          BAR
          "buffer_store_dword v53, %[eo], %[rp], %[bi] offen sc1\n\t"
          "s_branch 4f\n"
          "1:\n\t"
          FLAG_LOAD
          BAR
          "2:\n\t"
          "s_waitcnt vmcnt(0)\n\t"
          FLAG_TEST
          "s_cbranch_vccz 5f\n\t"
          FLAG_LOAD
          "s_sub_u32 %[spins], %[spins], 1\n\t"
          "s_cbranch_scc0 2b\n\t"
          "s_waitcnt vmcnt(0)\n\t"
          "v_mov_b32 v50, 0x7fc00000\n"
          "5:\n\t"
          PART_IO("buffer_load_dwordx4", "%[bi]", "%[bi2]")
          "s_branch 3b\n"
          "4:"
          : [spins] "+s"(spins), [t] "=&s"(tmp64)
          : [bi] "s"(bi), [bi2] "s"(bi + 4096), [fi] "s"(bi + FLAG_OFF), [bo] "s"(bo), [bo2] "s"(bo + 4096), [fo] "s"(bo + FLAG_OFF),
            [po] "v"(lane * 16), [eo] "v"(lane == 0 ? EPOCH_OFF : 2 * BOX_BYTES),
            [xi] "v"(xb + lane * 16), [xs] "v"(xb + g * XCH_BYTES + 2048 + c * 16), [rp] "s"(rp)
          : "memory", "vcc", "scc", "v16", "v17", "v18", "v19", "v20", "v21", "v22", "v23", "v24", "v25", "v26", "v27", "v28", "v29", "v30", "v31",
            "v32", "v33", "v34", "v35", "v36", "v37", "v38", "v39", "v40", "v41", "v42", "v43", "v44", "v45", "v46", "v47", "v48", "v49", "v50", "v51",
            "v52", "v53");
      return;
    }

    const int head = wave - 4;
    const unsigned ka0 = c * 256 + ((g ^ ((c & 7) << 1)) << 4);
    const int vr = 4 * g + (c >> 2);
    const unsigned va0 = KT_BYTES + vr * 128 + (((vr >> 1) & 3) << 5) + (c & 3) * 8;
    unsigned xi = Q_OFF + head * XCH_BYTES + lane * 16;
    u32x4 ones = {0x3F803F80u, 0x3F803F80u, 0x3F803F80u, 0x3F803F80u};
    asm volatile("" : "+v"(ones), "+v"(xi));
    f32x4 o[8], lsum = {0.f, 0.f, 0.f, 0.f};
#pragma unroll
    for (int db = 0; db < 8; ++db) { o[db] = f32x4{0.f, 0.f, 0.f, 0.f}; asm volatile("" : "+v"(o[db])); }
    float nm = 3.0e38f;
    unsigned sb = SLOT_BYTES;
    unsigned kx[4];
#pragma unroll
    for (int st = 0; st < 4; ++st) kx[st] = (ka0 ^ (st << 6)) + sb;
    BARRIER();
    u32x4 q[4], kf[4][4];
#pragma unroll
    for (int st = 0; st < 4; ++st) q[st] = *(const LAS u32x4*)(lds + (Q_OFF + head * 4096) + (ka0 ^ (st << 6)));
#pragma unroll
    for (int sub = 0; sub < 4; ++sub)
#pragma unroll
      for (int st = 0; st < 4; ++st) kf[sub][st] = *(const LAS u32x4*)(lds + (ka0 ^ (st << 6)) + sub * 4096);
    asm volatile("" : "+v"(kf[0][0]), "+v"(kf[0][1]), "+v"(kf[0][2]), "+v"(kf[0][3]), "+v"(kf[1][0]), "+v"(kf[1][1]), "+v"(kf[1][2]), "+v"(kf[1][3]),
                 "+v"(kf[2][0]), "+v"(kf[2][1]), "+v"(kf[2][2]), "+v"(kf[2][3]), "+v"(kf[3][0]), "+v"(kf[3][1]), "+v"(kf[3][2]), "+v"(kf[3][3]),
                 "+v"(q[0]), "+v"(q[1]), "+v"(q[2]), "+v"(q[3]));
    BARRIER();
    int left = NI;
    asm volatile("" : "+s"(left));
#pragma unroll 1
    do {
      f32x4 s[4], x[4];
      u32x4 p[2];
      u32x2 vf[8][2];
      const f32x4 zero = {0.f, 0.f, 0.f, 0.f};
      sb = (sb + SLOT_BYTES) & (RING_BYTES - 1);
      unsigned vx[4];
#define QK1(sub, st) do { s[sub] = mfma(kf[sub][st], q[st], (st) ? s[sub] : zero); kf[sub][st] = *(const LAS u32x4*)(lds + kx[st] + (sub) * 4096); } while (0)
#define FM(sub) x[sub] = s[sub] * C2 + nm
#define EX(sub, r) x[sub][r] = __builtin_amdgcn_exp2f(x[sub][r])
#define CV(sub, h) p[(sub) >> 1][2 * ((sub) & 1) + (h)] = pk(x[sub][2 * (h)], x[sub][2 * (h) + 1])
#define VX(i) do { vx[i] = (i) ? vx[0] ^ ((i) << 5) : va0 + sb; asm volatile("" : "+v"(vx[i])); } while (0)
#define KX(i) do { kx[i] = (i) ? kx[0] ^ ((i) << 6) : ka0 + sb; asm volatile("" : "+v"(kx[i])); } while (0)
#define VRD(i, off) do { vf[i][0] = trd(lds + vx[(i) & 3] + ((i) >> 2) * 4096 + (off)); vf[i][1] = trd(lds + vx[(i) & 3] + ((i) >> 2) * 4096 + 2048 + (off)); } while (0)
#define PV1(i, ob) o[(ob) + ((i) & 3)] = mfma(u32x4{vf[i][0][0], vf[i][0][1], vf[i][1][0], vf[i][1][1]}, p[(i) >> 2], o[(ob) + ((i) & 3)])
      QK1(0, 0); SB(); QK1(1, 0); VX(0); SB(); QK1(0, 1); VX(1); SB(); QK1(1, 1); VX(2); SB();
      QK1(0, 2); VX(3); SB(); QK1(1, 2); SB(); QK1(0, 3); SB(); QK1(1, 3); SB();
      QK1(2, 0); SB(); QK1(3, 0); FM(0); SB();
      QK1(2, 1); FM(1); EX(0, 0); SB(); QK1(3, 1); EX(0, 1); EX(0, 2); SB();
      QK1(2, 2); EX(0, 3); EX(1, 0); SB(); QK1(3, 2); EX(1, 1); EX(1, 2); SB();
      QK1(2, 3); EX(1, 3); CV(0, 0); SB(); QK1(3, 3); CV(0, 1); CV(1, 0); CV(1, 1); SB();
      unsigned any = (p[0][0] | p[0][1] | p[0][2]) | p[0][3];
      asm volatile("" : "+v"(any) :: "memory");
      SB();
      FM(2); FM(3); SB();
      EX(2, 0); EX(2, 1); EX(2, 2); EX(2, 3); EX(3, 0); EX(3, 1); EX(3, 2); EX(3, 3); SB();
      CV(2, 0); CV(2, 1); CV(3, 0); CV(3, 1);
      any = (any | p[1][0] | p[1][1]) | p[1][2] | p[1][3];
      if (__builtin_expect(__builtin_amdgcn_ballot_w64((any & GE2) != 0) != 0, 0)) {
        float t = s[0][0];
#pragma unroll
        for (int sub = 0; sub < 4; ++sub)
#pragma unroll
          for (int r = 0; r < 4; ++r) t = fmaxf(t, s[sub][r]);
        const float nn = fminf(nm, -fmaf(xmax4(t), C2, SLACK));
        const float alpha = __builtin_amdgcn_exp2f(nn - nm);
        nm = nn;
#pragma unroll
        for (int db = 0; db < 8; ++db) o[db] *= alpha;
        lsum *= alpha;
#pragma unroll
        for (int sub = 0; sub < 4; ++sub) { FM(sub); EX(sub, 0); EX(sub, 1); EX(sub, 2); EX(sub, 3); CV(sub, 0); CV(sub, 1); }
      }
      BARRIER();
      VRD(0, 0); SB(); VRD(1, 0); SB(); VRD(2, 0); SB(); VRD(3, 0); SB(); VRD(4, 0); SB(); VRD(5, 0); SB();
      lsum = mfma(ones, p[0], lsum);
      lsum = mfma(ones, p[1], lsum);
      SB();
      PV1(0, 0); SB(); VRD(6, 0); SB(); PV1(1, 0); SB(); VRD(7, 0); SB(); PV1(2, 0); SB(); PV1(3, 0); KX(0); SB();
      PV1(4, 0); KX(1); SB(); PV1(5, 0); KX(2); SB(); PV1(6, 0); KX(3); SB(); PV1(7, 0); SB();
      if (__builtin_expect(left > NB, 0)) {
        VRD(0, VH_BYTES); SB(); VRD(1, VH_BYTES); SB(); VRD(2, VH_BYTES); SB(); VRD(3, VH_BYTES); SB(); VRD(4, VH_BYTES); SB();
        PV1(0, 4); SB(); VRD(5, VH_BYTES); SB(); PV1(1, 4); SB(); VRD(6, VH_BYTES); SB(); PV1(2, 4); SB(); VRD(7, VH_BYTES); SB();
        PV1(3, 4); PV1(4, 4); PV1(5, 4); PV1(6, 4); PV1(7, 4); SB();
        if (left == NB + 1) {
          *(LAS f32x4*)(lds + xi + 2048) = f32x4{nm, 0.f, lsum[0], 0.f};
#pragma unroll
          for (int i = 0; i < 2; ++i) {
            const f32x4 a = o[4 + 2 * i], b = o[5 + 2 * i];
            *(LAS u32x4*)(lds + xi + i * 1024) = u32x4{pk(a[0], a[1]), pk(a[2], a[3]), pk(b[0], b[1]), pk(b[2], b[3])};
          }
        }
      }
    } while (--left != 0);
    {
      asm volatile("" : "+v"(xi));
      const unsigned ln = (xi >> 4) & 63, xb = Q_OFF + head * XCH_BYTES;
      const unsigned xw = xb + (ln & 15) * 144 + (ln >> 4) * 8, xr = xb + (ln >> 3) * 144 + (ln & 7) * 16;
      const int oo = (ln >> 3) * 256 + half * 128 + (ln & 7) * 16;
      const f32x4 st = *(LAS f32x4*)(lds + (xi & ~0x300u) + 2048);
      const u32x4 yy[2] = {*(LAS u32x4*)(lds + xi), *(LAS u32x4*)(lds + xi + 1024)};
      SB();
      const float nn = fminf(nm, st[0]);
      const float b1 = __builtin_amdgcn_exp2f(nn - nm), b2 = __builtin_amdgcn_exp2f(nn - st[0]);
      const float inv = __builtin_amdgcn_rcpf(fmaf(b1, lsum[0], b2 * st[2]));
      const float w1 = b1 * inv, w2 = b2 * inv;
#pragma unroll
      for (int i = 0; i < 4; ++i) {
        const unsigned y0 = yy[i >> 1][2 * (i & 1)], y1 = yy[i >> 1][2 * (i & 1) + 1];
        const f32x4 y = {__uint_as_float(y0 << 16), __uint_as_float(y0 & 0xFFFF0000u), __uint_as_float(y1 << 16), __uint_as_float(y1 & 0xFFFF0000u)};
        const f32x4 r = o[i] * w1 + y * w2;
        *(LAS u32x2*)(lds + xw + i * 32) = u32x2{pk(r[0], r[1]), pk(r[2], r[3])};
      }
      asm volatile("" ::: "memory");
      const u32x4 r0 = *(LAS u32x4*)(lds + xr), r1 = *(LAS u32x4*)(lds + xr + 8 * 144);
      const __amdgpu_buffer_rsrc_t ro = __builtin_amdgcn_make_buffer_rsrc((char*)O + ((size_t)pair * 4 + head) * (SQ * 256), 0, SQ * 256, 0x00020000);
      __builtin_amdgcn_raw_buffer_store_b128(r0, ro, oo, 0, 18);
      __builtin_amdgcn_raw_buffer_store_b128(r1, ro, oo + 2048, 0, 18);
    }
    return;
  }

  const int vo0 = (lane >> 3) * 256 + half * 128 + (((lane & 7) ^ ((lane >> 3) & 6)) << 4);
  const int vo1 = vo0 + 1024, vo2 = vo0 ^ 128, vo3 = vo2 + 1024;
  const __amdgpu_buffer_rsrc_t rk = __builtin_amdgcn_make_buffer_rsrc((char*)K + (size_t)pair * (SK * 256), 0, SK * 256, 0x00020000);
  const __amdgpu_buffer_rsrc_t rq = __builtin_amdgcn_make_buffer_rsrc((char*)Q + (size_t)pair * KT_BYTES, 0, KT_BYTES, 0x00020000);
  const __amdgpu_buffer_rsrc_t rv = __builtin_amdgcn_make_buffer_rsrc((char*)V + (size_t)pair * (SK * 256), 0, SK * 256, 0x00020000);
  const unsigned lbase = (unsigned)(uintptr_t)lds;
#define KTILE(r, so, dst) \
    asm volatile("s_mov_b32 m0, %0\n\ts_nop 0\n\t" \
                 "buffer_load_dwordx4 %1, %3, %4 offen nt lds\n\t" \
                 "buffer_load_dwordx4 %2, %3, %4 offen offset:1024 nt lds\n\t" \
                 "buffer_load_dwordx4 %1, %3, %4 offen offset:2048 nt lds\n\t" \
                 "buffer_load_dwordx4 %2, %3, %4 offen offset:3072 nt lds" \
                 :: "s"(lbase + (dst) + wave * 4096), "v"(ko0), "v"(ko1), "s"(r), "s"((so) + wave * 4096) : "memory")
#define VHALF(a, b, so, dst) \
    asm volatile("s_mov_b32 m0, %0\n\ts_nop 0\n\t" \
                 "buffer_load_dwordx4 %1, %3, %4 offen nt lds\n\t" \
                 "buffer_load_dwordx4 %2, %3, %4 offen offset:1024 nt lds" \
                 :: "s"(lbase + (dst) + wave * 2048), "v"(a), "v"(b), "s"(rv), "s"((so) + wave * 4096) : "memory")
#define SLOT(s) (((s) & 3) * SLOT_BYTES)
#define TOFF(u) ((((((u) >> 2) & 1) | (((u) & 3) << 1) | (((u) >> 3) << 3)) ^ ((pair & 3) << 3)) * KT_BYTES)
#define OFF1(t) TOFF(2 * (t) + half)
#define OFF2(t) TOFF(NA + (t))
#define K1(t) KTILE(rk, OFF1(t), SLOT(t))
#define K2(t) KTILE(rk, OFF2(t), SLOT(t))
#define V1(t) do { VHALF(vo0, vo1, OFF1(t), SLOT((t) + 2) + KT_BYTES); VHALF(vo2, vo3, OFF1(t), SLOT((t) + 2) + KT_BYTES + VH_BYTES); } while (0)
#define V2(t) VHALF(vo0, vo1, OFF2(t), SLOT((t) + 2) + KT_BYTES)
#define PACE_WAIT(d) do { while ((int)((unsigned)__builtin_amdgcn_s_memrealtime() - due) < 0) __builtin_amdgcn_s_sleep(1); due += (d); } while (0)
  KTILE(rk, kso - wave * 4096, SLOT(0)); KTILE(rq, 0, Q_OFF);
  unsigned due = (unsigned)t64 + (PACE0 - 5 * (PACE / 2)) - (__builtin_amdgcn_s_getreg(20 | (3 << 11)) & 1) * ODD;
  PACE_WAIT(PACE / 2); K1(1);
  PACE_WAIT(PACE / 2); V1(0);
  wait_vm<8>(); LBARRIER();
  PACE_WAIT(PACE / 2); K1(2);
  PACE_WAIT(PACE / 2); V1(1);
  wait_vm<12>(); LBARRIER(); PACE_WAIT(PACE / 2); K1(3);
  wait_vm<12>(); PACE_WAIT(PACE / 2); V1(2); wait_vm<12>(); PACE_WAIT(PACE / 2); K1(4); LBARRIER();
#pragma unroll 1
  for (int s = 3; s < NA - 2; ++s) {
    wait_vm<12>(); PACE_WAIT(PACE / 2); V1(s);
    wait_vm<12>(); LBARRIER(); PACE_WAIT(PACE / 2); K1(s + 2);
  }
  wait_vm<12>(); PACE_WAIT(PACE / 2); V1(NA - 2); wait_vm<12>(); LBARRIER(); PACE_WAIT(PACE / 4); K2(NA);
  wait_vm<8>(); LBARRIER(); PACE_WAIT(0); V1(NA - 1); K2(NA + 1);
  wait_vm<12>(); V2(NA); wait_vm<10>(); K2(NA + 2); LBARRIER();
#pragma unroll 1
  for (int s = NA + 1; s < NI - 2; ++s) {
    wait_vm<10>(); V2(s);
    wait_vm<8>(); K2(s + 2); LBARRIER();
  }
  V2(NI - 2);
  wait_vm<8>(); LBARRIER();
  V2(NI - 1);
  wait_vm<4>(); LBARRIER();
  wait_vm<2>(); LBARRIER();
  wait_vm<0>(); LBARRIER();
}

}

extern "C" void kernel_launch(void* const* d_in, const int* in_sizes, int n_in, void* d_out, int out_size, void* d_ws, size_t ws_size,
                              hipStream_t stream) {
  gqa_attn<<<2 * NPAIR, NTHREADS, 0, stream>>>((const __hip_bfloat16*)d_in[0], (const __hip_bfloat16*)d_in[1], (const __hip_bfloat16*)d_in[2],
                                                (__hip_bfloat16*)d_out, (unsigned char*)d_ws);
}
```

```cpp
#define PACE0 362
#include <hip/hip_runtime.h>
#include <hip/hip_bf16.h>
#include <stdint.h>

#define PACE 108

#define ODD 40

namespace {

constexpr int B_ = 16, HKV = 8, SQ = 16, SK = 2048;
constexpr int TK = 64, NT = SK / TK;
constexpr int NA = 12, NB = NT - 2 * NA, NI = NA + NB;
constexpr int NPAIR = B_ * HKV;
constexpr int KT_BYTES = TK * 256, VH_BYTES = TK * 128;
constexpr int SLOT_BYTES = KT_BYTES + 2 * VH_BYTES;
constexpr int RING_BYTES = 4 * SLOT_BYTES;
constexpr int Q_OFF = RING_BYTES;
constexpr int XCH_BYTES = 4096;
constexpr int LDS_BYTES = RING_BYTES + 4 * XCH_BYTES;
constexpr int NTHREADS = 576;
constexpr float C2 = 0.08838834764831845f * 1.4426950408889634f;
constexpr float SLACK = 7.0f;
constexpr unsigned GE2 = 0x40004000u;
constexpr int FLAG_OFF = 8192, EPOCH_OFF = FLAG_OFF + 1024, BOX_BYTES = EPOCH_OFF + 256;
constexpr int CODE_LINES = 44;
static_assert(NB == 8, "the mail wave's barrier count");

typedef __attribute__((ext_vector_type(8))) __bf16 bf16x8;
typedef __attribute__((ext_vector_type(4))) __bf16 bf16x4;
typedef __attribute__((ext_vector_type(2))) __bf16 bf16x2;
typedef __attribute__((ext_vector_type(4))) float f32x4;
typedef __attribute__((ext_vector_type(2))) float f32x2;
typedef __attribute__((ext_vector_type(4))) unsigned u32x4;
typedef __attribute__((ext_vector_type(2))) unsigned u32x2;
#define LAS __attribute__((address_space(3)))

#define BARRIER() asm volatile("s_waitcnt lgkmcnt(0)\n\ts_barrier" ::: "memory")
#define LBARRIER() asm volatile("s_barrier" ::: "memory")
#define SB() __builtin_amdgcn_sched_barrier(0)

template <int N> __device__ __forceinline__ void wait_vm() { asm volatile("s_waitcnt vmcnt(%0)" ::"n"(N) : "memory"); }

__device__ __forceinline__ float xmax4(float x) {
  auto s = __builtin_amdgcn_permlane16_swap(__float_as_uint(x), __float_as_uint(x), false, false);
  x = fmaxf(__uint_as_float(s[0]), __uint_as_float(s[1]));
  auto t = __builtin_amdgcn_permlane32_swap(__float_as_uint(x), __float_as_uint(x), false, false);
  return fmaxf(__uint_as_float(t[0]), __uint_as_float(t[1]));
}
__device__ __forceinline__ unsigned pk(float a, float b) { f32x2 v = {a, b}; return __builtin_bit_cast(unsigned, __builtin_convertvector(v, bf16x2)); }
__device__ __forceinline__ f32x4 mfma(u32x4 a, u32x4 b, f32x4 c) {
  return __builtin_amdgcn_mfma_f32_16x16x32_bf16(__builtin_bit_cast(bf16x8, a), __builtin_bit_cast(bf16x8, b), c, 0, 0, 0);
}
__device__ __forceinline__ u32x2 trd(LAS char* p) { return __builtin_bit_cast(u32x2, __builtin_amdgcn_ds_read_tr16_b64_v4bf16((LAS bf16x4*)p)); }

__global__ void __launch_bounds__(NTHREADS, 3)
gqa_attn(const __hip_bfloat16* __restrict__ Q, const __hip_bfloat16* __restrict__ K, const __hip_bfloat16* __restrict__ V,
         __hip_bfloat16* __restrict__ O, unsigned char* __restrict__ ws) {
  __shared__ __attribute__((aligned(1024))) char smem[LDS_BYTES];
  LAS char* lds = (LAS char*)smem;
  SB();
  const int lane = threadIdx.x & 63;
  const int wave = __builtin_amdgcn_readfirstlane(threadIdx.x >> 6);
  const int pair = ((blockIdx.x >> 3) & 7) | ((blockIdx.x & 7) << 3) | (blockIdx.x & 64), half = (blockIdx.x >> 7) & 1;
  const int g = lane >> 4, c = lane & 15;
  const int ko0 = g * 256 + ((c ^ (g << 1)) << 4), ko1 = ko0 ^ 128;
  const int kso = ((half << 1) | ((pair & 3) << 3)) * KT_BYTES + wave * 4096;
  asm volatile("s_cmp_lg_u32 %0, 8\n\ts_cbranch_scc1 1f\n\t"
               "s_load_dwordx2 s[42:43], %2, 0x10\n\t"
               "s_getpc_b64 s[40:41]\n\tglobal_load_dword v54, %1, s[40:41]\n\t"
               "v_mov_b32 v55, %3\n\ts_waitcnt lgkmcnt(0)\n\tglobal_load_dword v55, v55, s[42:43] nt\n1:"
               :: "s"(wave), "v"(min(lane, CODE_LINES) * 128), "s"(__builtin_amdgcn_kernarg_segment_ptr()), "s"(pair << 19)
               : "memory", "scc", "s40", "s41", "s42", "s43", "v54", "v55");
  asm volatile("" :: "s"(K), "s"(Q), "s"(V), "s"(kso), "s"(pair << 19), "v"(ko0), "v"(ko1));

  if (__builtin_expect(wave >= 4, 0)) {
    if (wave == 8) {
      const __amdgpu_buffer_rsrc_t rp = __builtin_amdgcn_make_buffer_rsrc(ws + (size_t)pair * (2 * BOX_BYTES), 0, 2 * BOX_BYTES, 0x00020000);
      const int bo = half * BOX_BYTES, bi = (half ^ 1) * BOX_BYTES;
      const unsigned xb = (unsigned)(uintptr_t)lds + Q_OFF;
      int spins = 1 << 20;
      unsigned long long tmp64;
#define BAR "s_barrier\n\t"
#define FLAG_LOAD "buffer_load_dwordx4 v[48:51], %[po], %[rp], %[fi] offen sc1\n\t"
#define FLAG_TEST "v_cmp_ne_u32 vcc, v53, v49\n\tv_cmp_ne_u32 %[t], v53, v51\n\ts_nop 3\n\ts_or_b64 vcc, vcc, %[t]\n\t"
#define PART_IO(op, s0, s1) \
      op " v[16:19], %[po], %[rp], " s0 " offen sc1\n\t" op " v[20:23], %[po], %[rp], " s0 " offen offset:1024 sc1\n\t" \
      op " v[24:27], %[po], %[rp], " s0 " offen offset:2048 sc1\n\t" op " v[28:31], %[po], %[rp], " s0 " offen offset:3072 sc1\n\t" \
      op " v[32:35], %[po], %[rp], " s1 " offen sc1\n\t" op " v[36:39], %[po], %[rp], " s1 " offen offset:1024 sc1\n\t" \
      op " v[40:43], %[po], %[rp], " s1 " offen offset:2048 sc1\n\t" op " v[44:47], %[po], %[rp], " s1 " offen offset:3072 sc1\n\t"
      asm volatile(
          BAR BAR BAR BAR BAR BAR
          "buffer_load_dword v52, off, %[rp], %[fo] offset:1024 sc1\n\t"
          "buffer_load_dword v53, off, %[rp], %[fi] offset:1024 sc1\n\t"
          BAR BAR BAR BAR BAR BAR BAR BAR
          "s_waitcnt vmcnt(0)\n\t"
          "v_add_u32 v52, 0x9e3779b1, v52\n\tv_add_u32 v53, 0x9e3779b1, v53\n\t"
          BAR
          "ds_read_b128 v[16:19], %[xi]\n\tds_read_b128 v[20:23], %[xi] offset:1024\n\t"
          "ds_read_b128 v[24:27], %[xi] offset:4096\n\tds_read_b128 v[28:31], %[xi] offset:5120\n\t"
          "ds_read_b128 v[32:35], %[xi] offset:8192\n\tds_read_b128 v[36:39], %[xi] offset:9216\n\t"
          "ds_read_b128 v[40:43], %[xi] offset:12288\n\tds_read_b128 v[44:47], %[xi] offset:13312\n\t"
          "ds_read_b128 v[48:51], %[xs]\n\t"
          "s_waitcnt lgkmcnt(0)\n\t"
          PART_IO("buffer_store_dwordx4", "%[bo]", "%[bo2]")
          "v_mov_b32 v49, v52\n\tv_mov_b32 v51, v52\n\t"
          BAR
          "s_waitcnt vmcnt(0)\n\t"
          "buffer_store_dwordx4 v[48:51], %[po], %[rp], %[fo] offen sc1\n\t"
          BAR BAR BAR
          FLAG_LOAD
          BAR
          "s_waitcnt vmcnt(0)\n\t"
          FLAG_TEST
          "s_cbranch_vccnz 1f\n\t"
          PART_IO("buffer_load_dwordx4", "%[bi]", "%[bi2]")
          BAR
          "3:\n\t"
          "ds_write_b128 %[xs], v[48:51]\n\t"
          "s_waitcnt vmcnt(0)\n\t"
          "ds_write_b128 %[xi], v[16:19]\n\tds_write_b128 %[xi], v[20:23] offset:1024\n\t"
          "ds_write_b128 %[xi], v[24:27] offset:4096\n\tds_write_b128 %[xi], v[28:31] offset:5120\n\t"
          "ds_write_b128 %[xi], v[32:35] offset:8192\n\tds_write_b128 %[xi], v[36:39] offset:9216\n\t"
          "ds_write_b128 %[xi], v[40:43] offset:12288\n\tds_write_b128 %[xi], v[44:47] offset:13312\n\t"
          "s_waitcnt lgkmcnt(0)\n\t"
          BAR
          "v_xor_b32 v53, v53, %[po]\n\t"
          "buffer_store_dword v53, %[eo], %[rp], %[bi] offen sc1\n\t"
          "s_branch 4f\n"
          "1:\n\t"
          FLAG_LOAD
          BAR
          "2:\n\t"
          "s_waitcnt vmcnt(0)\n\t"
          FLAG_TEST
          "s_cbranch_vccz 5f\n\t"
          FLAG_LOAD
          "s_sub_u32 %[spins], %[spins], 1\n\t"
          "s_cbranch_scc0 2b\n\t"
          "s_waitcnt vmcnt(0)\n\t"
          "v_mov_b32 v50, 0x7fc00000\n"
          "5:\n\t"
          PART_IO("buffer_load_dwordx4", "%[bi]", "%[bi2]")
          "s_branch 3b\n"
          "4:"
          : [spins] "+s"(spins), [t] "=&s"(tmp64)
          : [bi] "s"(bi), [bi2] "s"(bi + 4096), [fi] "s"(bi + FLAG_OFF), [bo] "s"(bo), [bo2] "s"(bo + 4096), [fo] "s"(bo + FLAG_OFF),
            [po] "v"(lane * 16), [eo] "v"(lane == 0 ? EPOCH_OFF : lane == 1 ? FLAG_OFF + 4 : 2 * BOX_BYTES),
            [xi] "v"(xb + lane * 16), [xs] "v"(xb + g * XCH_BYTES + 2048 + c * 16), [rp] "s"(rp)
          : "memory", "vcc", "scc", "v16", "v17", "v18", "v19", "v20", "v21", "v22", "v23", "v24", "v25", "v26", "v27", "v28", "v29", "v30", "v31",
            "v32", "v33", "v34", "v35", "v36", "v37", "v38", "v39", "v40", "v41", "v42", "v43", "v44", "v45", "v46", "v47", "v48", "v49", "v50", "v51",
            "v52", "v53");
      return;
    }

    const int head = wave - 4;
    const unsigned ka0 = c * 256 + ((g ^ ((c & 7) << 1)) << 4);
    const int vr = 4 * g + (c >> 2);
    const unsigned va0 = KT_BYTES + vr * 128 + (((vr >> 1) & 3) << 5) + (c & 3) * 8;
    unsigned xi = Q_OFF + head * XCH_BYTES + lane * 16;
    u32x4 ones = {0x3F803F80u, 0x3F803F80u, 0x3F803F80u, 0x3F803F80u};
    asm volatile("" : "+v"(ones), "+v"(xi));
    f32x4 o[8], lsum = {0.f, 0.f, 0.f, 0.f};
#pragma unroll
    for (int db = 0; db < 8; ++db) { o[db] = f32x4{0.f, 0.f, 0.f, 0.f}; asm volatile("" : "+v"(o[db])); }
    float nm = 3.0e38f;
    unsigned sb = SLOT_BYTES;
    unsigned kx[4];
#pragma unroll
    for (int st = 0; st < 4; ++st) kx[st] = (ka0 ^ (st << 6)) + sb;
    BARRIER();
    u32x4 q[4], kf[4][4];
#pragma unroll
    for (int st = 0; st < 4; ++st) q[st] = *(const LAS u32x4*)(lds + (Q_OFF + head * 4096) + (ka0 ^ (st << 6)));
#pragma unroll
    for (int sub = 0; sub < 4; ++sub)
#pragma unroll
      for (int st = 0; st < 4; ++st) kf[sub][st] = *(const LAS u32x4*)(lds + (ka0 ^ (st << 6)) + sub * 4096);
    asm volatile("" : "+v"(kf[0][0]), "+v"(kf[0][1]), "+v"(kf[0][2]), "+v"(kf[0][3]), "+v"(kf[1][0]), "+v"(kf[1][1]), "+v"(kf[1][2]), "+v"(kf[1][3]),
                 "+v"(kf[2][0]), "+v"(kf[2][1]), "+v"(kf[2][2]), "+v"(kf[2][3]), "+v"(kf[3][0]), "+v"(kf[3][1]), "+v"(kf[3][2]), "+v"(kf[3][3]),
                 "+v"(q[0]), "+v"(q[1]), "+v"(q[2]), "+v"(q[3]));
    BARRIER();
    int left = NI;
    asm volatile("" : "+s"(left));
#pragma unroll 1
    do {
      f32x4 s[4], x[4];
      u32x4 p[2];
      u32x2 vf[8][2];
      const f32x4 zero = {0.f, 0.f, 0.f, 0.f};
      sb = (sb + SLOT_BYTES) & (RING_BYTES - 1);
      unsigned vx[4];
#define QK1(sub, st) do { s[sub] = mfma(kf[sub][st], q[st], (st) ? s[sub] : zero); kf[sub][st] = *(const LAS u32x4*)(lds + kx[st] + (sub) * 4096); } while (0)
#define FM(sub) x[sub] = s[sub] * C2 + nm
#define EX(sub, r) x[sub][r] = __builtin_amdgcn_exp2f(x[sub][r])
#define CV(sub, h) p[(sub) >> 1][2 * ((sub) & 1) + (h)] = pk(x[sub][2 * (h)], x[sub][2 * (h) + 1])
#define VX(i) do { vx[i] = (i) ? vx[0] ^ ((i) << 5) : va0 + sb; asm volatile("" : "+v"(vx[i])); } while (0)
#define KX(i) do { kx[i] = (i) ? kx[0] ^ ((i) << 6) : ka0 + sb; asm volatile("" : "+v"(kx[i])); } while (0)
#define VRD(i, off) do { vf[i][0] = trd(lds + vx[(i) & 3] + ((i) >> 2) * 4096 + (off)); vf[i][1] = trd(lds + vx[(i) & 3] + ((i) >> 2) * 4096 + 2048 + (off)); } while (0)
#define PV1(i, ob) o[(ob) + ((i) & 3)] = mfma(u32x4{vf[i][0][0], vf[i][0][1], vf[i][1][0], vf[i][1][1]}, p[(i) >> 2], o[(ob) + ((i) & 3)])
      QK1(0, 0); SB(); QK1(1, 0); VX(0); SB(); QK1(0, 1); VX(1); SB(); QK1(1, 1); VX(2); SB();
      QK1(0, 2); VX(3); SB(); QK1(1, 2); SB(); QK1(0, 3); SB(); QK1(1, 3); SB();
      QK1(2, 0); SB(); QK1(3, 0); FM(0); SB();
      QK1(2, 1); FM(1); EX(0, 0); SB(); QK1(3, 1); EX(0, 1); EX(0, 2); SB();
      QK1(2, 2); EX(0, 3); EX(1, 0); SB(); QK1(3, 2); EX(1, 1); EX(1, 2); SB();
      QK1(2, 3); EX(1, 3); CV(0, 0); SB(); QK1(3, 3); CV(0, 1); CV(1, 0); CV(1, 1); SB();
      unsigned any = (p[0][0] | p[0][1] | p[0][2]) | p[0][3];
      asm volatile("" : "+v"(any) :: "memory");
      SB();
      FM(2); FM(3); SB();
      EX(2, 0); EX(2, 1); EX(2, 2); EX(2, 3); EX(3, 0); EX(3, 1); EX(3, 2); EX(3, 3); SB();
      CV(2, 0); CV(2, 1); CV(3, 0); CV(3, 1);
      any = (any | p[1][0] | p[1][1]) | p[1][2] | p[1][3];
      if (__builtin_expect(__builtin_amdgcn_ballot_w64((any & GE2) != 0) != 0, 0)) {
        float t = s[0][0];
#pragma unroll
        for (int sub = 0; sub < 4; ++sub)
#pragma unroll
          for (int r = 0; r < 4; ++r) t = fmaxf(t, s[sub][r]);
        const float nn = fminf(nm, -fmaf(xmax4(t), C2, SLACK));
        const float alpha = __builtin_amdgcn_exp2f(nn - nm);
        nm = nn;
#pragma unroll
        for (int db = 0; db < 8; ++db) o[db] *= alpha;
        lsum *= alpha;
#pragma unroll
        for (int sub = 0; sub < 4; ++sub) { FM(sub); EX(sub, 0); EX(sub, 1); EX(sub, 2); EX(sub, 3); CV(sub, 0); CV(sub, 1); }
      }
      BARRIER();
      VRD(0, 0); SB(); VRD(1, 0); SB(); VRD(2, 0); SB(); VRD(3, 0); SB(); VRD(4, 0); SB(); VRD(5, 0); SB();
      lsum = mfma(ones, p[0], lsum);
      lsum = mfma(ones, p[1], lsum);
      SB();
      PV1(0, 0); SB(); VRD(6, 0); SB(); PV1(1, 0); SB(); VRD(7, 0); SB(); PV1(2, 0); SB(); PV1(3, 0); KX(0); SB();
      PV1(4, 0); KX(1); SB(); PV1(5, 0); KX(2); SB(); PV1(6, 0); KX(3); SB(); PV1(7, 0); SB();
      if (__builtin_expect(left > NB, 0)) {
        VRD(0, VH_BYTES); SB(); VRD(1, VH_BYTES); SB(); VRD(2, VH_BYTES); SB(); VRD(3, VH_BYTES); SB(); VRD(4, VH_BYTES); SB();
        PV1(0, 4); SB(); VRD(5, VH_BYTES); SB(); PV1(1, 4); SB(); VRD(6, VH_BYTES); SB(); PV1(2, 4); SB(); VRD(7, VH_BYTES); SB();
        PV1(3, 4); PV1(4, 4); PV1(5, 4); PV1(6, 4); PV1(7, 4); SB();
        if (left == NB + 1) {
          *(LAS f32x4*)(lds + xi + 2048) = f32x4{nm, 0.f, lsum[0], 0.f};
#pragma unroll
          for (int i = 0; i < 2; ++i) {
            const f32x4 a = o[4 + 2 * i], b = o[5 + 2 * i];
            *(LAS u32x4*)(lds + xi + i * 1024) = u32x4{pk(a[0], a[1]), pk(a[2], a[3]), pk(b[0], b[1]), pk(b[2], b[3])};
          }
        }
      }
    } while (--left != 0);
    {
      asm volatile("" : "+v"(xi));
      const unsigned ln = (xi >> 4) & 63, xb = Q_OFF + head * XCH_BYTES;
      const unsigned xw = xb + (ln & 15) * 144 + (ln >> 4) * 8, xr = xb + (ln >> 3) * 144 + (ln & 7) * 16;
      const int oo = (ln >> 3) * 256 + half * 128 + (ln & 7) * 16;
      const f32x4 st = *(LAS f32x4*)(lds + (xi & ~0x300u) + 2048);
      const u32x4 yy[2] = {*(LAS u32x4*)(lds + xi), *(LAS u32x4*)(lds + xi + 1024)};
      SB();
      const float nn = fminf(nm, st[0]);
      const float b1 = __builtin_amdgcn_exp2f(nn - nm), b2 = __builtin_amdgcn_exp2f(nn - st[0]);
      const float inv = __builtin_amdgcn_rcpf(fmaf(b1, lsum[0], b2 * st[2]));
      const float w1 = b1 * inv, w2 = b2 * inv;
#pragma unroll
      for (int i = 0; i < 4; ++i) {
        const unsigned y0 = yy[i >> 1][2 * (i & 1)], y1 = yy[i >> 1][2 * (i & 1) + 1];
        const f32x4 y = {__uint_as_float(y0 << 16), __uint_as_float(y0 & 0xFFFF0000u), __uint_as_float(y1 << 16), __uint_as_float(y1 & 0xFFFF0000u)};
        const f32x4 r = o[i] * w1 + y * w2;
        *(LAS u32x2*)(lds + xw + i * 32) = u32x2{pk(r[0], r[1]), pk(r[2], r[3])};
      }
      asm volatile("" ::: "memory");
      const u32x4 r0 = *(LAS u32x4*)(lds + xr), r1 = *(LAS u32x4*)(lds + xr + 8 * 144);
      const __amdgpu_buffer_rsrc_t ro = __builtin_amdgcn_make_buffer_rsrc((char*)O + ((size_t)pair * 4 + head) * (SQ * 256), 0, SQ * 256, 0x00020000);
      __builtin_amdgcn_raw_buffer_store_b128(r0, ro, oo, 0, 18);
      __builtin_amdgcn_raw_buffer_store_b128(r1, ro, oo + 2048, 0, 18);
    }
    return;
  }

  const int vo0 = (lane >> 3) * 256 + half * 128 + (((lane & 7) ^ ((lane >> 3) & 6)) << 4);
  const int vo1 = vo0 + 1024, vo2 = vo0 ^ 128, vo3 = vo2 + 1024;
  const __amdgpu_buffer_rsrc_t rk = __builtin_amdgcn_make_buffer_rsrc((char*)K + (size_t)pair * (SK * 256), 0, SK * 256, 0x00020000);
  const __amdgpu_buffer_rsrc_t rq = __builtin_amdgcn_make_buffer_rsrc((char*)Q + (size_t)pair * KT_BYTES, 0, KT_BYTES, 0x00020000);
  const __amdgpu_buffer_rsrc_t rv = __builtin_amdgcn_make_buffer_rsrc((char*)V + (size_t)pair * (SK * 256), 0, SK * 256, 0x00020000);
  const unsigned lbase = (unsigned)(uintptr_t)lds;
#define KTILE(r, so, dst) \
    asm volatile("s_mov_b32 m0, %0\n\ts_nop 0\n\t" \
                 "buffer_load_dwordx4 %1, %3, %4 offen nt lds\n\t" \
                 "buffer_load_dwordx4 %2, %3, %4 offen offset:1024 nt lds\n\t" \
                 "buffer_load_dwordx4 %1, %3, %4 offen offset:2048 nt lds\n\t" \
                 "buffer_load_dwordx4 %2, %3, %4 offen offset:3072 nt lds" \
                 :: "s"(lbase + (dst) + wave * 4096), "v"(ko0), "v"(ko1), "s"(r), "s"((so) + wave * 4096) : "memory")
#define VHALF(a, b, so, dst) \
    asm volatile("s_mov_b32 m0, %0\n\ts_nop 0\n\t" \
                 "buffer_load_dwordx4 %1, %3, %4 offen nt lds\n\t" \
                 "buffer_load_dwordx4 %2, %3, %4 offen offset:1024 nt lds" \
                 :: "s"(lbase + (dst) + wave * 2048), "v"(a), "v"(b), "s"(rv), "s"((so) + wave * 4096) : "memory")
#define SLOT(s) (((s) & 3) * SLOT_BYTES)
#define TOFF(u) ((((((u) >> 2) & 1) | (((u) & 3) << 1) | (((u) >> 3) << 3)) ^ ((pair & 3) << 3)) * KT_BYTES)
#define OFF1(t) TOFF(2 * (t) + half)
#define OFF2(t) TOFF(NA + (t))
#define K1(t) KTILE(rk, OFF1(t), SLOT(t))
#define K2(t) KTILE(rk, OFF2(t), SLOT(t))
#define V1(t) do { VHALF(vo0, vo1, OFF1(t), SLOT((t) + 2) + KT_BYTES); VHALF(vo2, vo3, OFF1(t), SLOT((t) + 2) + KT_BYTES + VH_BYTES); } while (0)
#define V2(t) VHALF(vo0, vo1, OFF2(t), SLOT((t) + 2) + KT_BYTES)
#define PACE_WAIT(d) do { while ((int)((unsigned)__builtin_amdgcn_s_memrealtime() - due) < 0) __builtin_amdgcn_s_sleep(1); due += (d); } while (0)
  unsigned long long t64;
  asm volatile("s_memrealtime %0" : "=&s"(t64) :: "memory");
  KTILE(rk, kso - wave * 4096, SLOT(0)); KTILE(rq, 0, Q_OFF);
  asm volatile("s_waitcnt lgkmcnt(0)" : "+s"(t64) :: "memory");
  unsigned due = (unsigned)t64 + (PACE0 - 5 * (PACE / 2)) - (__builtin_amdgcn_s_getreg(20 | (3 << 11)) & 1) * ODD;
  PACE_WAIT(PACE / 2); K1(1);
  PACE_WAIT(PACE / 2); V1(0);
  wait_vm<8>(); LBARRIER();
  PACE_WAIT(PACE / 2); K1(2);
  PACE_WAIT(PACE / 2); V1(1);
  wait_vm<12>(); LBARRIER(); PACE_WAIT(PACE / 2); K1(3);
  wait_vm<12>(); PACE_WAIT(PACE / 2); V1(2); wait_vm<12>(); PACE_WAIT(PACE / 2); K1(4); LBARRIER();
#pragma unroll 1
  for (int s = 3; s < NA - 2; ++s) {
    wait_vm<12>(); PACE_WAIT(PACE / 2); V1(s);
    wait_vm<12>(); LBARRIER(); PACE_WAIT(PACE / 2); K1(s + 2);
  }
  wait_vm<12>(); PACE_WAIT(PACE / 2); V1(NA - 2); wait_vm<12>(); LBARRIER(); PACE_WAIT(PACE / 4); K2(NA);
  wait_vm<8>(); LBARRIER(); PACE_WAIT(0); V1(NA - 1); K2(NA + 1);
  wait_vm<12>(); V2(NA); wait_vm<10>(); K2(NA + 2); LBARRIER();
#pragma unroll 1
  for (int s = NA + 1; s < NI - 2; ++s) {
    wait_vm<10>(); V2(s);
    wait_vm<8>(); K2(s + 2); LBARRIER();
  }
  V2(NI - 2);
  wait_vm<8>(); LBARRIER();
  V2(NI - 1);
  wait_vm<4>(); LBARRIER();
  wait_vm<2>(); LBARRIER();
  wait_vm<0>(); LBARRIER();
}

}

extern "C" void kernel_launch(void* const* d_in, const int* in_sizes, int n_in, void* d_out, int out_size, void* d_ws, size_t ws_size,
                              hipStream_t stream) {
  gqa_attn<<<2 * NPAIR, NTHREADS, 0, stream>>>((const __hip_bfloat16*)d_in[0], (const __hip_bfloat16*)d_in[1], (const __hip_bfloat16*)d_in[2],
                                                (__hip_bfloat16*)d_out, (unsigned char*)d_ws);
}
```

```cpp
#include <hip/hip_runtime.h>
#include <hip/hip_bf16.h>
#include <stdint.h>

#define PACE 108
#define PACE0 416

#define ODD 40

namespace {

constexpr int B_ = 16, HKV = 8, SQ = 16, SK = 2048;
constexpr int TK = 64, NT = SK / TK;
constexpr int NA = 12, NB = NT - 2 * NA, NI = NA + NB;
constexpr int NPAIR = B_ * HKV;
constexpr int KT_BYTES = TK * 256, VH_BYTES = TK * 128;
constexpr int SLOT_BYTES = KT_BYTES + 2 * VH_BYTES;
constexpr int RING_BYTES = 4 * SLOT_BYTES;
constexpr int Q_OFF = RING_BYTES;
constexpr int XCH_BYTES = 4096;
constexpr int LDS_BYTES = RING_BYTES + 4 * XCH_BYTES;
constexpr int NTHREADS = 576;
constexpr float C2 = 0.08838834764831845f * 1.4426950408889634f;
constexpr float SLACK = 7.0f;
constexpr unsigned GE2 = 0x40004000u;
constexpr int FLAG_OFF = 8192, EPOCH_OFF = FLAG_OFF + 1024, BOX_BYTES = EPOCH_OFF + 256;
constexpr int CODE_LINES = 44;
static_assert(NB == 8, "the mail wave's barrier count");

typedef __attribute__((ext_vector_type(8))) __bf16 bf16x8;
typedef __attribute__((ext_vector_type(4))) __bf16 bf16x4;
typedef __attribute__((ext_vector_type(2))) __bf16 bf16x2;
typedef __attribute__((ext_vector_type(4))) float f32x4;
typedef __attribute__((ext_vector_type(2))) float f32x2;
typedef __attribute__((ext_vector_type(4))) unsigned u32x4;
typedef __attribute__((ext_vector_type(2))) unsigned u32x2;
#define LAS __attribute__((address_space(3)))

#define BARRIER() asm volatile("s_waitcnt lgkmcnt(0)\n\ts_barrier" ::: "memory")
#define LBARRIER() asm volatile("s_barrier" ::: "memory")
#define SB() __builtin_amdgcn_sched_barrier(0)

template <int N> __device__ __forceinline__ void wait_vm() { asm volatile("s_waitcnt vmcnt(%0)" ::"n"(N) : "memory"); }

__device__ __forceinline__ float xmax4(float x) {
  auto s = __builtin_amdgcn_permlane16_swap(__float_as_uint(x), __float_as_uint(x), false, false);
  x = fmaxf(__uint_as_float(s[0]), __uint_as_float(s[1]));
  auto t = __builtin_amdgcn_permlane32_swap(__float_as_uint(x), __float_as_uint(x), false, false);
  return fmaxf(__uint_as_float(t[0]), __uint_as_float(t[1]));
}
__device__ __forceinline__ unsigned pk(float a, float b) { f32x2 v = {a, b}; return __builtin_bit_cast(unsigned, __builtin_convertvector(v, bf16x2)); }
__device__ __forceinline__ f32x4 mfma(u32x4 a, u32x4 b, f32x4 c) {
  return __builtin_amdgcn_mfma_f32_16x16x32_bf16(__builtin_bit_cast(bf16x8, a), __builtin_bit_cast(bf16x8, b), c, 0, 0, 0);
}
__device__ __forceinline__ u32x2 trd(LAS char* p) { return __builtin_bit_cast(u32x2, __builtin_amdgcn_ds_read_tr16_b64_v4bf16((LAS bf16x4*)p)); }

__global__ void __launch_bounds__(NTHREADS, 3)
gqa_attn(const __hip_bfloat16* __restrict__ Q, const __hip_bfloat16* __restrict__ K, const __hip_bfloat16* __restrict__ V,
         __hip_bfloat16* __restrict__ O, unsigned char* __restrict__ ws) {
  __shared__ __attribute__((aligned(1024))) char smem[LDS_BYTES];
  LAS char* lds = (LAS char*)smem;
  const unsigned long long t64 = __builtin_amdgcn_s_memrealtime();
  SB();
  const int lane = threadIdx.x & 63;
  const int wave = __builtin_amdgcn_readfirstlane(threadIdx.x >> 6);
  const int pair = ((blockIdx.x >> 3) & 7) | ((blockIdx.x & 7) << 3) | (blockIdx.x & 64), half = (blockIdx.x >> 7) & 1;
  const int g = lane >> 4, c = lane & 15;
  const int ko0 = g * 256 + ((c ^ (g << 1)) << 4), ko1 = ko0 ^ 128;
  const int kso = ((half << 1) | ((pair & 3) << 3)) * KT_BYTES + wave * 4096;
  asm volatile("s_cmp_lg_u32 %0, 8\n\ts_cbranch_scc1 1f\n\t"
               "s_load_dwordx2 s[42:43], %2, 0x10\n\t"
               "s_getpc_b64 s[40:41]\n\tglobal_load_dword v54, %1, s[40:41]\n\t"
               "v_mov_b32 v55, %3\n\ts_waitcnt lgkmcnt(0)\n\tglobal_load_dword v55, v55, s[42:43] nt\n1:"
               :: "s"(wave), "v"(min(lane, CODE_LINES) * 128), "s"(__builtin_amdgcn_kernarg_segment_ptr()), "s"(pair << 19)
               : "memory", "scc", "s40", "s41", "s42", "s43", "v54", "v55");
  asm volatile("" :: "s"(K), "s"(Q), "s"(V), "s"(t64), "s"(kso), "s"(pair << 19), "v"(ko0), "v"(ko1));

  if (__builtin_expect(wave >= 4, 0)) {
    if (wave == 8) {
      const __amdgpu_buffer_rsrc_t rp = __builtin_amdgcn_make_buffer_rsrc(ws + (size_t)pair * (2 * BOX_BYTES), 0, 2 * BOX_BYTES, 0x00020000);
      const int bo = half * BOX_BYTES, bi = (half ^ 1) * BOX_BYTES;
      const unsigned xb = (unsigned)(uintptr_t)lds + Q_OFF;
      int spins = 1 << 20;
      unsigned long long tmp64;
#define BAR "s_barrier\n\t"
#define FLAG_LOAD "buffer_load_dwordx4 v[48:51], %[po], %[rp], %[fi] offen sc1\n\t"
#define FLAG_TEST "v_cmp_ne_u32 vcc, v53, v49\n\tv_cmp_ne_u32 %[t], v53, v51\n\ts_nop 3\n\ts_or_b64 vcc, vcc, %[t]\n\t"
#define PART_IO(op, s0, s1) \
      op " v[16:19], %[po], %[rp], " s0 " offen sc1\n\t" op " v[20:23], %[po], %[rp], " s0 " offen offset:1024 sc1\n\t" \
      op " v[24:27], %[po], %[rp], " s0 " offen offset:2048 sc1\n\t" op " v[28:31], %[po], %[rp], " s0 " offen offset:3072 sc1\n\t" \
      op " v[32:35], %[po], %[rp], " s1 " offen sc1\n\t" op " v[36:39], %[po], %[rp], " s1 " offen offset:1024 sc1\n\t" \
      op " v[40:43], %[po], %[rp], " s1 " offen offset:2048 sc1\n\t" op " v[44:47], %[po], %[rp], " s1 " offen offset:3072 sc1\n\t"
      asm volatile(
          BAR BAR BAR BAR BAR BAR
          "buffer_load_dword v52, off, %[rp], %[fo] offset:1024 sc1\n\t"
          "buffer_load_dword v53, off, %[rp], %[fi] offset:1024 sc1\n\t"
          BAR BAR BAR BAR BAR BAR BAR BAR
          "s_waitcnt vmcnt(0)\n\t"
          "v_add_u32 v52, 0x9e3779b1, v52\n\tv_add_u32 v53, 0x9e3779b1, v53\n\t"
          BAR
          "ds_read_b128 v[16:19], %[xi]\n\tds_read_b128 v[20:23], %[xi] offset:1024\n\t"
          "ds_read_b128 v[24:27], %[xi] offset:4096\n\tds_read_b128 v[28:31], %[xi] offset:5120\n\t"
          "ds_read_b128 v[32:35], %[xi] offset:8192\n\tds_read_b128 v[36:39], %[xi] offset:9216\n\t"
          "ds_read_b128 v[40:43], %[xi] offset:12288\n\tds_read_b128 v[44:47], %[xi] offset:13312\n\t"
          "ds_read_b128 v[48:51], %[xs]\n\t"
          "s_waitcnt lgkmcnt(0)\n\t"
          PART_IO("buffer_store_dwordx4", "%[bo]", "%[bo2]")
          "v_mov_b32 v49, v52\n\tv_mov_b32 v51, v52\n\t"
          BAR
          "s_waitcnt vmcnt(0)\n\t"
          "buffer_store_dwordx4 v[48:51], %[po], %[rp], %[fo] offen sc1\n\t"
          BAR BAR BAR
          FLAG_LOAD
          BAR
          "s_waitcnt vmcnt(0)\n\t"
          FLAG_TEST
          "s_cbranch_vccnz 1f\n\t"
          PART_IO("buffer_load_dwordx4", "%[bi]", "%[bi2]")
          BAR
          "3:\n\t"
          "ds_write_b128 %[xs], v[48:51]\n\t"
          "s_waitcnt vmcnt(0)\n\t"
          "ds_write_b128 %[xi], v[16:19]\n\tds_write_b128 %[xi], v[20:23] offset:1024\n\t"
          "ds_write_b128 %[xi], v[24:27] offset:4096\n\tds_write_b128 %[xi], v[28:31] offset:5120\n\t"
          "ds_write_b128 %[xi], v[32:35] offset:8192\n\tds_write_b128 %[xi], v[36:39] offset:9216\n\t"
          "ds_write_b128 %[xi], v[40:43] offset:12288\n\tds_write_b128 %[xi], v[44:47] offset:13312\n\t"
          "s_waitcnt lgkmcnt(0)\n\t"
          BAR
          "v_xor_b32 v53, v53, %[po]\n\t"
          "buffer_store_dword v53, %[eo], %[rp], %[bi] offen sc1\n\t"
          "s_branch 4f\n"
          "1:\n\t"
          FLAG_LOAD
          BAR
          "2:\n\t"
          "s_waitcnt vmcnt(0)\n\t"
          FLAG_TEST
          "s_cbranch_vccz 5f\n\t"
          FLAG_LOAD
          "s_sub_u32 %[spins], %[spins], 1\n\t"
          "s_cbranch_scc0 2b\n\t"
          "s_waitcnt vmcnt(0)\n\t"
          "v_mov_b32 v50, 0x7fc00000\n"
          "5:\n\t"
          PART_IO("buffer_load_dwordx4", "%[bi]", "%[bi2]")
          "s_branch 3b\n"
          "4:"
          : [spins] "+s"(spins), [t] "=&s"(tmp64)
          : [bi] "s"(bi), [bi2] "s"(bi + 4096), [fi] "s"(bi + FLAG_OFF), [bo] "s"(bo), [bo2] "s"(bo + 4096), [fo] "s"(bo + FLAG_OFF),
            [po] "v"(lane * 16), [eo] "v"(lane == 0 ? EPOCH_OFF : lane == 1 ? FLAG_OFF + 4 : 2 * BOX_BYTES),
            [xi] "v"(xb + lane * 16), [xs] "v"(xb + g * XCH_BYTES + 2048 + c * 16), [rp] "s"(rp)
          : "memory", "vcc", "scc", "v16", "v17", "v18", "v19", "v20", "v21", "v22", "v23", "v24", "v25", "v26", "v27", "v28", "v29", "v30", "v31",
            "v32", "v33", "v34", "v35", "v36", "v37", "v38", "v39", "v40", "v41", "v42", "v43", "v44", "v45", "v46", "v47", "v48", "v49", "v50", "v51",
            "v52", "v53");
      return;
    }

    const int head = wave - 4;
    const unsigned ka0 = c * 256 + ((g ^ ((c & 7) << 1)) << 4);
    const int vr = 4 * g + (c >> 2);
    const unsigned va0 = KT_BYTES + vr * 128 + (((vr >> 1) & 3) << 5) + (c & 3) * 8;
    unsigned xi = Q_OFF + head * XCH_BYTES + lane * 16;
    u32x4 ones = {0x3F803F80u, 0x3F803F80u, 0x3F803F80u, 0x3F803F80u};
    asm volatile("" : "+v"(ones), "+v"(xi));
    f32x4 o[8], lsum = {0.f, 0.f, 0.f, 0.f};
#pragma unroll
    for (int db = 0; db < 8; ++db) { o[db] = f32x4{0.f, 0.f, 0.f, 0.f}; asm volatile("" : "+v"(o[db])); }
    float nm = 3.0e38f;
    unsigned sb = SLOT_BYTES;
    unsigned kx[4];
#pragma unroll
    for (int st = 0; st < 4; ++st) kx[st] = (ka0 ^ (st << 6)) + sb;
    BARRIER();
    u32x4 q[4], kf[4][4];
#pragma unroll
    for (int st = 0; st < 4; ++st) q[st] = *(const LAS u32x4*)(lds + (Q_OFF + head * 4096) + (ka0 ^ (st << 6)));
#pragma unroll
    for (int sub = 0; sub < 4; ++sub)
#pragma unroll
      for (int st = 0; st < 4; ++st) kf[sub][st] = *(const LAS u32x4*)(lds + (ka0 ^ (st << 6)) + sub * 4096);
    asm volatile("" : "+v"(kf[0][0]), "+v"(kf[0][1]), "+v"(kf[0][2]), "+v"(kf[0][3]), "+v"(kf[1][0]), "+v"(kf[1][1]), "+v"(kf[1][2]), "+v"(kf[1][3]),
                 "+v"(kf[2][0]), "+v"(kf[2][1]), "+v"(kf[2][2]), "+v"(kf[2][3]), "+v"(kf[3][0]), "+v"(kf[3][1]), "+v"(kf[3][2]), "+v"(kf[3][3]),
                 "+v"(q[0]), "+v"(q[1]), "+v"(q[2]), "+v"(q[3]));
    BARRIER();
    int left = NI;
    asm volatile("" : "+s"(left));
#pragma unroll 1
    do {
      f32x4 s[4], x[4];
      u32x4 p[2];
      u32x2 vf[8][2];
      const f32x4 zero = {0.f, 0.f, 0.f, 0.f};
      sb = (sb + SLOT_BYTES) & (RING_BYTES - 1);
      unsigned vx[4];
#define QK1(sub, st) do { s[sub] = mfma(kf[sub][st], q[st], (st) ? s[sub] : zero); kf[sub][st] = *(const LAS u32x4*)(lds + kx[st] + (sub) * 4096); } while (0)
#define FM(sub) x[sub] = s[sub] * C2 + nm
#define EX(sub, r) x[sub][r] = __builtin_amdgcn_exp2f(x[sub][r])
#define CV(sub, h) p[(sub) >> 1][2 * ((sub) & 1) + (h)] = pk(x[sub][2 * (h)], x[sub][2 * (h) + 1])
#define VX(i) do { vx[i] = (i) ? vx[0] ^ ((i) << 5) : va0 + sb; asm volatile("" : "+v"(vx[i])); } while (0)
#define KX(i) do { kx[i] = (i) ? kx[0] ^ ((i) << 6) : ka0 + sb; asm volatile("" : "+v"(kx[i])); } while (0)
#define VRD(i, off) do { vf[i][0] = trd(lds + vx[(i) & 3] + ((i) >> 2) * 4096 + (off)); vf[i][1] = trd(lds + vx[(i) & 3] + ((i) >> 2) * 4096 + 2048 + (off)); } while (0)
#define PV1(i, ob) o[(ob) + ((i) & 3)] = mfma(u32x4{vf[i][0][0], vf[i][0][1], vf[i][1][0], vf[i][1][1]}, p[(i) >> 2], o[(ob) + ((i) & 3)])
      QK1(0, 0); SB(); QK1(1, 0); VX(0); SB(); QK1(0, 1); VX(1); SB(); QK1(1, 1); VX(2); SB();
      QK1(0, 2); VX(3); SB(); QK1(1, 2); SB(); QK1(0, 3); SB(); QK1(1, 3); SB();
      QK1(2, 0); SB(); QK1(3, 0); FM(0); SB();
      QK1(2, 1); FM(1); EX(0, 0); SB(); QK1(3, 1); EX(0, 1); EX(0, 2); SB();
      QK1(2, 2); EX(0, 3); EX(1, 0); SB(); QK1(3, 2); EX(1, 1); EX(1, 2); SB();
      QK1(2, 3); EX(1, 3); CV(0, 0); SB(); QK1(3, 3); CV(0, 1); CV(1, 0); CV(1, 1); SB();
      unsigned any = (p[0][0] | p[0][1] | p[0][2]) | p[0][3];
      asm volatile("" : "+v"(any) :: "memory");
      SB();
      FM(2); FM(3); SB();
      EX(2, 0); EX(2, 1); EX(2, 2); EX(2, 3); EX(3, 0); EX(3, 1); EX(3, 2); EX(3, 3); SB();
      CV(2, 0); CV(2, 1); CV(3, 0); CV(3, 1);
      any = (any | p[1][0] | p[1][1]) | p[1][2] | p[1][3];
      if (__builtin_expect(__builtin_amdgcn_ballot_w64((any & GE2) != 0) != 0, 0)) {
        float t = s[0][0];
#pragma unroll
        for (int sub = 0; sub < 4; ++sub)
#pragma unroll
          for (int r = 0; r < 4; ++r) t = fmaxf(t, s[sub][r]);
        const float nn = fminf(nm, -fmaf(xmax4(t), C2, SLACK));
        const float alpha = __builtin_amdgcn_exp2f(nn - nm);
        nm = nn;
#pragma unroll
        for (int db = 0; db < 8; ++db) o[db] *= alpha;
        lsum *= alpha;
#pragma unroll
        for (int sub = 0; sub < 4; ++sub) { FM(sub); EX(sub, 0); EX(sub, 1); EX(sub, 2); EX(sub, 3); CV(sub, 0); CV(sub, 1); }
      }
      BARRIER();
      VRD(0, 0); SB(); VRD(1, 0); SB(); VRD(2, 0); SB(); VRD(3, 0); SB(); VRD(4, 0); SB(); VRD(5, 0); SB();
      lsum = mfma(ones, p[0], lsum);
      lsum = mfma(ones, p[1], lsum);
      SB();
      PV1(0, 0); SB(); VRD(6, 0); SB(); PV1(1, 0); SB(); VRD(7, 0); SB(); PV1(2, 0); SB(); PV1(3, 0); KX(0); SB();
      PV1(4, 0); KX(1); SB(); PV1(5, 0); KX(2); SB(); PV1(6, 0); KX(3); SB(); PV1(7, 0); SB();
      if (__builtin_expect(left > NB, 0)) {
        VRD(0, VH_BYTES); SB(); VRD(1, VH_BYTES); SB(); VRD(2, VH_BYTES); SB(); VRD(3, VH_BYTES); SB(); VRD(4, VH_BYTES); SB();
        PV1(0, 4); SB(); VRD(5, VH_BYTES); SB(); PV1(1, 4); SB(); VRD(6, VH_BYTES); SB(); PV1(2, 4); SB(); VRD(7, VH_BYTES); SB();
        PV1(3, 4); PV1(4, 4); PV1(5, 4); PV1(6, 4); PV1(7, 4); SB();
        if (left == NB + 1) {
          *(LAS f32x4*)(lds + xi + 2048) = f32x4{nm, 0.f, lsum[0], 0.f};
#pragma unroll
          for (int i = 0; i < 2; ++i) {
            const f32x4 a = o[4 + 2 * i], b = o[5 + 2 * i];
            *(LAS u32x4*)(lds + xi + i * 1024) = u32x4{pk(a[0], a[1]), pk(a[2], a[3]), pk(b[0], b[1]), pk(b[2], b[3])};
          }
        }
      }
    } while (--left != 0);
    {
      asm volatile("" : "+v"(xi));
      const unsigned ln = (xi >> 4) & 63, xb = Q_OFF + head * XCH_BYTES;
      const unsigned xw = xb + (ln & 15) * 144 + (ln >> 4) * 8, xr = xb + (ln >> 3) * 144 + (ln & 7) * 16;
      const int oo = (ln >> 3) * 256 + half * 128 + (ln & 7) * 16;
      const f32x4 st = *(LAS f32x4*)(lds + (xi & ~0x300u) + 2048);
      const u32x4 yy[2] = {*(LAS u32x4*)(lds + xi), *(LAS u32x4*)(lds + xi + 1024)};
      SB();
      const float nn = fminf(nm, st[0]);
      const float b1 = __builtin_amdgcn_exp2f(nn - nm), b2 = __builtin_amdgcn_exp2f(nn - st[0]);
      const float inv = __builtin_amdgcn_rcpf(fmaf(b1, lsum[0], b2 * st[2]));
      const float w1 = b1 * inv, w2 = b2 * inv;
#pragma unroll
      for (int i = 0; i < 4; ++i) {
        const unsigned y0 = yy[i >> 1][2 * (i & 1)], y1 = yy[i >> 1][2 * (i & 1) + 1];
        const f32x4 y = {__uint_as_float(y0 << 16), __uint_as_float(y0 & 0xFFFF0000u), __uint_as_float(y1 << 16), __uint_as_float(y1 & 0xFFFF0000u)};
        const f32x4 r = o[i] * w1 + y * w2;
        *(LAS u32x2*)(lds + xw + i * 32) = u32x2{pk(r[0], r[1]), pk(r[2], r[3])};
      }
      asm volatile("" ::: "memory");
      const u32x4 r0 = *(LAS u32x4*)(lds + xr), r1 = *(LAS u32x4*)(lds + xr + 8 * 144);
      const __amdgpu_buffer_rsrc_t ro = __builtin_amdgcn_make_buffer_rsrc((char*)O + ((size_t)pair * 4 + head) * (SQ * 256), 0, SQ * 256, 0x00020000);
      __builtin_amdgcn_raw_buffer_store_b128(r0, ro, oo, 0, 18);
      __builtin_amdgcn_raw_buffer_store_b128(r1, ro, oo + 2048, 0, 18);
    }
    return;
  }

  const int vo0 = (lane >> 3) * 256 + half * 128 + (((lane & 7) ^ ((lane >> 3) & 6)) << 4);
  const int vo1 = vo0 + 1024, vo2 = vo0 ^ 128, vo3 = vo2 + 1024;
  const __amdgpu_buffer_rsrc_t rk = __builtin_amdgcn_make_buffer_rsrc((char*)K + (size_t)pair * (SK * 256), 0, SK * 256, 0x00020000);
  const __amdgpu_buffer_rsrc_t rq = __builtin_amdgcn_make_buffer_rsrc((char*)Q + (size_t)pair * KT_BYTES, 0, KT_BYTES, 0x00020000);
  const __amdgpu_buffer_rsrc_t rv = __builtin_amdgcn_make_buffer_rsrc((char*)V + (size_t)pair * (SK * 256), 0, SK * 256, 0x00020000);
  const unsigned lbase = (unsigned)(uintptr_t)lds;
#define KTILE(r, so, dst) \
    asm volatile("s_mov_b32 m0, %0\n\ts_nop 0\n\t" \
                 "buffer_load_dwordx4 %1, %3, %4 offen nt lds\n\t" \
                 "buffer_load_dwordx4 %2, %3, %4 offen offset:1024 nt lds\n\t" \
                 "buffer_load_dwordx4 %1, %3, %4 offen offset:2048 nt lds\n\t" \
                 "buffer_load_dwordx4 %2, %3, %4 offen offset:3072 nt lds" \
                 :: "s"(lbase + (dst) + wave * 4096), "v"(ko0), "v"(ko1), "s"(r), "s"((so) + wave * 4096) : "memory")
#define VHALF(a, b, so, dst) \
    asm volatile("s_mov_b32 m0, %0\n\ts_nop 0\n\t" \
                 "buffer_load_dwordx4 %1, %3, %4 offen nt lds\n\t" \
                 "buffer_load_dwordx4 %2, %3, %4 offen offset:1024 nt lds" \
                 :: "s"(lbase + (dst) + wave * 2048), "v"(a), "v"(b), "s"(rv), "s"((so) + wave * 4096) : "memory")
#define SLOT(s) (((s) & 3) * SLOT_BYTES)
#define TOFF(u) ((((((u) >> 2) & 1) | (((u) & 3) << 1) | (((u) >> 3) << 3)) ^ ((pair & 3) << 3)) * KT_BYTES)
#define OFF1(t) TOFF(2 * (t) + half)
#define OFF2(t) TOFF(NA + (t))
#define K1(t) KTILE(rk, OFF1(t), SLOT(t))
#define K2(t) KTILE(rk, OFF2(t), SLOT(t))
#define V1(t) do { VHALF(vo0, vo1, OFF1(t), SLOT((t) + 2) + KT_BYTES); VHALF(vo2, vo3, OFF1(t), SLOT((t) + 2) + KT_BYTES + VH_BYTES); } while (0)
#define V2(t) VHALF(vo0, vo1, OFF2(t), SLOT((t) + 2) + KT_BYTES)
#define PACE_WAIT(d) do { while ((int)((unsigned)__builtin_amdgcn_s_memrealtime() - due) < 0) __builtin_amdgcn_s_sleep(1); due += (d); } while (0)
  KTILE(rk, kso - wave * 4096, SLOT(0)); KTILE(rq, 0, Q_OFF);
  unsigned due = (unsigned)t64 + (PACE0 - 5 * (PACE / 2)) - (__builtin_amdgcn_s_getreg(20 | (3 << 11)) & 1) * ODD;
  PACE_WAIT(PACE / 2); K1(1);
  PACE_WAIT(PACE / 2); V1(0);
  wait_vm<8>(); LBARRIER();
  PACE_WAIT(PACE / 2); K1(2);
  PACE_WAIT(PACE / 2); V1(1);
  wait_vm<12>(); LBARRIER(); PACE_WAIT(PACE / 2); K1(3);
  wait_vm<12>(); PACE_WAIT(PACE / 2); V1(2); wait_vm<12>(); PACE_WAIT(PACE / 2); K1(4); LBARRIER();
#pragma unroll 1
  for (int s = 3; s < NA - 2; ++s) {
    wait_vm<12>(); PACE_WAIT(PACE / 2); V1(s);
    wait_vm<12>(); LBARRIER(); PACE_WAIT(PACE / 2); K1(s + 2);
  }
  wait_vm<12>(); PACE_WAIT(PACE / 2); V1(NA - 2); wait_vm<12>(); LBARRIER(); PACE_WAIT(PACE / 4); K2(NA);
  wait_vm<8>(); LBARRIER(); PACE_WAIT(0); V1(NA - 1); K2(NA + 1);
  wait_vm<12>(); V2(NA); wait_vm<10>(); K2(NA + 2); LBARRIER();
#pragma unroll 1
  for (int s = NA + 1; s < NI - 2; ++s) {
    wait_vm<10>(); V2(s);
    wait_vm<8>(); K2(s + 2); LBARRIER();
  }
  V2(NI - 2);
  wait_vm<8>(); LBARRIER();
  V2(NI - 1);
  wait_vm<4>(); LBARRIER();
  wait_vm<2>(); LBARRIER();
  wait_vm<0>(); LBARRIER();
}

}

extern "C" void kernel_launch(void* const* d_in, const int* in_sizes, int n_in, void* d_out, int out_size, void* d_ws, size_t ws_size,
                              hipStream_t stream) {
  gqa_attn<<<2 * NPAIR, NTHREADS, 0, stream>>>((const __hip_bfloat16*)d_in[0], (const __hip_bfloat16*)d_in[1], (const __hip_bfloat16*)d_in[2],
                                                (__hip_bfloat16*)d_out, (unsigned char*)d_ws);
}
```

```cpp
#define LATEB 1
#define PACE0 362
#define COLD_ADD 0
#include <hip/hip_runtime.h>
#include <hip/hip_bf16.h>
#include <stdint.h>

#define PACE 108

#define ODD 40

#define COLD_TH 60
namespace {

constexpr int B_ = 16, HKV = 8, SQ = 16, SK = 2048;
constexpr int TK = 64, NT = SK / TK;
constexpr int NA = 12, NB = NT - 2 * NA, NI = NA + NB;
constexpr int NPAIR = B_ * HKV;
constexpr int KT_BYTES = TK * 256, VH_BYTES = TK * 128;
constexpr int SLOT_BYTES = KT_BYTES + 2 * VH_BYTES;
constexpr int RING_BYTES = 4 * SLOT_BYTES;
constexpr int Q_OFF = RING_BYTES;
constexpr int XCH_BYTES = 4096;
constexpr int LDS_BYTES = RING_BYTES + 4 * XCH_BYTES;
constexpr int NTHREADS = 576;
constexpr float C2 = 0.08838834764831845f * 1.4426950408889634f;
constexpr float SLACK = 7.0f;
constexpr unsigned GE2 = 0x40004000u;
constexpr int FLAG_OFF = 8192, EPOCH_OFF = FLAG_OFF + 1024, BOX_BYTES = EPOCH_OFF + 256;
constexpr int CODE_LINES = 44;
static_assert(NB == 8, "the mail wave's barrier count");

typedef __attribute__((ext_vector_type(8))) __bf16 bf16x8;
typedef __attribute__((ext_vector_type(4))) __bf16 bf16x4;
typedef __attribute__((ext_vector_type(2))) __bf16 bf16x2;
typedef __attribute__((ext_vector_type(4))) float f32x4;
typedef __attribute__((ext_vector_type(2))) float f32x2;
typedef __attribute__((ext_vector_type(4))) unsigned u32x4;
typedef __attribute__((ext_vector_type(2))) unsigned u32x2;
#define LAS __attribute__((address_space(3)))

#define BARRIER() asm volatile("s_waitcnt lgkmcnt(0)\n\ts_barrier" ::: "memory")
#define LBARRIER() asm volatile("s_barrier" ::: "memory")
#define SB() __builtin_amdgcn_sched_barrier(0)

template <int N> __device__ __forceinline__ void wait_vm() { asm volatile("s_waitcnt vmcnt(%0)" ::"n"(N) : "memory"); }

__device__ __forceinline__ float xmax4(float x) {
  auto s = __builtin_amdgcn_permlane16_swap(__float_as_uint(x), __float_as_uint(x), false, false);
  x = fmaxf(__uint_as_float(s[0]), __uint_as_float(s[1]));
  auto t = __builtin_amdgcn_permlane32_swap(__float_as_uint(x), __float_as_uint(x), false, false);
  return fmaxf(__uint_as_float(t[0]), __uint_as_float(t[1]));
}
__device__ __forceinline__ unsigned pk(float a, float b) { f32x2 v = {a, b}; return __builtin_bit_cast(unsigned, __builtin_convertvector(v, bf16x2)); }
__device__ __forceinline__ f32x4 mfma(u32x4 a, u32x4 b, f32x4 c) {
  return __builtin_amdgcn_mfma_f32_16x16x32_bf16(__builtin_bit_cast(bf16x8, a), __builtin_bit_cast(bf16x8, b), c, 0, 0, 0);
}
__device__ __forceinline__ u32x2 trd(LAS char* p) { return __builtin_bit_cast(u32x2, __builtin_amdgcn_ds_read_tr16_b64_v4bf16((LAS bf16x4*)p)); }

__global__ void __launch_bounds__(NTHREADS, 3)
gqa_attn(const __hip_bfloat16* __restrict__ Q, const __hip_bfloat16* __restrict__ K, const __hip_bfloat16* __restrict__ V,
         __hip_bfloat16* __restrict__ O, unsigned char* __restrict__ ws) {
  __shared__ __attribute__((aligned(1024))) char smem[LDS_BYTES];
  LAS char* lds = (LAS char*)smem;
  const unsigned long long t64 = __builtin_amdgcn_s_memrealtime();
  SB();
  const int lane = threadIdx.x & 63;
  const int wave = __builtin_amdgcn_readfirstlane(threadIdx.x >> 6);
  const int pair = ((blockIdx.x >> 3) & 7) | ((blockIdx.x & 7) << 3) | (blockIdx.x & 64), half = (blockIdx.x >> 7) & 1;
  const int g = lane >> 4, c = lane & 15;
  const int ko0 = g * 256 + ((c ^ (g << 1)) << 4), ko1 = ko0 ^ 128;
  const int kso = ((half << 1) | ((pair & 3) << 3)) * KT_BYTES + wave * 4096;
  asm volatile("s_cmp_lg_u32 %0, 8\n\ts_cbranch_scc1 1f\n\t"
               "s_load_dwordx2 s[42:43], %2, 0x10\n\t"
               "s_getpc_b64 s[40:41]\n\tglobal_load_dword v54, %1, s[40:41]\n\t"
               "v_mov_b32 v55, %3\n\ts_waitcnt lgkmcnt(0)\n\tglobal_load_dword v55, v55, s[42:43] nt\n1:"
               :: "s"(wave), "v"(min(lane, CODE_LINES) * 128), "s"(__builtin_amdgcn_kernarg_segment_ptr()), "s"(pair << 19)
               : "memory", "scc", "s40", "s41", "s42", "s43", "v54", "v55");
  asm volatile("" :: "s"(K), "s"(Q), "s"(V), "s"(t64), "s"(kso), "s"(pair << 19), "v"(ko0), "v"(ko1));

  if (__builtin_expect(wave >= 4, 0)) {
    if (wave == 8) {
      const __amdgpu_buffer_rsrc_t rp = __builtin_amdgcn_make_buffer_rsrc(ws + (size_t)pair * (2 * BOX_BYTES), 0, 2 * BOX_BYTES, 0x00020000);
      const int bo = half * BOX_BYTES, bi = (half ^ 1) * BOX_BYTES;
      const unsigned xb = (unsigned)(uintptr_t)lds + Q_OFF;
      int spins = 1 << 20;
      unsigned long long tmp64;
#define BAR "s_barrier\n\t"
#define FLAG_LOAD "buffer_load_dwordx4 v[48:51], %[po], %[rp], %[fi] offen sc1\n\t"
#define FLAG_TEST "v_cmp_ne_u32 vcc, v53, v49\n\tv_cmp_ne_u32 %[t], v53, v51\n\ts_nop 3\n\ts_or_b64 vcc, vcc, %[t]\n\t"
#define PART_IO(op, s0, s1) \
      op " v[16:19], %[po], %[rp], " s0 " offen sc1\n\t" op " v[20:23], %[po], %[rp], " s0 " offen offset:1024 sc1\n\t" \
      op " v[24:27], %[po], %[rp], " s0 " offen offset:2048 sc1\n\t" op " v[28:31], %[po], %[rp], " s0 " offen offset:3072 sc1\n\t" \
      op " v[32:35], %[po], %[rp], " s1 " offen sc1\n\t" op " v[36:39], %[po], %[rp], " s1 " offen offset:1024 sc1\n\t" \
      op " v[40:43], %[po], %[rp], " s1 " offen offset:2048 sc1\n\t" op " v[44:47], %[po], %[rp], " s1 " offen offset:3072 sc1\n\t"
      asm volatile(
          BAR BAR BAR BAR BAR BAR
          "buffer_load_dword v52, off, %[rp], %[fo] offset:1024 sc1\n\t"
          "buffer_load_dword v53, off, %[rp], %[fi] offset:1024 sc1\n\t"
          BAR BAR BAR BAR BAR BAR BAR BAR
          "s_waitcnt vmcnt(0)\n\t"
          "v_add_u32 v52, 0x9e3779b1, v52\n\tv_add_u32 v53, 0x9e3779b1, v53\n\t"
          BAR
          "ds_read_b128 v[16:19], %[xi]\n\tds_read_b128 v[20:23], %[xi] offset:1024\n\t"
          "ds_read_b128 v[24:27], %[xi] offset:4096\n\tds_read_b128 v[28:31], %[xi] offset:5120\n\t"
          "ds_read_b128 v[32:35], %[xi] offset:8192\n\tds_read_b128 v[36:39], %[xi] offset:9216\n\t"
          "ds_read_b128 v[40:43], %[xi] offset:12288\n\tds_read_b128 v[44:47], %[xi] offset:13312\n\t"
          "ds_read_b128 v[48:51], %[xs]\n\t"
          "s_waitcnt lgkmcnt(0)\n\t"
          PART_IO("buffer_store_dwordx4", "%[bo]", "%[bo2]")
          "v_mov_b32 v49, v52\n\tv_mov_b32 v51, v52\n\t"
          BAR
          "s_waitcnt vmcnt(0)\n\t"
          "buffer_store_dwordx4 v[48:51], %[po], %[rp], %[fo] offen sc1\n\t"
          BAR BAR BAR
          FLAG_LOAD
          BAR
          "s_waitcnt vmcnt(0)\n\t"
          FLAG_TEST
          "s_cbranch_vccnz 1f\n\t"
          PART_IO("buffer_load_dwordx4", "%[bi]", "%[bi2]")
          BAR
          "3:\n\t"
          "ds_write_b128 %[xs], v[48:51]\n\t"
          "s_waitcnt vmcnt(0)\n\t"
          "ds_write_b128 %[xi], v[16:19]\n\tds_write_b128 %[xi], v[20:23] offset:1024\n\t"
          "ds_write_b128 %[xi], v[24:27] offset:4096\n\tds_write_b128 %[xi], v[28:31] offset:5120\n\t"
          "ds_write_b128 %[xi], v[32:35] offset:8192\n\tds_write_b128 %[xi], v[36:39] offset:9216\n\t"
          "ds_write_b128 %[xi], v[40:43] offset:12288\n\tds_write_b128 %[xi], v[44:47] offset:13312\n\t"
          "s_waitcnt lgkmcnt(0)\n\t"
          BAR
          "v_xor_b32 v53, v53, %[po]\n\t"
          "buffer_store_dword v53, %[eo], %[rp], %[bi] offen sc1\n\t"
          "s_branch 4f\n"
          "1:\n\t"
          FLAG_LOAD
          BAR
          "2:\n\t"
          "s_waitcnt vmcnt(0)\n\t"
          FLAG_TEST
          "s_cbranch_vccz 5f\n\t"
          "s_sleep 4\n\t"
          FLAG_LOAD
          "s_sub_u32 %[spins], %[spins], 1\n\t"
          "s_cbranch_scc0 2b\n\t"
          "s_waitcnt vmcnt(0)\n\t"
          "v_mov_b32 v50, 0x7fc00000\n"
          "5:\n\t"
          PART_IO("buffer_load_dwordx4", "%[bi]", "%[bi2]")
          "s_branch 3b\n"
          "4:"
          : [spins] "+s"(spins), [t] "=&s"(tmp64)
          : [bi] "s"(bi), [bi2] "s"(bi + 4096), [fi] "s"(bi + FLAG_OFF), [bo] "s"(bo), [bo2] "s"(bo + 4096), [fo] "s"(bo + FLAG_OFF),
            [po] "v"(lane * 16), [eo] "v"(lane == 0 ? EPOCH_OFF : lane == 1 ? FLAG_OFF + 4 : 2 * BOX_BYTES),
            [xi] "v"(xb + lane * 16), [xs] "v"(xb + g * XCH_BYTES + 2048 + c * 16), [rp] "s"(rp)
          : "memory", "vcc", "scc", "v16", "v17", "v18", "v19", "v20", "v21", "v22", "v23", "v24", "v25", "v26", "v27", "v28", "v29", "v30", "v31",
            "v32", "v33", "v34", "v35", "v36", "v37", "v38", "v39", "v40", "v41", "v42", "v43", "v44", "v45", "v46", "v47", "v48", "v49", "v50", "v51",
            "v52", "v53", "v54", "v55");
      return;
    }

    const int head = wave - 4;
    const unsigned ka0 = c * 256 + ((g ^ ((c & 7) << 1)) << 4);
    const int vr = 4 * g + (c >> 2);
    const unsigned va0 = KT_BYTES + vr * 128 + (((vr >> 1) & 3) << 5) + (c & 3) * 8;
    unsigned xi = Q_OFF + head * XCH_BYTES + lane * 16;
    u32x4 ones = {0x3F803F80u, 0x3F803F80u, 0x3F803F80u, 0x3F803F80u};
    asm volatile("" : "+v"(ones), "+v"(xi));
    f32x4 o[8], lsum = {0.f, 0.f, 0.f, 0.f};
#pragma unroll
    for (int db = 0; db < 8; ++db) { o[db] = f32x4{0.f, 0.f, 0.f, 0.f}; asm volatile("" : "+v"(o[db])); }
    float nm = 3.0e38f;
    unsigned sb = SLOT_BYTES;
    unsigned kx[4];
#pragma unroll
    for (int st = 0; st < 4; ++st) kx[st] = (ka0 ^ (st << 6)) + sb;
    BARRIER();
    u32x4 q[4], kf[4][4];
#pragma unroll
    for (int st = 0; st < 4; ++st) q[st] = *(const LAS u32x4*)(lds + (Q_OFF + head * 4096) + (ka0 ^ (st << 6)));
#pragma unroll
    for (int sub = 0; sub < 4; ++sub)
#pragma unroll
      for (int st = 0; st < 4; ++st) kf[sub][st] = *(const LAS u32x4*)(lds + (ka0 ^ (st << 6)) + sub * 4096);
    asm volatile("" : "+v"(kf[0][0]), "+v"(kf[0][1]), "+v"(kf[0][2]), "+v"(kf[0][3]), "+v"(kf[1][0]), "+v"(kf[1][1]), "+v"(kf[1][2]), "+v"(kf[1][3]),
                 "+v"(kf[2][0]), "+v"(kf[2][1]), "+v"(kf[2][2]), "+v"(kf[2][3]), "+v"(kf[3][0]), "+v"(kf[3][1]), "+v"(kf[3][2]), "+v"(kf[3][3]),
                 "+v"(q[0]), "+v"(q[1]), "+v"(q[2]), "+v"(q[3]));
    BARRIER();
    int left = NI;
    asm volatile("" : "+s"(left));
#pragma unroll 1
    do {
      f32x4 s[4], x[4];
      u32x4 p[2];
      u32x2 vf[8][2];
      const f32x4 zero = {0.f, 0.f, 0.f, 0.f};
      sb = (sb + SLOT_BYTES) & (RING_BYTES - 1);
      unsigned vx[4];
#define QK1(sub, st) do { s[sub] = mfma(kf[sub][st], q[st], (st) ? s[sub] : zero); kf[sub][st] = *(const LAS u32x4*)(lds + kx[st] + (sub) * 4096); } while (0)
#define FM(sub) x[sub] = s[sub] * C2 + nm
#define EX(sub, r) x[sub][r] = __builtin_amdgcn_exp2f(x[sub][r])
#define CV(sub, h) p[(sub) >> 1][2 * ((sub) & 1) + (h)] = pk(x[sub][2 * (h)], x[sub][2 * (h) + 1])
#define VX(i) do { vx[i] = (i) ? vx[0] ^ ((i) << 5) : va0 + sb; asm volatile("" : "+v"(vx[i])); } while (0)
#define KX(i) do { kx[i] = (i) ? kx[0] ^ ((i) << 6) : ka0 + sb; asm volatile("" : "+v"(kx[i])); } while (0)
#define VRD(i, off) do { vf[i][0] = trd(lds + vx[(i) & 3] + ((i) >> 2) * 4096 + (off)); vf[i][1] = trd(lds + vx[(i) & 3] + ((i) >> 2) * 4096 + 2048 + (off)); } while (0)
#define PV1(i, ob) o[(ob) + ((i) & 3)] = mfma(u32x4{vf[i][0][0], vf[i][0][1], vf[i][1][0], vf[i][1][1]}, p[(i) >> 2], o[(ob) + ((i) & 3)])
      QK1(0, 0); SB(); QK1(1, 0); VX(0); SB(); QK1(0, 1); VX(1); SB(); QK1(1, 1); VX(2); SB();
      QK1(0, 2); VX(3); SB(); QK1(1, 2); SB(); QK1(0, 3); SB(); QK1(1, 3); SB();
      QK1(2, 0); SB(); QK1(3, 0); FM(0); SB();
      QK1(2, 1); FM(1); EX(0, 0); SB(); QK1(3, 1); EX(0, 1); EX(0, 2); SB();
      QK1(2, 2); EX(0, 3); EX(1, 0); SB(); QK1(3, 2); EX(1, 1); EX(1, 2); SB();
      QK1(2, 3); EX(1, 3); CV(0, 0); SB(); QK1(3, 3); CV(0, 1); CV(1, 0); CV(1, 1); SB();
      unsigned any = (p[0][0] | p[0][1] | p[0][2]) | p[0][3];
      asm volatile("" : "+v"(any) :: "memory");
      SB();
      FM(2); FM(3); SB();
      EX(2, 0); EX(2, 1); EX(2, 2); EX(2, 3); EX(3, 0); EX(3, 1); EX(3, 2); EX(3, 3); SB();
      CV(2, 0); CV(2, 1); CV(3, 0); CV(3, 1);
      any = (any | p[1][0] | p[1][1]) | p[1][2] | p[1][3];
      if (__builtin_expect(__builtin_amdgcn_ballot_w64((any & GE2) != 0) != 0, 0)) {
        float t = s[0][0];
#pragma unroll
        for (int sub = 0; sub < 4; ++sub)
#pragma unroll
          for (int r = 0; r < 4; ++r) t = fmaxf(t, s[sub][r]);
        const float nn = fminf(nm, -fmaf(xmax4(t), C2, SLACK));
        const float alpha = __builtin_amdgcn_exp2f(nn - nm);
        nm = nn;
#pragma unroll
        for (int db = 0; db < 8; ++db) o[db] *= alpha;
        lsum *= alpha;
#pragma unroll
        for (int sub = 0; sub < 4; ++sub) { FM(sub); EX(sub, 0); EX(sub, 1); EX(sub, 2); EX(sub, 3); CV(sub, 0); CV(sub, 1); }
      }
      BARRIER();
      VRD(0, 0); SB(); VRD(1, 0); SB(); VRD(2, 0); SB(); VRD(3, 0); SB(); VRD(4, 0); SB(); VRD(5, 0); SB();
      lsum = mfma(ones, p[0], lsum);
      lsum = mfma(ones, p[1], lsum);
      SB();
      PV1(0, 0); SB(); VRD(6, 0); SB(); PV1(1, 0); SB(); VRD(7, 0); SB(); PV1(2, 0); SB(); PV1(3, 0); KX(0); SB();
      PV1(4, 0); KX(1); SB(); PV1(5, 0); KX(2); SB(); PV1(6, 0); KX(3); SB(); PV1(7, 0); SB();
      if (__builtin_expect(left > NB, 0)) {
        VRD(0, VH_BYTES); SB(); VRD(1, VH_BYTES); SB(); VRD(2, VH_BYTES); SB(); VRD(3, VH_BYTES); SB(); VRD(4, VH_BYTES); SB();
        PV1(0, 4); SB(); VRD(5, VH_BYTES); SB(); PV1(1, 4); SB(); VRD(6, VH_BYTES); SB(); PV1(2, 4); SB(); VRD(7, VH_BYTES); SB();
        PV1(3, 4); PV1(4, 4); PV1(5, 4); PV1(6, 4); PV1(7, 4); SB();
        if (left == NB + 1) {
          *(LAS f32x4*)(lds + xi + 2048) = f32x4{nm, 0.f, lsum[0], 0.f};
#pragma unroll
          for (int i = 0; i < 2; ++i) {
            const f32x4 a = o[4 + 2 * i], b = o[5 + 2 * i];
            *(LAS u32x4*)(lds + xi + i * 1024) = u32x4{pk(a[0], a[1]), pk(a[2], a[3]), pk(b[0], b[1]), pk(b[2], b[3])};
          }
        }
      }
    } while (--left != 0);
    {
      asm volatile("" : "+v"(xi));
      const unsigned ln = (xi >> 4) & 63, xb = Q_OFF + head * XCH_BYTES;
      const unsigned xw = xb + (ln & 15) * 144 + (ln >> 4) * 8, xr = xb + (ln >> 3) * 144 + (ln & 7) * 16;
      const int oo = (ln >> 3) * 256 + half * 128 + (ln & 7) * 16;
      const f32x4 st = *(LAS f32x4*)(lds + (xi & ~0x300u) + 2048);
      const u32x4 yy[2] = {*(LAS u32x4*)(lds + xi), *(LAS u32x4*)(lds + xi + 1024)};
      SB();
      const float nn = fminf(nm, st[0]);
      const float b1 = __builtin_amdgcn_exp2f(nn - nm), b2 = __builtin_amdgcn_exp2f(nn - st[0]);
      const float inv = __builtin_amdgcn_rcpf(fmaf(b1, lsum[0], b2 * st[2]));
      const float w1 = b1 * inv, w2 = b2 * inv;
#pragma unroll
      for (int i = 0; i < 4; ++i) {
        const unsigned y0 = yy[i >> 1][2 * (i & 1)], y1 = yy[i >> 1][2 * (i & 1) + 1];
        const f32x4 y = {__uint_as_float(y0 << 16), __uint_as_float(y0 & 0xFFFF0000u), __uint_as_float(y1 << 16), __uint_as_float(y1 & 0xFFFF0000u)};
        const f32x4 r = o[i] * w1 + y * w2;
        *(LAS u32x2*)(lds + xw + i * 32) = u32x2{pk(r[0], r[1]), pk(r[2], r[3])};
      }
      asm volatile("" ::: "memory");
      const u32x4 r0 = *(LAS u32x4*)(lds + xr), r1 = *(LAS u32x4*)(lds + xr + 8 * 144);
      const __amdgpu_buffer_rsrc_t ro = __builtin_amdgcn_make_buffer_rsrc((char*)O + ((size_t)pair * 4 + head) * (SQ * 256), 0, SQ * 256, 0x00020000);
      __builtin_amdgcn_raw_buffer_store_b128(r0, ro, oo, 0, 18);
      __builtin_amdgcn_raw_buffer_store_b128(r1, ro, oo + 2048, 0, 18);
    }
    return;
  }

  const int vo0 = (lane >> 3) * 256 + half * 128 + (((lane & 7) ^ ((lane >> 3) & 6)) << 4);
  const int vo1 = vo0 + 1024, vo2 = vo0 ^ 128, vo3 = vo2 + 1024;
  const __amdgpu_buffer_rsrc_t rk = __builtin_amdgcn_make_buffer_rsrc((char*)K + (size_t)pair * (SK * 256), 0, SK * 256, 0x00020000);
  const __amdgpu_buffer_rsrc_t rq = __builtin_amdgcn_make_buffer_rsrc((char*)Q + (size_t)pair * KT_BYTES, 0, KT_BYTES, 0x00020000);
  const __amdgpu_buffer_rsrc_t rv = __builtin_amdgcn_make_buffer_rsrc((char*)V + (size_t)pair * (SK * 256), 0, SK * 256, 0x00020000);
  const unsigned lbase = (unsigned)(uintptr_t)lds;
#define KTILE(r, so, dst) \
    asm volatile("s_mov_b32 m0, %0\n\ts_nop 0\n\t" \
                 "buffer_load_dwordx4 %1, %3, %4 offen nt lds\n\t" \
                 "buffer_load_dwordx4 %2, %3, %4 offen offset:1024 nt lds\n\t" \
                 "buffer_load_dwordx4 %1, %3, %4 offen offset:2048 nt lds\n\t" \
                 "buffer_load_dwordx4 %2, %3, %4 offen offset:3072 nt lds" \
                 :: "s"(lbase + (dst) + wave * 4096), "v"(ko0), "v"(ko1), "s"(r), "s"((so) + wave * 4096) : "memory")
#define VHALF(a, b, so, dst) \
    asm volatile("s_mov_b32 m0, %0\n\ts_nop 0\n\t" \
                 "buffer_load_dwordx4 %1, %3, %4 offen nt lds\n\t" \
                 "buffer_load_dwordx4 %2, %3, %4 offen offset:1024 nt lds" \
                 :: "s"(lbase + (dst) + wave * 2048), "v"(a), "v"(b), "s"(rv), "s"((so) + wave * 4096) : "memory")
#define SLOT(s) (((s) & 3) * SLOT_BYTES)
#define TOFF(u) ((((((u) >> 2) & 1) | (((u) & 3) << 1) | (((u) >> 3) << 3)) ^ ((pair & 3) << 3)) * KT_BYTES)
#define OFF1(t) TOFF(2 * (t) + half)
#define OFF2(t) TOFF(NA + (t))
#define K1(t) KTILE(rk, OFF1(t), SLOT(t))
#define K2(t) KTILE(rk, OFF2(t), SLOT(t))
#define V1(t) do { VHALF(vo0, vo1, OFF1(t), SLOT((t) + 2) + KT_BYTES); VHALF(vo2, vo3, OFF1(t), SLOT((t) + 2) + KT_BYTES + VH_BYTES); } while (0)
#define V2(t) VHALF(vo0, vo1, OFF2(t), SLOT((t) + 2) + KT_BYTES)
#define PACE_WAIT(d) do { while ((int)((unsigned)__builtin_amdgcn_s_memrealtime() - due) < 0) __builtin_amdgcn_s_sleep(1); due += (d); } while (0)
  unsigned long long t1;
  asm volatile("s_memrealtime %0" : "=&s"(t1) :: "memory");
  KTILE(rk, kso - wave * 4096, SLOT(0)); KTILE(rq, 0, Q_OFF);
  asm volatile("s_waitcnt lgkmcnt(0)" : "+s"(t1) :: "memory");
  unsigned due = (unsigned)(LATEB ? t1 : t64) + (PACE0 - 5 * (PACE / 2)) - (__builtin_amdgcn_s_getreg(20 | (3 << 11)) & 1) * ODD;
  if ((unsigned)t1 - (unsigned)t64 > COLD_TH) due += COLD_ADD;
  PACE_WAIT(PACE / 2); K1(1);
  PACE_WAIT(PACE / 2); V1(0);
  wait_vm<8>(); LBARRIER();
  PACE_WAIT(PACE / 2); K1(2);
  PACE_WAIT(PACE / 2); V1(1);
  wait_vm<12>(); LBARRIER(); PACE_WAIT(PACE / 2); K1(3);
  wait_vm<12>(); PACE_WAIT(PACE / 2); V1(2); wait_vm<12>(); PACE_WAIT(PACE / 2); K1(4); LBARRIER();
#pragma unroll 1
  for (int s = 3; s < NA - 2; ++s) {
    wait_vm<12>(); PACE_WAIT(PACE / 2); V1(s);
    wait_vm<12>(); LBARRIER(); PACE_WAIT(PACE / 2); K1(s + 2);
  }
  wait_vm<12>(); PACE_WAIT(PACE / 2); V1(NA - 2); wait_vm<12>(); LBARRIER(); PACE_WAIT(PACE / 4); K2(NA);
  wait_vm<8>(); LBARRIER(); PACE_WAIT(0); V1(NA - 1); K2(NA + 1);
  wait_vm<12>(); V2(NA); wait_vm<10>(); K2(NA + 2); LBARRIER();
#pragma unroll 1
  for (int s = NA + 1; s < NI - 2; ++s) {
    wait_vm<10>(); V2(s);
    wait_vm<8>(); K2(s + 2); LBARRIER();
  }
  V2(NI - 2);
  wait_vm<8>(); LBARRIER();
  V2(NI - 1);
  wait_vm<4>(); LBARRIER();
  wait_vm<2>(); LBARRIER();
  wait_vm<0>(); LBARRIER();
}

}

extern "C" void kernel_launch(void* const* d_in, const int* in_sizes, int n_in, void* d_out, int out_size, void* d_ws, size_t ws_size,
                              hipStream_t stream) {
  gqa_attn<<<2 * NPAIR, NTHREADS, 0, stream>>>((const __hip_bfloat16*)d_in[0], (const __hip_bfloat16*)d_in[1], (const __hip_bfloat16*)d_in[2],
                                                (__hip_bfloat16*)d_out, (unsigned char*)d_ws);
}
```

```cpp
#include <hip/hip_runtime.h>
#include <hip/hip_bf16.h>
#include <stdint.h>

#define PACE 108
#define PACE0 364

#define ODD 40

namespace {

constexpr int B_ = 16, HKV = 8, SQ = 16, SK = 2048;
constexpr int TK = 64, NT = SK / TK;
constexpr int NA = 12, NB = NT - 2 * NA, NI = NA + NB;
constexpr int NPAIR = B_ * HKV;
constexpr int KT_BYTES = TK * 256, VH_BYTES = TK * 128;
constexpr int SLOT_BYTES = KT_BYTES + 2 * VH_BYTES;
constexpr int RING_BYTES = 4 * SLOT_BYTES;
constexpr int Q_OFF = RING_BYTES;
constexpr int XCH_BYTES = 4096;
constexpr int LDS_BYTES = RING_BYTES + 4 * XCH_BYTES;
constexpr int NTHREADS = 576;
constexpr float C2 = 0.08838834764831845f * 1.4426950408889634f;
constexpr float SLACK = 7.0f;
constexpr unsigned GE2 = 0x40004000u;
constexpr int FLAG_OFF = 8192, EPOCH_OFF = FLAG_OFF + 1024, BOX_BYTES = EPOCH_OFF + 256;
constexpr int CODE_LINES = 44;
static_assert(NB == 8, "the mail wave's barrier count");

typedef __attribute__((ext_vector_type(8))) __bf16 bf16x8;
typedef __attribute__((ext_vector_type(4))) __bf16 bf16x4;
typedef __attribute__((ext_vector_type(2))) __bf16 bf16x2;
typedef __attribute__((ext_vector_type(4))) float f32x4;
typedef __attribute__((ext_vector_type(2))) float f32x2;
typedef __attribute__((ext_vector_type(4))) unsigned u32x4;
typedef __attribute__((ext_vector_type(2))) unsigned u32x2;
#define LAS __attribute__((address_space(3)))

#define BARRIER() asm volatile("s_waitcnt lgkmcnt(0)\n\ts_barrier" ::: "memory")
#define LBARRIER() asm volatile("s_barrier" ::: "memory")
#define SB() __builtin_amdgcn_sched_barrier(0)

template <int N> __device__ __forceinline__ void wait_vm() { asm volatile("s_waitcnt vmcnt(%0)" ::"n"(N) : "memory"); }

__device__ __forceinline__ float xmax4(float x) {
  auto s = __builtin_amdgcn_permlane16_swap(__float_as_uint(x), __float_as_uint(x), false, false);
  x = fmaxf(__uint_as_float(s[0]), __uint_as_float(s[1]));
  auto t = __builtin_amdgcn_permlane32_swap(__float_as_uint(x), __float_as_uint(x), false, false);
  return fmaxf(__uint_as_float(t[0]), __uint_as_float(t[1]));
}
__device__ __forceinline__ unsigned pk(float a, float b) { f32x2 v = {a, b}; return __builtin_bit_cast(unsigned, __builtin_convertvector(v, bf16x2)); }
__device__ __forceinline__ f32x4 mfma(u32x4 a, u32x4 b, f32x4 c) {
  return __builtin_amdgcn_mfma_f32_16x16x32_bf16(__builtin_bit_cast(bf16x8, a), __builtin_bit_cast(bf16x8, b), c, 0, 0, 0);
}
__device__ __forceinline__ u32x2 trd(LAS char* p) { return __builtin_bit_cast(u32x2, __builtin_amdgcn_ds_read_tr16_b64_v4bf16((LAS bf16x4*)p)); }

__global__ void __launch_bounds__(NTHREADS, 3)
gqa_attn(const __hip_bfloat16* __restrict__ Q, const __hip_bfloat16* __restrict__ K, const __hip_bfloat16* __restrict__ V,
         __hip_bfloat16* __restrict__ O, unsigned char* __restrict__ ws) {
  __shared__ __attribute__((aligned(1024))) char smem[LDS_BYTES];
  LAS char* lds = (LAS char*)smem;
  SB();
  const int lane = threadIdx.x & 63;
  const int wave = __builtin_amdgcn_readfirstlane(threadIdx.x >> 6);
  const int pair = ((blockIdx.x >> 3) & 7) | ((blockIdx.x & 7) << 3) | (blockIdx.x & 64), half = (blockIdx.x >> 7) & 1;
  const int g = lane >> 4, c = lane & 15;
  const int ko0 = g * 256 + ((c ^ (g << 1)) << 4), ko1 = ko0 ^ 128;
  const int kso = ((half << 1) | ((pair & 3) << 3)) * KT_BYTES + wave * 4096;
  asm volatile("s_cmp_lg_u32 %0, 8\n\ts_cbranch_scc1 1f\n\t"
               "s_load_dwordx2 s[42:43], %2, 0x10\n\t"
               "s_getpc_b64 s[40:41]\n\tglobal_load_dword v54, %1, s[40:41]\n\t"
               "v_mov_b32 v55, %3\n\ts_waitcnt lgkmcnt(0)\n\tglobal_load_dword v55, v55, s[42:43] nt\n1:"
               :: "s"(wave), "v"(min(lane, CODE_LINES) * 128), "s"(__builtin_amdgcn_kernarg_segment_ptr()), "s"(pair << 19)
               : "memory", "scc", "s40", "s41", "s42", "s43", "v54", "v55");
  asm volatile("" :: "s"(K), "s"(Q), "s"(V), "s"(kso), "s"(pair << 19), "v"(ko0), "v"(ko1));

  if (__builtin_expect(wave >= 4, 0)) {
    if (wave == 8) {
      const __amdgpu_buffer_rsrc_t rp = __builtin_amdgcn_make_buffer_rsrc(ws + (size_t)pair * (2 * BOX_BYTES), 0, 2 * BOX_BYTES, 0x00020000);
      const int bo = half * BOX_BYTES, bi = (half ^ 1) * BOX_BYTES;
      const unsigned xb = (unsigned)(uintptr_t)lds + Q_OFF;
      int spins = 1 << 20;
      unsigned long long tmp64;
#define BAR "s_barrier\n\t"
#define FLAG_LOAD "buffer_load_dwordx4 v[48:51], %[po], %[rp], %[fi] offen sc1\n\t"
#define FLAG_TEST "v_cmp_ne_u32 vcc, v53, v49\n\tv_cmp_ne_u32 %[t], v53, v51\n\ts_nop 3\n\ts_or_b64 vcc, vcc, %[t]\n\t"
#define PART_IO(op, s0, s1) \
      op " v[16:19], %[po], %[rp], " s0 " offen sc1\n\t" op " v[20:23], %[po], %[rp], " s0 " offen offset:1024 sc1\n\t" \
      op " v[24:27], %[po], %[rp], " s0 " offen offset:2048 sc1\n\t" op " v[28:31], %[po], %[rp], " s0 " offen offset:3072 sc1\n\t" \
      op " v[32:35], %[po], %[rp], " s1 " offen sc1\n\t" op " v[36:39], %[po], %[rp], " s1 " offen offset:1024 sc1\n\t" \
      op " v[40:43], %[po], %[rp], " s1 " offen offset:2048 sc1\n\t" op " v[44:47], %[po], %[rp], " s1 " offen offset:3072 sc1\n\t"
      asm volatile(
          BAR BAR BAR BAR BAR BAR
          "buffer_load_dword v52, off, %[rp], %[fo] offset:1024 sc1\n\t"
          "buffer_load_dword v53, off, %[rp], %[fi] offset:1024 sc1\n\t"
          BAR BAR BAR BAR BAR BAR BAR BAR
          "s_waitcnt vmcnt(0)\n\t"
          "v_add_u32 v52, 0x9e3779b1, v52\n\tv_add_u32 v53, 0x9e3779b1, v53\n\t"
          BAR
          "ds_read_b128 v[16:19], %[xi]\n\tds_read_b128 v[20:23], %[xi] offset:1024\n\t"
          "ds_read_b128 v[24:27], %[xi] offset:4096\n\tds_read_b128 v[28:31], %[xi] offset:5120\n\t"
          "ds_read_b128 v[32:35], %[xi] offset:8192\n\tds_read_b128 v[36:39], %[xi] offset:9216\n\t"
          "ds_read_b128 v[40:43], %[xi] offset:12288\n\tds_read_b128 v[44:47], %[xi] offset:13312\n\t"
          "ds_read_b128 v[48:51], %[xs]\n\t"
          "s_waitcnt lgkmcnt(0)\n\t"
          PART_IO("buffer_store_dwordx4", "%[bo]", "%[bo2]")
          "v_mov_b32 v49, v52\n\tv_mov_b32 v51, v52\n\t"
          BAR
          "s_waitcnt vmcnt(0)\n\t"
          "buffer_store_dwordx4 v[48:51], %[po], %[rp], %[fo] offen sc1\n\t"
          BAR BAR BAR
          FLAG_LOAD
          BAR
          "s_waitcnt vmcnt(0)\n\t"
          FLAG_TEST
          "s_cbranch_vccnz 1f\n\t"
          PART_IO("buffer_load_dwordx4", "%[bi]", "%[bi2]")
          BAR
          "3:\n\t"
          "ds_write_b128 %[xs], v[48:51]\n\t"
          "s_waitcnt vmcnt(0)\n\t"
          "ds_write_b128 %[xi], v[16:19]\n\tds_write_b128 %[xi], v[20:23] offset:1024\n\t"
          "ds_write_b128 %[xi], v[24:27] offset:4096\n\tds_write_b128 %[xi], v[28:31] offset:5120\n\t"
          "ds_write_b128 %[xi], v[32:35] offset:8192\n\tds_write_b128 %[xi], v[36:39] offset:9216\n\t"
          "ds_write_b128 %[xi], v[40:43] offset:12288\n\tds_write_b128 %[xi], v[44:47] offset:13312\n\t"
          "s_waitcnt lgkmcnt(0)\n\t"
          BAR
          "v_xor_b32 v53, v53, %[po]\n\t"
          "buffer_store_dword v53, %[eo], %[rp], %[bi] offen sc1\n\t"
          "s_branch 4f\n"
          "1:\n\t"
          FLAG_LOAD
          BAR
          "2:\n\t"
          "s_waitcnt vmcnt(0)\n\t"
          FLAG_TEST
          "s_cbranch_vccz 5f\n\t"
          "s_sleep 4\n\t"
          FLAG_LOAD
          "s_sub_u32 %[spins], %[spins], 1\n\t"
          "s_cbranch_scc0 2b\n\t"
          "s_waitcnt vmcnt(0)\n\t"
          "v_mov_b32 v50, 0x7fc00000\n"
          "5:\n\t"
          PART_IO("buffer_load_dwordx4", "%[bi]", "%[bi2]")
          "s_branch 3b\n"
          "4:"
          : [spins] "+s"(spins), [t] "=&s"(tmp64)
          : [bi] "s"(bi), [bi2] "s"(bi + 4096), [fi] "s"(bi + FLAG_OFF), [bo] "s"(bo), [bo2] "s"(bo + 4096), [fo] "s"(bo + FLAG_OFF),
            [po] "v"(lane * 16), [eo] "v"(lane == 0 ? EPOCH_OFF : lane == 1 ? FLAG_OFF + 4 : 2 * BOX_BYTES),
            [xi] "v"(xb + lane * 16), [xs] "v"(xb + g * XCH_BYTES + 2048 + c * 16), [rp] "s"(rp)
          : "memory", "vcc", "scc", "v16", "v17", "v18", "v19", "v20", "v21", "v22", "v23", "v24", "v25", "v26", "v27", "v28", "v29", "v30", "v31",
            "v32", "v33", "v34", "v35", "v36", "v37", "v38", "v39", "v40", "v41", "v42", "v43", "v44", "v45", "v46", "v47", "v48", "v49", "v50", "v51",
            "v52", "v53", "v54", "v55");
      return;
    }

    const int head = wave - 4;
    const unsigned ka0 = c * 256 + ((g ^ ((c & 7) << 1)) << 4);
    const int vr = 4 * g + (c >> 2);
    const unsigned va0 = KT_BYTES + vr * 128 + (((vr >> 1) & 3) << 5) + (c & 3) * 8;
    unsigned xi = Q_OFF + head * XCH_BYTES + lane * 16;
    u32x4 ones = {0x3F803F80u, 0x3F803F80u, 0x3F803F80u, 0x3F803F80u};
    asm volatile("" : "+v"(ones), "+v"(xi));
    f32x4 o[8], lsum = {0.f, 0.f, 0.f, 0.f};
#pragma unroll
    for (int db = 0; db < 8; ++db) { o[db] = f32x4{0.f, 0.f, 0.f, 0.f}; asm volatile("" : "+v"(o[db])); }
    float nm = 3.0e38f;
    unsigned sb = SLOT_BYTES;
    unsigned kx[4];
#pragma unroll
    for (int st = 0; st < 4; ++st) kx[st] = (ka0 ^ (st << 6)) + sb;
    BARRIER();
    u32x4 q[4], kf[4][4];
#pragma unroll
    for (int st = 0; st < 4; ++st) q[st] = *(const LAS u32x4*)(lds + (Q_OFF + head * 4096) + (ka0 ^ (st << 6)));
#pragma unroll
    for (int sub = 0; sub < 4; ++sub)
#pragma unroll
      for (int st = 0; st < 4; ++st) kf[sub][st] = *(const LAS u32x4*)(lds + (ka0 ^ (st << 6)) + sub * 4096);
    asm volatile("" : "+v"(kf[0][0]), "+v"(kf[0][1]), "+v"(kf[0][2]), "+v"(kf[0][3]), "+v"(kf[1][0]), "+v"(kf[1][1]), "+v"(kf[1][2]), "+v"(kf[1][3]),
                 "+v"(kf[2][0]), "+v"(kf[2][1]), "+v"(kf[2][2]), "+v"(kf[2][3]), "+v"(kf[3][0]), "+v"(kf[3][1]), "+v"(kf[3][2]), "+v"(kf[3][3]),
                 "+v"(q[0]), "+v"(q[1]), "+v"(q[2]), "+v"(q[3]));
    BARRIER();
    int left = NI;
    asm volatile("" : "+s"(left));
#pragma unroll 1
    do {
      f32x4 s[4], x[4];
      u32x4 p[2];
      u32x2 vf[8][2];
      const f32x4 zero = {0.f, 0.f, 0.f, 0.f};
      sb = (sb + SLOT_BYTES) & (RING_BYTES - 1);
      unsigned vx[4];
#define QK1(sub, st) do { s[sub] = mfma(kf[sub][st], q[st], (st) ? s[sub] : zero); kf[sub][st] = *(const LAS u32x4*)(lds + kx[st] + (sub) * 4096); } while (0)
#define FM(sub) x[sub] = s[sub] * C2 + nm
#define EX(sub, r) x[sub][r] = __builtin_amdgcn_exp2f(x[sub][r])
#define CV(sub, h) p[(sub) >> 1][2 * ((sub) & 1) + (h)] = pk(x[sub][2 * (h)], x[sub][2 * (h) + 1])
#define VX(i) do { vx[i] = (i) ? vx[0] ^ ((i) << 5) : va0 + sb; asm volatile("" : "+v"(vx[i])); } while (0)
#define KX(i) do { kx[i] = (i) ? kx[0] ^ ((i) << 6) : ka0 + sb; asm volatile("" : "+v"(kx[i])); } while (0)
#define VRD(i, off) do { vf[i][0] = trd(lds + vx[(i) & 3] + ((i) >> 2) * 4096 + (off)); vf[i][1] = trd(lds + vx[(i) & 3] + ((i) >> 2) * 4096 + 2048 + (off)); } while (0)
#define PV1(i, ob) o[(ob) + ((i) & 3)] = mfma(u32x4{vf[i][0][0], vf[i][0][1], vf[i][1][0], vf[i][1][1]}, p[(i) >> 2], o[(ob) + ((i) & 3)])
      QK1(0, 0); SB(); QK1(1, 0); VX(0); SB(); QK1(0, 1); VX(1); SB(); QK1(1, 1); VX(2); SB();
      QK1(0, 2); VX(3); SB(); QK1(1, 2); SB(); QK1(0, 3); SB(); QK1(1, 3); SB();
      QK1(2, 0); SB(); QK1(3, 0); FM(0); SB();
      QK1(2, 1); FM(1); EX(0, 0); SB(); QK1(3, 1); EX(0, 1); EX(0, 2); SB();
      QK1(2, 2); EX(0, 3); EX(1, 0); SB(); QK1(3, 2); EX(1, 1); EX(1, 2); SB();
      QK1(2, 3); EX(1, 3); CV(0, 0); SB(); QK1(3, 3); CV(0, 1); CV(1, 0); CV(1, 1); SB();
      unsigned any = (p[0][0] | p[0][1] | p[0][2]) | p[0][3];
      asm volatile("" : "+v"(any) :: "memory");
      SB();
      FM(2); FM(3); SB();
      EX(2, 0); EX(2, 1); EX(2, 2); EX(2, 3); EX(3, 0); EX(3, 1); EX(3, 2); EX(3, 3); SB();
      CV(2, 0); CV(2, 1); CV(3, 0); CV(3, 1);
      any = (any | p[1][0] | p[1][1]) | p[1][2] | p[1][3];
      if (__builtin_expect(__builtin_amdgcn_ballot_w64((any & GE2) != 0) != 0, 0)) {
        float t = s[0][0];
#pragma unroll
        for (int sub = 0; sub < 4; ++sub)
#pragma unroll
          for (int r = 0; r < 4; ++r) t = fmaxf(t, s[sub][r]);
        const float nn = fminf(nm, -fmaf(xmax4(t), C2, SLACK));
        const float alpha = __builtin_amdgcn_exp2f(nn - nm);
        nm = nn;
#pragma unroll
        for (int db = 0; db < 8; ++db) o[db] *= alpha;
        lsum *= alpha;
#pragma unroll
        for (int sub = 0; sub < 4; ++sub) { FM(sub); EX(sub, 0); EX(sub, 1); EX(sub, 2); EX(sub, 3); CV(sub, 0); CV(sub, 1); }
      }
      BARRIER();
      VRD(0, 0); SB(); VRD(1, 0); SB(); VRD(2, 0); SB(); VRD(3, 0); SB(); VRD(4, 0); SB(); VRD(5, 0); SB();
      lsum = mfma(ones, p[0], lsum);
      lsum = mfma(ones, p[1], lsum);
      SB();
      PV1(0, 0); SB(); VRD(6, 0); SB(); PV1(1, 0); SB(); VRD(7, 0); SB(); PV1(2, 0); SB(); PV1(3, 0); KX(0); SB();
      PV1(4, 0); KX(1); SB(); PV1(5, 0); KX(2); SB(); PV1(6, 0); KX(3); SB(); PV1(7, 0); SB();
      if (__builtin_expect(left > NB, 0)) {
        VRD(0, VH_BYTES); SB(); VRD(1, VH_BYTES); SB(); VRD(2, VH_BYTES); SB(); VRD(3, VH_BYTES); SB(); VRD(4, VH_BYTES); SB();
        PV1(0, 4); SB(); VRD(5, VH_BYTES); SB(); PV1(1, 4); SB(); VRD(6, VH_BYTES); SB(); PV1(2, 4); SB(); VRD(7, VH_BYTES); SB();
        PV1(3, 4); PV1(4, 4); PV1(5, 4); PV1(6, 4); PV1(7, 4); SB();
        if (left == NB + 1) {
          *(LAS f32x4*)(lds + xi + 2048) = f32x4{nm, 0.f, lsum[0], 0.f};
#pragma unroll
          for (int i = 0; i < 2; ++i) {
            const f32x4 a = o[4 + 2 * i], b = o[5 + 2 * i];
            *(LAS u32x4*)(lds + xi + i * 1024) = u32x4{pk(a[0], a[1]), pk(a[2], a[3]), pk(b[0], b[1]), pk(b[2], b[3])};
          }
        }
      }
    } while (--left != 0);
    {
      asm volatile("" : "+v"(xi));
      const unsigned ln = (xi >> 4) & 63, xb = Q_OFF + head * XCH_BYTES;
      const unsigned xw = xb + (ln & 15) * 144 + (ln >> 4) * 8, xr = xb + (ln >> 3) * 144 + (ln & 7) * 16;
      const int oo = (ln >> 3) * 256 + half * 128 + (ln & 7) * 16;
      const f32x4 st = *(LAS f32x4*)(lds + (xi & ~0x300u) + 2048);
      const u32x4 yy[2] = {*(LAS u32x4*)(lds + xi), *(LAS u32x4*)(lds + xi + 1024)};
      SB();
      const float nn = fminf(nm, st[0]);
      const float b1 = __builtin_amdgcn_exp2f(nn - nm), b2 = __builtin_amdgcn_exp2f(nn - st[0]);
      const float inv = __builtin_amdgcn_rcpf(fmaf(b1, lsum[0], b2 * st[2]));
      const float w1 = b1 * inv, w2 = b2 * inv;
#pragma unroll
      for (int i = 0; i < 4; ++i) {
        const unsigned y0 = yy[i >> 1][2 * (i & 1)], y1 = yy[i >> 1][2 * (i & 1) + 1];
        const f32x4 y = {__uint_as_float(y0 << 16), __uint_as_float(y0 & 0xFFFF0000u), __uint_as_float(y1 << 16), __uint_as_float(y1 & 0xFFFF0000u)};
        const f32x4 r = o[i] * w1 + y * w2;
        *(LAS u32x2*)(lds + xw + i * 32) = u32x2{pk(r[0], r[1]), pk(r[2], r[3])};
      }
      asm volatile("" ::: "memory");
      const u32x4 r0 = *(LAS u32x4*)(lds + xr), r1 = *(LAS u32x4*)(lds + xr + 8 * 144);
      const __amdgpu_buffer_rsrc_t ro = __builtin_amdgcn_make_buffer_rsrc((char*)O + ((size_t)pair * 4 + head) * (SQ * 256), 0, SQ * 256, 0x00020000);
      __builtin_amdgcn_raw_buffer_store_b128(r0, ro, oo, 0, 18);
      __builtin_amdgcn_raw_buffer_store_b128(r1, ro, oo + 2048, 0, 18);
    }
    return;
  }

  const int vo0 = (lane >> 3) * 256 + half * 128 + (((lane & 7) ^ ((lane >> 3) & 6)) << 4);
  const int vo1 = vo0 + 1024, vo2 = vo0 ^ 128, vo3 = vo2 + 1024;
  const __amdgpu_buffer_rsrc_t rk = __builtin_amdgcn_make_buffer_rsrc((char*)K + (size_t)pair * (SK * 256), 0, SK * 256, 0x00020000);
  const __amdgpu_buffer_rsrc_t rq = __builtin_amdgcn_make_buffer_rsrc((char*)Q + (size_t)pair * KT_BYTES, 0, KT_BYTES, 0x00020000);
  const __amdgpu_buffer_rsrc_t rv = __builtin_amdgcn_make_buffer_rsrc((char*)V + (size_t)pair * (SK * 256), 0, SK * 256, 0x00020000);
  const unsigned lbase = (unsigned)(uintptr_t)lds;
#define KTILE(r, so, dst) \
    asm volatile("s_mov_b32 m0, %0\n\ts_nop 0\n\t" \
                 "buffer_load_dwordx4 %1, %3, %4 offen nt lds\n\t" \
                 "buffer_load_dwordx4 %2, %3, %4 offen offset:1024 nt lds\n\t" \
                 "buffer_load_dwordx4 %1, %3, %4 offen offset:2048 nt lds\n\t" \
                 "buffer_load_dwordx4 %2, %3, %4 offen offset:3072 nt lds" \
                 :: "s"(lbase + (dst) + wave * 4096), "v"(ko0), "v"(ko1), "s"(r), "s"((so) + wave * 4096) : "memory")
#define VHALF(a, b, so, dst) \
    asm volatile("s_mov_b32 m0, %0\n\ts_nop 0\n\t" \
                 "buffer_load_dwordx4 %1, %3, %4 offen nt lds\n\t" \
                 "buffer_load_dwordx4 %2, %3, %4 offen offset:1024 nt lds" \
                 :: "s"(lbase + (dst) + wave * 2048), "v"(a), "v"(b), "s"(rv), "s"((so) + wave * 4096) : "memory")
#define SLOT(s) (((s) & 3) * SLOT_BYTES)
#define TOFF(u) ((((((u) >> 2) & 1) | (((u) & 3) << 1) | (((u) >> 3) << 3)) ^ ((pair & 3) << 3)) * KT_BYTES)
#define OFF1(t) TOFF(2 * (t) + half)
#define OFF2(t) TOFF(NA + (t))
#define K1(t) KTILE(rk, OFF1(t), SLOT(t))
#define K2(t) KTILE(rk, OFF2(t), SLOT(t))
#define V1(t) do { VHALF(vo0, vo1, OFF1(t), SLOT((t) + 2) + KT_BYTES); VHALF(vo2, vo3, OFF1(t), SLOT((t) + 2) + KT_BYTES + VH_BYTES); } while (0)
#define V2(t) VHALF(vo0, vo1, OFF2(t), SLOT((t) + 2) + KT_BYTES)
#define PACE_WAIT(d) do { while ((int)((unsigned)__builtin_amdgcn_s_memrealtime() - due) < 0) __builtin_amdgcn_s_sleep(1); due += (d); } while (0)
  unsigned long long t64;
  asm volatile("s_memrealtime %0" : "=&s"(t64) :: "memory");
  KTILE(rk, kso - wave * 4096, SLOT(0)); KTILE(rq, 0, Q_OFF);
  asm volatile("s_waitcnt lgkmcnt(0)" : "+s"(t64) :: "memory");
  unsigned due = (unsigned)t64 + (PACE0 - 5 * (PACE / 2)) - (__builtin_amdgcn_s_getreg(20 | (3 << 11)) & 1) * ODD;
  PACE_WAIT(PACE / 2); K1(1);
  PACE_WAIT(PACE / 2); V1(0);
  wait_vm<8>(); LBARRIER();
  PACE_WAIT(PACE / 2); K1(2);
  PACE_WAIT(PACE / 2); V1(1);
  wait_vm<12>(); LBARRIER(); PACE_WAIT(PACE / 2); K1(3);
  wait_vm<12>(); PACE_WAIT(PACE / 2); V1(2); wait_vm<12>(); PACE_WAIT(PACE / 2); K1(4); LBARRIER();
#pragma unroll 1
  for (int s = 3; s < NA - 2; ++s) {
    wait_vm<12>(); PACE_WAIT(PACE / 2); V1(s);
    wait_vm<12>(); LBARRIER(); PACE_WAIT(PACE / 2); K1(s + 2);
  }
  wait_vm<12>(); PACE_WAIT(PACE / 2); V1(NA - 2); wait_vm<12>(); LBARRIER(); PACE_WAIT(PACE / 4); K2(NA);
  wait_vm<8>(); LBARRIER(); PACE_WAIT(0); V1(NA - 1); K2(NA + 1);
  wait_vm<12>(); V2(NA); wait_vm<10>(); K2(NA + 2); LBARRIER();
#pragma unroll 1
  for (int s = NA + 1; s < NI - 2; ++s) {
    wait_vm<10>(); V2(s);
    wait_vm<8>(); K2(s + 2); LBARRIER();
  }
  V2(NI - 2);
  wait_vm<8>(); LBARRIER();
  V2(NI - 1);
  wait_vm<4>(); LBARRIER();
  wait_vm<2>(); LBARRIER();
  wait_vm<0>(); LBARRIER();
}

}

extern "C" void kernel_launch(void* const* d_in, const int* in_sizes, int n_in, void* d_out, int out_size, void* d_ws, size_t ws_size,
                              hipStream_t stream) {
  gqa_attn<<<2 * NPAIR, NTHREADS, 0, stream>>>((const __hip_bfloat16*)d_in[0], (const __hip_bfloat16*)d_in[1], (const __hip_bfloat16*)d_in[2],
                                                (__hip_bfloat16*)d_out, (unsigned char*)d_ws);
}
```
